```python
import torch

def _softmax(x, axis=-1):
    return torch.softmax(x, dim=axis)
def _gelu(x):
    return torch.nn.functional.gelu(x, approximate="tanh")
def _silu(x):
    return torch.nn.functional.silu(x)
def _sigmoid(x):
    return torch.sigmoid(x)
def _layernorm(x, axis=-1, eps=1e-5):
    m = x.mean(dim=axis, keepdim=True)
    v = x.var(dim=axis, keepdim=True, unbiased=False)
    return (x - m) / torch.sqrt(v + eps)
def _scatter_add(tgt, idx, src, axis=0):
    return tgt.clone().index_add_(axis, idx, src)
def _segment_sum(src, seg, n):
    out = torch.zeros((n,) + src.shape[1:], dtype=src.dtype)
    return out.index_add_(0, seg, src)
def _pad(x, pw):
    flat = tuple(v for pair in reversed(pw) for v in pair)
    return torch.nn.functional.pad(x, flat)

kernel_name = 'concat_heads__gqa_block_cached__gqa_block_cached__near_ridge'

def setup_inputs():

    _w = (lambda a: (lambda t: t.to(torch.bfloat16) if t.is_floating_point() else t)(torch.as_tensor(a)))
    def _A_setup():
        B, Sq, D = 8, 256, 640
        Hq, Hkv, Sk, Dh = 32, 32, 1024, 128
        assert Hq % Hkv == 0, (Hq, Hkv)
        return {
            "x": _w((lambda *s: torch.randn(*s, dtype=torch.bfloat16) * 0.8)(B, Sq, D)),
            "k_cache": _w((lambda *s: torch.randn(*s, dtype=torch.bfloat16) * 0.8)(B, Hkv, Sk, Dh)),
            "v_cache": _w((lambda *s: torch.randn(*s, dtype=torch.bfloat16) * 0.05)(B, Hkv, Sk, Dh)),
            "wq": _w((lambda *s: torch.randn(*s, dtype=torch.bfloat16) * 0.8)(D, Hq * Dh) * (D ** -0.5)),
            "wo": _w((lambda *s: torch.randn(*s, dtype=torch.bfloat16) * 0.05)(Hq * Dh, D) * ((Hq * Dh) ** -0.5)),
        }
    _A_in = _A_setup()

    def _B_setup():
        B, Sq, D = 8, 256, 640
        Hq, Hkv, Sk, Dh = 32, 32, 1024, 128
        assert Hq % Hkv == 0, (Hq, Hkv)
        return {
            "x": _w((lambda *s: torch.randn(*s, dtype=torch.bfloat16) * 0.8)(B, Sq, D)),
            "k_cache": _w((lambda *s: torch.randn(*s, dtype=torch.bfloat16) * 0.8)(B, Hkv, Sk, Dh)),
            "v_cache": _w((lambda *s: torch.randn(*s, dtype=torch.bfloat16) * 0.05)(B, Hkv, Sk, Dh)),
            "wq": _w((lambda *s: torch.randn(*s, dtype=torch.bfloat16) * 0.8)(D, Hq * Dh) * (D ** -0.5)),
            "wo": _w((lambda *s: torch.randn(*s, dtype=torch.bfloat16) * 0.05)(Hq * Dh, D) * ((Hq * Dh) ** -0.5)),
        }
    _B_in = _B_setup()

    return {
        "A_x": _A_in["x"],
        "A_k_cache": _A_in["k_cache"],
        "A_v_cache": _A_in["v_cache"],
        "A_wq": _A_in["wq"],
        "A_wo": _A_in["wo"],
        "B_x": _B_in["x"],
        "B_k_cache": _B_in["k_cache"],
        "B_v_cache": _B_in["v_cache"],
        "B_wq": _B_in["wq"],
        "B_wo": _B_in["wo"],
    }

def reference(A_x, A_k_cache, A_v_cache, A_wq, A_wo, B_x, B_k_cache, B_v_cache, B_wq, B_wo):
    def _A_ref(x, k_cache, v_cache, wq, wo):
        B, Sq, D = x.shape
        Hq, Dh = 32, 128
        Hkv = k_cache.shape[1]
        rep = Hq // Hkv
        q = (lambda x, *d: x.permute(*d))((x @ wq).reshape(B, Sq, Hq, Dh), 0, 2, 1, 3)
        k = (lambda x, n, axis: torch.repeat_interleave(x, n, dim=axis))(k_cache, rep, axis=1)
        v = (lambda x, n, axis: torch.repeat_interleave(x, n, dim=axis))(v_cache, rep, axis=1)
        scores = torch.einsum("bhqd,bhkd->bhqk", q, k) * (Dh ** -0.5)
        p = _softmax(scores, axis=-1)
        o = torch.einsum("bhqk,bhkd->bhqd", p, v)
        return (lambda x, *d: x.permute(*d))(o, 0, 2, 1, 3).reshape(B, Sq, Hq * Dh) @ wo

    def _B_ref(x, k_cache, v_cache, wq, wo):
        B, Sq, D = x.shape
        Hq, Dh = 32, 128
        Hkv = k_cache.shape[1]
        rep = Hq // Hkv
        q = (lambda x, *d: x.permute(*d))((x @ wq).reshape(B, Sq, Hq, Dh), 0, 2, 1, 3)
        k = (lambda x, n, axis: torch.repeat_interleave(x, n, dim=axis))(k_cache, rep, axis=1)
        v = (lambda x, n, axis: torch.repeat_interleave(x, n, dim=axis))(v_cache, rep, axis=1)
        scores = torch.einsum("bhqd,bhkd->bhqk", q, k) * (Dh ** -0.5)
        p = _softmax(scores, axis=-1)
        o = torch.einsum("bhqk,bhkd->bhqd", p, v)
        return (lambda x, *d: x.permute(*d))(o, 0, 2, 1, 3).reshape(B, Sq, Hq * Dh) @ wo

    a = _A_ref(A_x, A_k_cache, A_v_cache, A_wq, A_wo)
    b = _B_ref(B_x, B_k_cache, B_v_cache, B_wq, B_wo)
    return torch.concatenate([a, b], axis=-1)
```

```cpp
#include <hip/hip_runtime.h>
constexpr unsigned WS_O = 0, WS_G = 33554432, WS_E = WS_G + 1048576;

__global__ __launch_bounds__(256, 1) void k1(const char* xA, const char* kA, const char* vA, const char* wqA, const char* woA,
                                             const char* xB, const char* kB, const char* vB, const char* wqB, const char* woB, char* ws, char* out) {
  extern __shared__ __attribute__((aligned(1024))) char lds[];
  const unsigned bid = blockIdx.x, j = bid & 7u, i = bid >> 3;
  const unsigned br = j >> 2, b = ((j >> 1) & 1u) * 4u + (i & 3u), hp = (j & 1u) * 8u + (i >> 2);
  const unsigned w = __builtin_amdgcn_readfirstlane(threadIdx.x >> 6);
  const char* x = (br ? xB : xA) + (size_t)b * (256 * 1280);
  const char* wq = (br ? wqB : wqA) + hp * 512u;
  const char* k = (br ? kB : kA) + (size_t)(b * 32u + 2u * hp) * 262144u;
  const char* v = (br ? vB : vA) + (size_t)(b * 32u + 2u * hp) * 262144u;
  const unsigned oo = WS_O + (br * 8u + b) * 2097152u + hp * 8192u;
  char* of = ws + oo;
  const unsigned qs = WS_G + ((br * 8u + b) * 4u + w) * 8192u - oo;
  const unsigned sl = hp >> 2, nq = hp & 3u;
  const char* wf = (br ? woB : woA) + sl * 1310720u + nq * 320u;
  const unsigned ex = WS_E + (((br * 8u + b) * 4u + nq) * 4u + w) * 81920u - oo;
  char* y = out + (size_t)(b * 256u + w * 64u + sl * 16u) * 2560u + br * 1280u + nq * 320u;
  const char* wo = (bid >> 7 ? woB : woA) + ((bid & 127u) * 4u + w) * 10240u;
  const unsigned ldsb = (unsigned)(size_t)lds;
  asm volatile(
    "s_mov_b32 s75, 0\n"
    "s_mov_b64 s[66:67], 1\n"
    "TOP%=:\n"
    "s_mov_b64 s[44:45], %0\n"
    "s_and_b32 s45, s45, 0xffff\n"
    "s_mov_b32 s46, 327680\n"
    "s_mov_b32 s47, 0x20000\n"
    "s_mov_b64 s[48:49], %1\n"
    "s_sub_u32 s48, s48, 1024\n"
    "s_subb_u32 s49, s49, 0\n"
    "s_and_b32 s49, s49, 0xffff\n"
    "s_mov_b32 s50, 5236224\n"
    "s_mov_b32 s51, 0x20000\n"
    "s_mov_b32 s68, %7\n"
    "s_mov_b32 s69, %8\n"
    "v_mbcnt_lo_u32_b32 v0, -1, 0\n"
    "v_mbcnt_hi_u32_b32 v0, -1, v0\n"
    "v_lshlrev_b32 v229, 4, v0\n"
    "v_bfe_u32 v1, v0, 2, 3\n"
    "v_lshrrev_b32 v2, 3, v0\n"
    "v_and_b32 v2, 2, v2\n"
    "v_and_b32 v3, 3, v0\n"
    "v_xor_b32 v5, v2, v3\n"
    "v_lshlrev_b32 v2, 4, v5\n"
    "v_lshrrev_b32 v6, 5, v0\n"
    "v_lshl_add_u32 v2, v6, 6, v2\n"
    "v_xor_b32 v3, 48, v2\n"
    "v_mul_u32_u24 v4, 8192, v1\n"
    "v_add_u32 v240, v4, v2\n"
    "v_add_u32 v240, 1024, v240\n"
    "v_add_u32 v241, v4, v2\n"
    "v_add_u32 v241, 128, v241\n"
    "v_add_u32 v242, v4, v3\n"
    "v_add_u32 v242, 64512, v242\n"
    "v_add_u32 v243, v4, v3\n"
    "v_add_u32 v243, 63616, v243\n"
    "v_mul_u32_u24 v4, 256, v1\n"
    "v_add_u32 v224, v4, v2\n"
    "v_add_u32 v224, 1024, v224\n"
    "v_add_u32 v225, v4, v2\n"
    "v_add_u32 v225, 128, v225\n"
    "v_add_u32 v226, v4, v3\n"
    "v_add_u32 v226, 1024, v226\n"
    "v_add_u32 v227, v4, v3\n"
    "v_add_u32 v227, 128, v227\n"
    "v_mul_u32_u24 v4, 1280, v1\n"
    "v_add_u32 v244, v4, v2\n"
    "v_add_u32 v245, v4, v3\n"
    "s_lshl_b32 s71, s68, 12\n"
    "s_add_u32 s72, s69, s71\n"
    "s_add_u32 s73, s72, 65536\n"
    "s_add_u32 s81, s73, s71\n"
    "s_lshl_b32 s98, s68, 19\n"
    "s_lshl_b32 s80, s68, 17\n"
    "s_mul_i32 s79, s68, 81920\n"
    "s_lshl_b32 s77, %9, 2\n"
    "s_add_u32 s77, s77, %10\n"
    "s_and_b32 s77, s77, 15\n"
    "s_lshl_b32 s77, s77, 2\n"
    "s_add_u32 s77, s77, s68\n"
    "s_lshl_b32 s77, s77, 10\n"
    "s_getpc_b64 s[36:37]\n"
    "s_and_b32 s36, s36, 0xffffff80\n"
    "s_and_b32 s37, s37, 0xffff\n"
    "s_mov_b32 s38, SAFE%=-CPSAFE%=\n"
    "CPSAFE%=:\n"
    "s_mov_b32 s39, 0x20000\n"
    "buffer_load_dwordx4 a[252:255], v229, s[36:39], s77 offen\n"
    "s_add_u32 m0, s72, 0\n"
    "s_add_u32 s76, s80, 0\n"
    "buffer_load_dwordx4 v240, s[48:51], s76 offen offset:0 lds\n"
    "buffer_load_dwordx4 v241, s[48:51], s76 offen offset:1024 lds\n"
    "buffer_load_dwordx4 v242, s[48:51], s76 offen offset:2048 lds\n"
    "buffer_load_dwordx4 v243, s[48:51], s76 offen offset:3072 lds\n"
    "s_add_u32 m0, s72, 16384\n"
    "s_add_u32 s76, s80, 256\n"
    "buffer_load_dwordx4 v240, s[48:51], s76 offen offset:0 lds\n"
    "buffer_load_dwordx4 v241, s[48:51], s76 offen offset:1024 lds\n"
    "buffer_load_dwordx4 v242, s[48:51], s76 offen offset:2048 lds\n"
    "buffer_load_dwordx4 v243, s[48:51], s76 offen offset:3072 lds\n"
    "s_add_u32 m0, s81, 0\n"
    "s_add_u32 s76, s79, 0\n"
    "buffer_load_dwordx4 v244, s[44:47], s76 offen lds\n"
    "s_add_u32 m0, s81, 1024\n"
    "s_add_u32 s76, s79, 10240\n"
    "buffer_load_dwordx4 v245, s[44:47], s76 offen lds\n"
    "s_add_u32 m0, s81, 2048\n"
    "s_add_u32 s76, s79, 20480\n"
    "buffer_load_dwordx4 v244, s[44:47], s76 offen lds\n"
    "s_add_u32 m0, s81, 3072\n"
    "s_add_u32 s76, s79, 30720\n"
    "buffer_load_dwordx4 v245, s[44:47], s76 offen lds\n"
    "s_add_u32 m0, s81, 4096\n"
    "s_add_u32 s76, s79, 40960\n"
    "buffer_load_dwordx4 v244, s[44:47], s76 offen lds\n"
    "s_add_u32 m0, s81, 5120\n"
    "s_add_u32 s76, s79, 51200\n"
    "buffer_load_dwordx4 v245, s[44:47], s76 offen lds\n"
    "s_add_u32 m0, s81, 6144\n"
    "s_add_u32 s76, s79, 61440\n"
    "buffer_load_dwordx4 v244, s[44:47], s76 offen lds\n"
    "s_add_u32 m0, s81, 7168\n"
    "s_add_u32 s76, s79, 71680\n"
    "buffer_load_dwordx4 v245, s[44:47], s76 offen lds\n"
    "s_add_u32 s80, s80, 524288\n"
    "s_add_u32 s79, s79, 128\n"
    "s_add_u32 m0, s72, 32768\n"
    "s_add_u32 s76, s80, 0\n"
    "buffer_load_dwordx4 v240, s[48:51], s76 offen offset:0 lds\n"
    "buffer_load_dwordx4 v241, s[48:51], s76 offen offset:1024 lds\n"
    "buffer_load_dwordx4 v242, s[48:51], s76 offen offset:2048 lds\n"
    "buffer_load_dwordx4 v243, s[48:51], s76 offen offset:3072 lds\n"
    "s_add_u32 m0, s72, 49152\n"
    "s_add_u32 s76, s80, 256\n"
    "buffer_load_dwordx4 v240, s[48:51], s76 offen offset:0 lds\n"
    "buffer_load_dwordx4 v241, s[48:51], s76 offen offset:1024 lds\n"
    "buffer_load_dwordx4 v242, s[48:51], s76 offen offset:2048 lds\n"
    "buffer_load_dwordx4 v243, s[48:51], s76 offen offset:3072 lds\n"
    "s_add_u32 m0, s81, 32768\n"
    "s_add_u32 s76, s79, 0\n"
    "buffer_load_dwordx4 v244, s[44:47], s76 offen lds\n"
    "s_add_u32 m0, s81, 33792\n"
    "s_add_u32 s76, s79, 10240\n"
    "buffer_load_dwordx4 v245, s[44:47], s76 offen lds\n"
    "s_add_u32 m0, s81, 34816\n"
    "s_add_u32 s76, s79, 20480\n"
    "buffer_load_dwordx4 v244, s[44:47], s76 offen lds\n"
    "s_add_u32 m0, s81, 35840\n"
    "s_add_u32 s76, s79, 30720\n"
    "buffer_load_dwordx4 v245, s[44:47], s76 offen lds\n"
    "s_add_u32 m0, s81, 36864\n"
    "s_add_u32 s76, s79, 40960\n"
    "buffer_load_dwordx4 v244, s[44:47], s76 offen lds\n"
    "s_add_u32 m0, s81, 37888\n"
    "s_add_u32 s76, s79, 51200\n"
    "buffer_load_dwordx4 v245, s[44:47], s76 offen lds\n"
    "s_add_u32 m0, s81, 38912\n"
    "s_add_u32 s76, s79, 61440\n"
    "buffer_load_dwordx4 v244, s[44:47], s76 offen lds\n"
    "s_add_u32 m0, s81, 39936\n"
    "s_add_u32 s76, s79, 71680\n"
    "buffer_load_dwordx4 v245, s[44:47], s76 offen lds\n"
    "s_add_u32 s80, s80, 524288\n"
    "s_add_u32 s79, s79, 128\n"
    "s_mov_b64 s[36:37], %2\n"
    "s_sub_u32 s36, s36, 1024\n"
    "s_subb_u32 s37, s37, 0\n"
    "s_and_b32 s37, s37, 0xffff\n"
    "s_mov_b32 s38, 525312\n"
    "s_mov_b32 s39, 0x20000\n"
    "s_mov_b64 s[40:41], %3\n"
    "s_sub_u32 s40, s40, 1024\n"
    "s_subb_u32 s41, s41, 0\n"
    "s_and_b32 s41, s41, 0xffff\n"
    "s_mov_b32 s42, 525312\n"
    "s_mov_b32 s43, 0x20000\n"
    "s_mov_b64 s[52:53], %4\n"
    "s_and_b32 s53, s53, 0xffff\n"
    "s_mov_b32 s54, 2097152\n"
    "s_mov_b32 s55, 0x20000\n"
    "s_mov_b64 s[56:57], %4\n"
    "s_add_u32 s56, s56, %5\n"
    "s_addc_u32 s57, s57, 0\n"
    "s_and_b32 s57, s57, 0xffff\n"
    "s_mov_b32 s58, 8192\n"
    "s_mov_b32 s59, 0x20000\n"
    "s_mov_b64 s[60:61], %6\n"
    "s_and_b32 s61, s61, 0xffff\n"
    "s_mov_b32 s62, 10240\n"
    "s_mov_b32 s63, 0x20000\n"
    "v_lshrrev_b32 v1, 4, v0\n"
    "v_lshrrev_b32 v2, 1, v0\n"
    "v_and_b32 v2, 2, v2\n"
    "v_bfe_u32 v3, v0, 3, 1\n"
    "v_mul_u32_u24 v4, 3, v3\n"
    "v_xor_b32 v2, v2, v4\n"
    "v_xor_b32 v2, v2, v1\n"
    "v_and_b32 v4, 7, v0\n"
    "v_lshlrev_b32 v4, 6, v4\n"
    "v_lshl_add_u32 v2, v2, 4, v4\n"
    "v_lshl_add_u32 v4, v3, 11, v2\n"
    "v_add_u32 v228, s69, v4\n"
    "v_lshl_add_u32 v4, v3, 10, v2\n"
    "v_add_u32 v238, s81, v4\n"
    "v_lshrrev_b32 v2, 3, v0\n"
    "v_and_b32 v2, 2, v2\n"
    "v_mul_u32_u24 v3, 3, v6\n"
    "v_xor_b32 v2, v2, v3\n"
    "v_bfe_u32 v3, v0, 1, 1\n"
    "v_xor_b32 v2, v2, v3\n"
    "v_lshlrev_b32 v2, 4, v2\n"
    "v_and_b32 v3, 1, v0\n"
    "v_lshl_add_u32 v2, v3, 3, v2\n"
    "v_bfe_u32 v3, v0, 2, 3\n"
    "v_lshl_add_u32 v2, v3, 6, v2\n"
    "s_add_u32 s77, s69, 65536\n"
    "v_lshl_add_u32 v3, v6, 11, v2\n"
    "v_xor_b32 v4, 32, v3\n"
    "v_add_u32 v230, s77, v3\n"
    "v_add_u32 v231, s77, v4\n"
    "v_and_b32 v2, 1, v1\n"
    "v_mul_u32_u24 v2, 3, v2\n"
    "v_and_b32 v3, 3, v0\n"
    "v_xor_b32 v2, v2, v3\n"
    "v_lshlrev_b32 v2, 4, v2\n"
    "v_bfe_u32 v3, v0, 2, 2\n"
    "v_lshl_add_u32 v2, v3, 6, v2\n"
    "v_lshl_add_u32 v2, v1, 11, v2\n"
    "v_xor_b32 v3, 288, v2\n"
    "v_add_u32 v236, s69, v2\n"
    "v_add_u32 v237, s69, v3\n"
    "v_and_b32 v2, 3, v0\n"
    "v_cmp_eq_u32 vcc, 0, v2\n"
    "v_mov_b32 v3, 0x3f803f80\n"
    "s_nop 0\n"
    "v_cndmask_b32 v232, 0, v3, vcc\n"
    "v_mov_b32 v233, v232\n"
    "v_mov_b32 v234, v232\n"
    "v_mov_b32 v235, v232\n"
    "s_mov_b32 s84, 0x3e0293ee\n"
    "s_mov_b32 s85, 0x3e0293ee\n"
    "s_lshl_b32 s82, s68, 19\n"
    "s_mov_b64 s[96:97], 0\n"
    "v_lshrrev_b32 v2, 3, v0\n"
    "v_mul_u32_u24 v2, 1280, v2\n"
    "v_and_b32 v3, 7, v0\n"
    "v_lshl_add_u32 v248, v3, 4, v2\n"
    "v_and_b32 v3, 31, v0\n"
    "v_lshlrev_b32 v3, 4, v3\n"
    "v_lshl_add_u32 v249, v6, 13, v3\n"
    "s_lshl_b32 s86, %9, 2\n"
    "s_add_u32 s86, s86, s68\n"
    "s_mul_i32 s86, s86, 10240\n"
    "s_add_u32 s86, s86, 256\n"
    "s_lshl_b32 s87, %10, 2\n"
    "s_add_u32 s87, s87, s68\n"
    "s_lshl_b32 s87, s87, 15\n"
    "s_add_u32 s87, s87, 1048576\n"
    "buffer_load_dwordx4 a[252:255], v248, s[44:47], s86 offen\n"
    "buffer_load_dwordx4 a[252:255], v249, s[48:51], s87 offen offset:1024\n"
    "s_add_u32 s87, s87, 16384\n"
    "s_add_u32 s86, s86, 128\n"
    "buffer_load_dwordx4 a[252:255], v249, s[48:51], s87 offen offset:1024\n"
    "s_add_u32 s87, s87, 507904\n"
    "buffer_load_dwordx4 a[252:255], v248, s[44:47], s86 offen\n"
    "buffer_load_dwordx4 a[252:255], v249, s[48:51], s87 offen offset:1024\n"
    "s_add_u32 s87, s87, 16384\n"
    "s_add_u32 s86, s86, 128\n"
    "buffer_load_dwordx4 a[252:255], v249, s[48:51], s87 offen offset:1024\n"
    "s_add_u32 s87, s87, 507904\n"
    "v_and_b32 v3, 3, v0\n"
    "v_lshlrev_b32 v4, 11, v3\n"
    "v_lshlrev_b32 v3, 9, v3\n"
    "s_lshl_b32 s77, %14, 11\n"
    "s_and_b32 s78, %9, 3\n"
    "s_lshl_b32 s78, s78, 9\n"
    "s_add_u32 s78, s78, 384\n"
    "v_add_u32 v3, s77, v3\n"
    "v_add_u32 v4, s78, v4\n"
    "v_and_b32 v5, 4, v0\n"
    "v_cmp_eq_u32 vcc, 0, v5\n"
    "s_nop 1\n"
    "v_cndmask_b32 v3, v4, v3, vcc\n"
    "v_and_b32 v4, 8, v0\n"
    "v_lshl_add_u32 v3, v4, 3, v3\n"
    "buffer_load_dword v252, v3, s[56:59], 0 offen sc0 sc1\n"
    "s_waitcnt vmcnt(23)\n"
    "s_barrier\n"
    "ds_read_b128 v[192:195], v238 offset:0\n"
    "ds_read_b128 v[196:199], v238 offset:2048\n"
    "ds_read_b128 v[200:203], v238 offset:4096\n"
    "ds_read_b128 v[204:207], v238 offset:6144\n"
    "ds_read_b64_tr_b16 v[128:129], v236 offset:0\n"
    "ds_read_b64_tr_b16 v[130:131], v237 offset:0\n"
    "ds_read_b64_tr_b16 v[132:133], v236 offset:8\n"
    "ds_read_b64_tr_b16 v[134:135], v237 offset:8\n"
    "ds_read_b64_tr_b16 v[136:137], v236 offset:512\n"
    "ds_read_b64_tr_b16 v[138:139], v237 offset:512\n"
    "ds_read_b64_tr_b16 v[140:141], v236 offset:520\n"
    "ds_read_b64_tr_b16 v[142:143], v237 offset:520\n"
    "ds_read_b64_tr_b16 v[144:145], v236 offset:1024\n"
    "ds_read_b64_tr_b16 v[146:147], v237 offset:1024\n"
    "ds_read_b64_tr_b16 v[148:149], v236 offset:1032\n"
    "ds_read_b64_tr_b16 v[150:151], v237 offset:1032\n"
    "ds_read_b64_tr_b16 v[152:153], v236 offset:1536\n"
    "ds_read_b64_tr_b16 v[154:155], v237 offset:1536\n"
    "ds_read_b64_tr_b16 v[156:157], v236 offset:1544\n"
    "ds_read_b64_tr_b16 v[158:159], v237 offset:1544\n"
    "s_waitcnt lgkmcnt(0)\n"
    "v_mfma_f32_16x16x32_bf16 v[0:3], v[128:131], v[192:195], 0\n"
    "v_mfma_f32_16x16x32_bf16 v[32:35], v[128:131], v[196:199], 0\n"
    "ds_read_b64_tr_b16 v[160:161], v236 offset:16384\n"
    "ds_read_b64_tr_b16 v[162:163], v237 offset:16384\n"
    "v_mfma_f32_16x16x32_bf16 v[64:67], v[128:131], v[200:203], 0\n"
    "v_mfma_f32_16x16x32_bf16 v[96:99], v[128:131], v[204:207], 0\n"
    "ds_read_b64_tr_b16 v[164:165], v236 offset:16392\n"
    "ds_read_b64_tr_b16 v[166:167], v237 offset:16392\n"
    "v_mfma_f32_16x16x32_bf16 v[4:7], v[132:135], v[192:195], 0\n"
    "v_mfma_f32_16x16x32_bf16 v[36:39], v[132:135], v[196:199], 0\n"
    "ds_read_b64_tr_b16 v[168:169], v236 offset:16896\n"
    "ds_read_b64_tr_b16 v[170:171], v237 offset:16896\n"
    "v_mfma_f32_16x16x32_bf16 v[68:71], v[132:135], v[200:203], 0\n"
    "v_mfma_f32_16x16x32_bf16 v[100:103], v[132:135], v[204:207], 0\n"
    "ds_read_b64_tr_b16 v[172:173], v236 offset:16904\n"
    "ds_read_b64_tr_b16 v[174:175], v237 offset:16904\n"
    "v_mfma_f32_16x16x32_bf16 v[8:11], v[136:139], v[192:195], 0\n"
    "v_mfma_f32_16x16x32_bf16 v[40:43], v[136:139], v[196:199], 0\n"
    "ds_read_b64_tr_b16 v[176:177], v236 offset:17408\n"
    "ds_read_b64_tr_b16 v[178:179], v237 offset:17408\n"
    "v_mfma_f32_16x16x32_bf16 v[72:75], v[136:139], v[200:203], 0\n"
    "v_mfma_f32_16x16x32_bf16 v[104:107], v[136:139], v[204:207], 0\n"
    "ds_read_b64_tr_b16 v[180:181], v236 offset:17416\n"
    "ds_read_b64_tr_b16 v[182:183], v237 offset:17416\n"
    "v_mfma_f32_16x16x32_bf16 v[12:15], v[140:143], v[192:195], 0\n"
    "v_mfma_f32_16x16x32_bf16 v[44:47], v[140:143], v[196:199], 0\n"
    "ds_read_b64_tr_b16 v[184:185], v236 offset:17920\n"
    "ds_read_b64_tr_b16 v[186:187], v237 offset:17920\n"
    "v_mfma_f32_16x16x32_bf16 v[76:79], v[140:143], v[200:203], 0\n"
    "v_mfma_f32_16x16x32_bf16 v[108:111], v[140:143], v[204:207], 0\n"
    "ds_read_b64_tr_b16 v[188:189], v236 offset:17928\n"
    "ds_read_b64_tr_b16 v[190:191], v237 offset:17928\n"
    "v_mfma_f32_16x16x32_bf16 v[16:19], v[144:147], v[192:195], 0\n"
    "v_mfma_f32_16x16x32_bf16 v[48:51], v[144:147], v[196:199], 0\n"
    "v_mfma_f32_16x16x32_bf16 v[80:83], v[144:147], v[200:203], 0\n"
    "v_mfma_f32_16x16x32_bf16 v[112:115], v[144:147], v[204:207], 0\n"
    "v_mfma_f32_16x16x32_bf16 v[20:23], v[148:151], v[192:195], 0\n"
    "v_mfma_f32_16x16x32_bf16 v[52:55], v[148:151], v[196:199], 0\n"
    "v_mfma_f32_16x16x32_bf16 v[84:87], v[148:151], v[200:203], 0\n"
    "v_mfma_f32_16x16x32_bf16 v[116:119], v[148:151], v[204:207], 0\n"
    "v_mfma_f32_16x16x32_bf16 v[24:27], v[152:155], v[192:195], 0\n"
    "v_mfma_f32_16x16x32_bf16 v[56:59], v[152:155], v[196:199], 0\n"
    "v_mfma_f32_16x16x32_bf16 v[88:91], v[152:155], v[200:203], 0\n"
    "v_mfma_f32_16x16x32_bf16 v[120:123], v[152:155], v[204:207], 0\n"
    "v_mfma_f32_16x16x32_bf16 v[28:31], v[156:159], v[192:195], 0\n"
    "v_mfma_f32_16x16x32_bf16 v[60:63], v[156:159], v[196:199], 0\n"
    "v_mfma_f32_16x16x32_bf16 v[92:95], v[156:159], v[200:203], 0\n"
    "v_mfma_f32_16x16x32_bf16 v[124:127], v[156:159], v[204:207], 0\n"
    "s_waitcnt lgkmcnt(0)\n"
    "v_mfma_f32_16x16x32_bf16 a[0:3], v[160:163], v[192:195], 0\n"
    "v_mfma_f32_16x16x32_bf16 a[32:35], v[160:163], v[196:199], 0\n"
    "ds_read_b128 v[208:211], v238 offset:512\n"
    "ds_read_b128 v[212:215], v238 offset:2560\n"
    "v_mfma_f32_16x16x32_bf16 a[64:67], v[160:163], v[200:203], 0\n"
    "v_mfma_f32_16x16x32_bf16 a[96:99], v[160:163], v[204:207], 0\n"
    "ds_read_b128 v[216:219], v238 offset:4608\n"
    "ds_read_b128 v[220:223], v238 offset:6656\n"
    "v_mfma_f32_16x16x32_bf16 a[4:7], v[164:167], v[192:195], 0\n"
    "v_mfma_f32_16x16x32_bf16 a[36:39], v[164:167], v[196:199], 0\n"
    "ds_read_b64_tr_b16 v[128:129], v236 offset:8192\n"
    "ds_read_b64_tr_b16 v[130:131], v237 offset:8192\n"
    "v_mfma_f32_16x16x32_bf16 a[68:71], v[164:167], v[200:203], 0\n"
    "v_mfma_f32_16x16x32_bf16 a[100:103], v[164:167], v[204:207], 0\n"
    "ds_read_b64_tr_b16 v[132:133], v236 offset:8200\n"
    "ds_read_b64_tr_b16 v[134:135], v237 offset:8200\n"
    "v_mfma_f32_16x16x32_bf16 a[8:11], v[168:171], v[192:195], 0\n"
    "v_mfma_f32_16x16x32_bf16 a[40:43], v[168:171], v[196:199], 0\n"
    "ds_read_b64_tr_b16 v[136:137], v236 offset:8704\n"
    "ds_read_b64_tr_b16 v[138:139], v237 offset:8704\n"
    "v_mfma_f32_16x16x32_bf16 a[72:75], v[168:171], v[200:203], 0\n"
    "buffer_load_dwordx4 a[252:255], v248, s[44:47], s86 offen\n"
    "v_mfma_f32_16x16x32_bf16 a[104:107], v[168:171], v[204:207], 0\n"
    "ds_read_b64_tr_b16 v[140:141], v236 offset:8712\n"
    "ds_read_b64_tr_b16 v[142:143], v237 offset:8712\n"
    "v_mfma_f32_16x16x32_bf16 a[12:15], v[172:175], v[192:195], 0\n"
    "v_mfma_f32_16x16x32_bf16 a[44:47], v[172:175], v[196:199], 0\n"
    "ds_read_b64_tr_b16 v[144:145], v236 offset:9216\n"
    "ds_read_b64_tr_b16 v[146:147], v237 offset:9216\n"
    "v_mfma_f32_16x16x32_bf16 a[76:79], v[172:175], v[200:203], 0\n"
    "v_mfma_f32_16x16x32_bf16 a[108:111], v[172:175], v[204:207], 0\n"
    "ds_read_b64_tr_b16 v[148:149], v236 offset:9224\n"
    "ds_read_b64_tr_b16 v[150:151], v237 offset:9224\n"
    "v_mfma_f32_16x16x32_bf16 a[16:19], v[176:179], v[192:195], 0\n"
    "v_mfma_f32_16x16x32_bf16 a[48:51], v[176:179], v[196:199], 0\n"
    "ds_read_b64_tr_b16 v[152:153], v236 offset:9728\n"
    "ds_read_b64_tr_b16 v[154:155], v237 offset:9728\n"
    "v_mfma_f32_16x16x32_bf16 a[80:83], v[176:179], v[200:203], 0\n"
    "v_mfma_f32_16x16x32_bf16 a[112:115], v[176:179], v[204:207], 0\n"
    "ds_read_b64_tr_b16 v[156:157], v236 offset:9736\n"
    "ds_read_b64_tr_b16 v[158:159], v237 offset:9736\n"
    "v_mfma_f32_16x16x32_bf16 a[20:23], v[180:183], v[192:195], 0\n"
    "v_mfma_f32_16x16x32_bf16 a[52:55], v[180:183], v[196:199], 0\n"
    "v_mfma_f32_16x16x32_bf16 a[84:87], v[180:183], v[200:203], 0\n"
    "v_mfma_f32_16x16x32_bf16 a[116:119], v[180:183], v[204:207], 0\n"
    "buffer_load_dwordx4 a[252:255], v249, s[48:51], s87 offen offset:1024\n"
    "v_mfma_f32_16x16x32_bf16 a[24:27], v[184:187], v[192:195], 0\n"
    "v_mfma_f32_16x16x32_bf16 a[56:59], v[184:187], v[196:199], 0\n"
    "v_mfma_f32_16x16x32_bf16 a[88:91], v[184:187], v[200:203], 0\n"
    "v_mfma_f32_16x16x32_bf16 a[120:123], v[184:187], v[204:207], 0\n"
    "v_mfma_f32_16x16x32_bf16 a[28:31], v[188:191], v[192:195], 0\n"
    "v_mfma_f32_16x16x32_bf16 a[60:63], v[188:191], v[196:199], 0\n"
    "v_mfma_f32_16x16x32_bf16 a[92:95], v[188:191], v[200:203], 0\n"
    "v_mfma_f32_16x16x32_bf16 a[124:127], v[188:191], v[204:207], 0\n"
    "s_waitcnt lgkmcnt(0)\n"
    "v_mfma_f32_16x16x32_bf16 v[0:3], v[128:131], v[208:211], v[0:3]\n"
    "s_add_u32 s87, s87, 16384\n"
    "s_add_u32 s86, s86, 128\n"
    "v_mfma_f32_16x16x32_bf16 v[32:35], v[128:131], v[212:215], v[32:35]\n"
    "ds_read_b64_tr_b16 v[160:161], v236 offset:24576\n"
    "ds_read_b64_tr_b16 v[162:163], v237 offset:24576\n"
    "v_mfma_f32_16x16x32_bf16 v[64:67], v[128:131], v[216:219], v[64:67]\n"
    "v_mfma_f32_16x16x32_bf16 v[96:99], v[128:131], v[220:223], v[96:99]\n"
    "ds_read_b64_tr_b16 v[164:165], v236 offset:24584\n"
    "ds_read_b64_tr_b16 v[166:167], v237 offset:24584\n"
    "v_mfma_f32_16x16x32_bf16 v[4:7], v[132:135], v[208:211], v[4:7]\n"
    "v_mfma_f32_16x16x32_bf16 v[36:39], v[132:135], v[212:215], v[36:39]\n"
    "ds_read_b64_tr_b16 v[168:169], v236 offset:25088\n"
    "ds_read_b64_tr_b16 v[170:171], v237 offset:25088\n"
    "v_mfma_f32_16x16x32_bf16 v[68:71], v[132:135], v[216:219], v[68:71]\n"
    "v_mfma_f32_16x16x32_bf16 v[100:103], v[132:135], v[220:223], v[100:103]\n"
    "ds_read_b64_tr_b16 v[172:173], v236 offset:25096\n"
    "ds_read_b64_tr_b16 v[174:175], v237 offset:25096\n"
    "v_mfma_f32_16x16x32_bf16 v[8:11], v[136:139], v[208:211], v[8:11]\n"
    "v_mfma_f32_16x16x32_bf16 v[40:43], v[136:139], v[212:215], v[40:43]\n"
    "ds_read_b64_tr_b16 v[176:177], v236 offset:25600\n"
    "ds_read_b64_tr_b16 v[178:179], v237 offset:25600\n"
    "v_mfma_f32_16x16x32_bf16 v[72:75], v[136:139], v[216:219], v[72:75]\n"
    "v_mfma_f32_16x16x32_bf16 v[104:107], v[136:139], v[220:223], v[104:107]\n"
    "ds_read_b64_tr_b16 v[180:181], v236 offset:25608\n"
    "ds_read_b64_tr_b16 v[182:183], v237 offset:25608\n"
    "v_mfma_f32_16x16x32_bf16 v[12:15], v[140:143], v[208:211], v[12:15]\n"
    "v_mfma_f32_16x16x32_bf16 v[44:47], v[140:143], v[212:215], v[44:47]\n"
    "ds_read_b64_tr_b16 v[184:185], v236 offset:26112\n"
    "ds_read_b64_tr_b16 v[186:187], v237 offset:26112\n"
    "v_mfma_f32_16x16x32_bf16 v[76:79], v[140:143], v[216:219], v[76:79]\n"
    "v_mfma_f32_16x16x32_bf16 v[108:111], v[140:143], v[220:223], v[108:111]\n"
    "ds_read_b64_tr_b16 v[188:189], v236 offset:26120\n"
    "ds_read_b64_tr_b16 v[190:191], v237 offset:26120\n"
    "v_mfma_f32_16x16x32_bf16 v[16:19], v[144:147], v[208:211], v[16:19]\n"
    "v_mfma_f32_16x16x32_bf16 v[48:51], v[144:147], v[212:215], v[48:51]\n"
    "buffer_load_dwordx4 a[252:255], v249, s[48:51], s87 offen offset:1024\n"
    "v_mfma_f32_16x16x32_bf16 v[80:83], v[144:147], v[216:219], v[80:83]\n"
    "v_mfma_f32_16x16x32_bf16 v[112:115], v[144:147], v[220:223], v[112:115]\n"
    "v_mfma_f32_16x16x32_bf16 v[20:23], v[148:151], v[208:211], v[20:23]\n"
    "v_mfma_f32_16x16x32_bf16 v[52:55], v[148:151], v[212:215], v[52:55]\n"
    "v_mfma_f32_16x16x32_bf16 v[84:87], v[148:151], v[216:219], v[84:87]\n"
    "v_mfma_f32_16x16x32_bf16 v[116:119], v[148:151], v[220:223], v[116:119]\n"
    "v_mfma_f32_16x16x32_bf16 v[24:27], v[152:155], v[208:211], v[24:27]\n"
    "v_mfma_f32_16x16x32_bf16 v[56:59], v[152:155], v[212:215], v[56:59]\n"
    "s_add_u32 s87, s87, 507904\n"
    "v_mfma_f32_16x16x32_bf16 v[88:91], v[152:155], v[216:219], v[88:91]\n"
    "v_mfma_f32_16x16x32_bf16 v[120:123], v[152:155], v[220:223], v[120:123]\n"
    "v_mfma_f32_16x16x32_bf16 v[28:31], v[156:159], v[208:211], v[28:31]\n"
    "v_mfma_f32_16x16x32_bf16 v[60:63], v[156:159], v[212:215], v[60:63]\n"
    "v_mfma_f32_16x16x32_bf16 v[92:95], v[156:159], v[216:219], v[92:95]\n"
    "v_mfma_f32_16x16x32_bf16 v[124:127], v[156:159], v[220:223], v[124:127]\n"
    "s_waitcnt lgkmcnt(0)\n"
    "v_mfma_f32_16x16x32_bf16 a[0:3], v[160:163], v[208:211], a[0:3]\n"
    "s_waitcnt vmcnt(10)\n"
    "s_barrier\n"
    "v_mfma_f32_16x16x32_bf16 a[32:35], v[160:163], v[212:215], a[32:35]\n"
    "ds_read_b128 v[192:195], v238 offset:32768\n"
    "ds_read_b128 v[196:199], v238 offset:34816\n"
    "v_mfma_f32_16x16x32_bf16 a[64:67], v[160:163], v[216:219], a[64:67]\n"
    "s_add_u32 m0, s72, 0\n"
    "s_add_u32 s76, s80, 0\n"
    "v_mfma_f32_16x16x32_bf16 a[96:99], v[160:163], v[220:223], a[96:99]\n"
    "ds_read_b128 v[200:203], v238 offset:36864\n"
    "ds_read_b128 v[204:207], v238 offset:38912\n"
    "v_mfma_f32_16x16x32_bf16 a[4:7], v[164:167], v[208:211], a[4:7]\n"
    "buffer_load_dwordx4 v240, s[48:51], s76 offen offset:0 lds\n"
    "v_mfma_f32_16x16x32_bf16 a[36:39], v[164:167], v[212:215], a[36:39]\n"
    "ds_read_b64_tr_b16 v[128:129], v236 offset:32768\n"
    "ds_read_b64_tr_b16 v[130:131], v237 offset:32768\n"
    "v_mfma_f32_16x16x32_bf16 a[68:71], v[164:167], v[216:219], a[68:71]\n"
    "v_mfma_f32_16x16x32_bf16 a[100:103], v[164:167], v[220:223], a[100:103]\n"
    "ds_read_b64_tr_b16 v[132:133], v236 offset:32776\n"
    "ds_read_b64_tr_b16 v[134:135], v237 offset:32776\n"
    "v_mfma_f32_16x16x32_bf16 a[8:11], v[168:171], v[208:211], a[8:11]\n"
    "buffer_load_dwordx4 v241, s[48:51], s76 offen offset:1024 lds\n"
    "v_mfma_f32_16x16x32_bf16 a[40:43], v[168:171], v[212:215], a[40:43]\n"
    "ds_read_b64_tr_b16 v[136:137], v236 offset:33280\n"
    "ds_read_b64_tr_b16 v[138:139], v237 offset:33280\n"
    "v_mfma_f32_16x16x32_bf16 a[72:75], v[168:171], v[216:219], a[72:75]\n"
    "v_mfma_f32_16x16x32_bf16 a[104:107], v[168:171], v[220:223], a[104:107]\n"
    "ds_read_b64_tr_b16 v[140:141], v236 offset:33288\n"
    "ds_read_b64_tr_b16 v[142:143], v237 offset:33288\n"
    "v_mfma_f32_16x16x32_bf16 a[12:15], v[172:175], v[208:211], a[12:15]\n"
    "buffer_load_dwordx4 v242, s[48:51], s76 offen offset:2048 lds\n"
    "v_mfma_f32_16x16x32_bf16 a[44:47], v[172:175], v[212:215], a[44:47]\n"
    "ds_read_b64_tr_b16 v[144:145], v236 offset:33792\n"
    "ds_read_b64_tr_b16 v[146:147], v237 offset:33792\n"
    "v_mfma_f32_16x16x32_bf16 a[76:79], v[172:175], v[216:219], a[76:79]\n"
    "v_mfma_f32_16x16x32_bf16 a[108:111], v[172:175], v[220:223], a[108:111]\n"
    "ds_read_b64_tr_b16 v[148:149], v236 offset:33800\n"
    "ds_read_b64_tr_b16 v[150:151], v237 offset:33800\n"
    "v_mfma_f32_16x16x32_bf16 a[16:19], v[176:179], v[208:211], a[16:19]\n"
    "buffer_load_dwordx4 v243, s[48:51], s76 offen offset:3072 lds\n"
    "v_mfma_f32_16x16x32_bf16 a[48:51], v[176:179], v[212:215], a[48:51]\n"
    "ds_read_b64_tr_b16 v[152:153], v236 offset:34304\n"
    "ds_read_b64_tr_b16 v[154:155], v237 offset:34304\n"
    "v_mfma_f32_16x16x32_bf16 a[80:83], v[176:179], v[216:219], a[80:83]\n"
    "v_mfma_f32_16x16x32_bf16 a[112:115], v[176:179], v[220:223], a[112:115]\n"
    "ds_read_b64_tr_b16 v[156:157], v236 offset:34312\n"
    "ds_read_b64_tr_b16 v[158:159], v237 offset:34312\n"
    "v_mfma_f32_16x16x32_bf16 a[20:23], v[180:183], v[208:211], a[20:23]\n"
    "s_add_u32 m0, s72, 16384\n"
    "s_add_u32 s76, s80, 256\n"
    "v_mfma_f32_16x16x32_bf16 a[52:55], v[180:183], v[212:215], a[52:55]\n"
    "v_mfma_f32_16x16x32_bf16 a[84:87], v[180:183], v[216:219], a[84:87]\n"
    "buffer_load_dwordx4 v240, s[48:51], s76 offen offset:0 lds\n"
    "v_mfma_f32_16x16x32_bf16 a[116:119], v[180:183], v[220:223], a[116:119]\n"
    "v_mfma_f32_16x16x32_bf16 a[24:27], v[184:187], v[208:211], a[24:27]\n"
    "buffer_load_dwordx4 v241, s[48:51], s76 offen offset:1024 lds\n"
    "v_mfma_f32_16x16x32_bf16 a[56:59], v[184:187], v[212:215], a[56:59]\n"
    "v_mfma_f32_16x16x32_bf16 a[88:91], v[184:187], v[216:219], a[88:91]\n"
    "buffer_load_dwordx4 v242, s[48:51], s76 offen offset:2048 lds\n"
    "v_mfma_f32_16x16x32_bf16 a[120:123], v[184:187], v[220:223], a[120:123]\n"
    "v_mfma_f32_16x16x32_bf16 a[28:31], v[188:191], v[208:211], a[28:31]\n"
    "buffer_load_dwordx4 v243, s[48:51], s76 offen offset:3072 lds\n"
    "v_mfma_f32_16x16x32_bf16 a[60:63], v[188:191], v[212:215], a[60:63]\n"
    "v_mfma_f32_16x16x32_bf16 a[92:95], v[188:191], v[216:219], a[92:95]\n"
    "v_mfma_f32_16x16x32_bf16 a[124:127], v[188:191], v[220:223], a[124:127]\n"
    "s_waitcnt lgkmcnt(0)\n"
    "v_mfma_f32_16x16x32_bf16 v[0:3], v[128:131], v[192:195], v[0:3]\n"
    "s_add_u32 m0, s81, 0\n"
    "s_add_u32 s76, s79, 0\n"
    "v_mfma_f32_16x16x32_bf16 v[32:35], v[128:131], v[196:199], v[32:35]\n"
    "ds_read_b64_tr_b16 v[160:161], v236 offset:49152\n"
    "ds_read_b64_tr_b16 v[162:163], v237 offset:49152\n"
    "v_mfma_f32_16x16x32_bf16 v[64:67], v[128:131], v[200:203], v[64:67]\n"
    "buffer_load_dwordx4 v244, s[44:47], s76 offen lds\n"
    "v_mfma_f32_16x16x32_bf16 v[96:99], v[128:131], v[204:207], v[96:99]\n"
    "ds_read_b64_tr_b16 v[164:165], v236 offset:49160\n"
    "ds_read_b64_tr_b16 v[166:167], v237 offset:49160\n"
    "v_mfma_f32_16x16x32_bf16 v[4:7], v[132:135], v[192:195], v[4:7]\n"
    "v_mfma_f32_16x16x32_bf16 v[36:39], v[132:135], v[196:199], v[36:39]\n"
    "ds_read_b64_tr_b16 v[168:169], v236 offset:49664\n"
    "ds_read_b64_tr_b16 v[170:171], v237 offset:49664\n"
    "v_mfma_f32_16x16x32_bf16 v[68:71], v[132:135], v[200:203], v[68:71]\n"
    "s_add_u32 m0, s81, 1024\n"
    "s_add_u32 s76, s79, 10240\n"
    "v_mfma_f32_16x16x32_bf16 v[100:103], v[132:135], v[204:207], v[100:103]\n"
    "ds_read_b64_tr_b16 v[172:173], v236 offset:49672\n"
    "ds_read_b64_tr_b16 v[174:175], v237 offset:49672\n"
    "v_mfma_f32_16x16x32_bf16 v[8:11], v[136:139], v[192:195], v[8:11]\n"
    "buffer_load_dwordx4 v245, s[44:47], s76 offen lds\n"
    "v_mfma_f32_16x16x32_bf16 v[40:43], v[136:139], v[196:199], v[40:43]\n"
    "ds_read_b64_tr_b16 v[176:177], v236 offset:50176\n"
    "ds_read_b64_tr_b16 v[178:179], v237 offset:50176\n"
    "v_mfma_f32_16x16x32_bf16 v[72:75], v[136:139], v[200:203], v[72:75]\n"
    "v_mfma_f32_16x16x32_bf16 v[104:107], v[136:139], v[204:207], v[104:107]\n"
    "ds_read_b64_tr_b16 v[180:181], v236 offset:50184\n"
    "ds_read_b64_tr_b16 v[182:183], v237 offset:50184\n"
    "v_mfma_f32_16x16x32_bf16 v[12:15], v[140:143], v[192:195], v[12:15]\n"
    "s_add_u32 m0, s81, 2048\n"
    "s_add_u32 s76, s79, 20480\n"
    "v_mfma_f32_16x16x32_bf16 v[44:47], v[140:143], v[196:199], v[44:47]\n"
    "ds_read_b64_tr_b16 v[184:185], v236 offset:50688\n"
    "ds_read_b64_tr_b16 v[186:187], v237 offset:50688\n"
    "v_mfma_f32_16x16x32_bf16 v[76:79], v[140:143], v[200:203], v[76:79]\n"
    "v_mfma_f32_16x16x32_bf16 v[108:111], v[140:143], v[204:207], v[108:111]\n"
    "ds_read_b64_tr_b16 v[188:189], v236 offset:50696\n"
    "ds_read_b64_tr_b16 v[190:191], v237 offset:50696\n"
    "v_mfma_f32_16x16x32_bf16 v[16:19], v[144:147], v[192:195], v[16:19]\n"
    "buffer_load_dwordx4 v244, s[44:47], s76 offen lds\n"
    "v_mfma_f32_16x16x32_bf16 v[48:51], v[144:147], v[196:199], v[48:51]\n"
    "s_add_u32 m0, s81, 3072\n"
    "s_add_u32 s76, s79, 30720\n"
    "v_mfma_f32_16x16x32_bf16 v[80:83], v[144:147], v[200:203], v[80:83]\n"
    "v_mfma_f32_16x16x32_bf16 v[112:115], v[144:147], v[204:207], v[112:115]\n"
    "buffer_load_dwordx4 v245, s[44:47], s76 offen lds\n"
    "v_mfma_f32_16x16x32_bf16 v[20:23], v[148:151], v[192:195], v[20:23]\n"
    "s_add_u32 m0, s81, 4096\n"
    "s_add_u32 s76, s79, 40960\n"
    "v_mfma_f32_16x16x32_bf16 v[52:55], v[148:151], v[196:199], v[52:55]\n"
    "v_mfma_f32_16x16x32_bf16 v[84:87], v[148:151], v[200:203], v[84:87]\n"
    "buffer_load_dwordx4 v244, s[44:47], s76 offen lds\n"
    "v_mfma_f32_16x16x32_bf16 v[116:119], v[148:151], v[204:207], v[116:119]\n"
    "v_mfma_f32_16x16x32_bf16 v[24:27], v[152:155], v[192:195], v[24:27]\n"
    "s_add_u32 m0, s81, 5120\n"
    "s_add_u32 s76, s79, 51200\n"
    "v_mfma_f32_16x16x32_bf16 v[56:59], v[152:155], v[196:199], v[56:59]\n"
    "buffer_load_dwordx4 v245, s[44:47], s76 offen lds\n"
    "v_mfma_f32_16x16x32_bf16 v[88:91], v[152:155], v[200:203], v[88:91]\n"
    "v_mfma_f32_16x16x32_bf16 v[120:123], v[152:155], v[204:207], v[120:123]\n"
    "s_add_u32 m0, s81, 6144\n"
    "s_add_u32 s76, s79, 61440\n"
    "v_mfma_f32_16x16x32_bf16 v[28:31], v[156:159], v[192:195], v[28:31]\n"
    "v_mfma_f32_16x16x32_bf16 v[60:63], v[156:159], v[196:199], v[60:63]\n"
    "buffer_load_dwordx4 v244, s[44:47], s76 offen lds\n"
    "v_mfma_f32_16x16x32_bf16 v[92:95], v[156:159], v[200:203], v[92:95]\n"
    "s_add_u32 m0, s81, 7168\n"
    "s_add_u32 s76, s79, 71680\n"
    "v_mfma_f32_16x16x32_bf16 v[124:127], v[156:159], v[204:207], v[124:127]\n"
    "s_waitcnt lgkmcnt(0)\n"
    "v_mfma_f32_16x16x32_bf16 a[0:3], v[160:163], v[192:195], a[0:3]\n"
    "buffer_load_dwordx4 v245, s[44:47], s76 offen lds\n"
    "v_mfma_f32_16x16x32_bf16 a[32:35], v[160:163], v[196:199], a[32:35]\n"
    "ds_read_b128 v[208:211], v238 offset:33280\n"
    "ds_read_b128 v[212:215], v238 offset:35328\n"
    "v_mfma_f32_16x16x32_bf16 a[64:67], v[160:163], v[200:203], a[64:67]\n"
    "v_mfma_f32_16x16x32_bf16 a[96:99], v[160:163], v[204:207], a[96:99]\n"
    "ds_read_b128 v[216:219], v238 offset:37376\n"
    "ds_read_b128 v[220:223], v238 offset:39424\n"
    "v_mfma_f32_16x16x32_bf16 a[4:7], v[164:167], v[192:195], a[4:7]\n"
    "s_add_u32 s80, s80, 524288\n"
    "s_add_u32 s79, s79, 128\n"
    "v_mfma_f32_16x16x32_bf16 a[36:39], v[164:167], v[196:199], a[36:39]\n"
    "ds_read_b64_tr_b16 v[128:129], v236 offset:40960\n"
    "ds_read_b64_tr_b16 v[130:131], v237 offset:40960\n"
    "v_mfma_f32_16x16x32_bf16 a[68:71], v[164:167], v[200:203], a[68:71]\n"
    "v_mfma_f32_16x16x32_bf16 a[100:103], v[164:167], v[204:207], a[100:103]\n"
    "ds_read_b64_tr_b16 v[132:133], v236 offset:40968\n"
    "ds_read_b64_tr_b16 v[134:135], v237 offset:40968\n"
    "v_mfma_f32_16x16x32_bf16 a[8:11], v[168:171], v[192:195], a[8:11]\n"
    "v_mfma_f32_16x16x32_bf16 a[40:43], v[168:171], v[196:199], a[40:43]\n"
    "ds_read_b64_tr_b16 v[136:137], v236 offset:41472\n"
    "ds_read_b64_tr_b16 v[138:139], v237 offset:41472\n"
    "v_mfma_f32_16x16x32_bf16 a[72:75], v[168:171], v[200:203], a[72:75]\n"
    "buffer_load_dwordx4 a[252:255], v248, s[44:47], s86 offen\n"
    "v_mfma_f32_16x16x32_bf16 a[104:107], v[168:171], v[204:207], a[104:107]\n"
    "ds_read_b64_tr_b16 v[140:141], v236 offset:41480\n"
    "ds_read_b64_tr_b16 v[142:143], v237 offset:41480\n"
    "v_mfma_f32_16x16x32_bf16 a[12:15], v[172:175], v[192:195], a[12:15]\n"
    "v_mfma_f32_16x16x32_bf16 a[44:47], v[172:175], v[196:199], a[44:47]\n"
    "ds_read_b64_tr_b16 v[144:145], v236 offset:41984\n"
    "ds_read_b64_tr_b16 v[146:147], v237 offset:41984\n"
    "v_mfma_f32_16x16x32_bf16 a[76:79], v[172:175], v[200:203], a[76:79]\n"
    "v_mfma_f32_16x16x32_bf16 a[108:111], v[172:175], v[204:207], a[108:111]\n"
    "ds_read_b64_tr_b16 v[148:149], v236 offset:41992\n"
    "ds_read_b64_tr_b16 v[150:151], v237 offset:41992\n"
    "v_mfma_f32_16x16x32_bf16 a[16:19], v[176:179], v[192:195], a[16:19]\n"
    "v_mfma_f32_16x16x32_bf16 a[48:51], v[176:179], v[196:199], a[48:51]\n"
    "ds_read_b64_tr_b16 v[152:153], v236 offset:42496\n"
    "ds_read_b64_tr_b16 v[154:155], v237 offset:42496\n"
    "v_mfma_f32_16x16x32_bf16 a[80:83], v[176:179], v[200:203], a[80:83]\n"
    "buffer_load_dwordx4 a[252:255], v249, s[48:51], s87 offen offset:1024\n"
    "v_mfma_f32_16x16x32_bf16 a[112:115], v[176:179], v[204:207], a[112:115]\n"
    "ds_read_b64_tr_b16 v[156:157], v236 offset:42504\n"
    "ds_read_b64_tr_b16 v[158:159], v237 offset:42504\n"
    "v_mfma_f32_16x16x32_bf16 a[20:23], v[180:183], v[192:195], a[20:23]\n"
    "v_mfma_f32_16x16x32_bf16 a[52:55], v[180:183], v[196:199], a[52:55]\n"
    "v_mfma_f32_16x16x32_bf16 a[84:87], v[180:183], v[200:203], a[84:87]\n"
    "v_mfma_f32_16x16x32_bf16 a[116:119], v[180:183], v[204:207], a[116:119]\n"
    "s_add_u32 s87, s87, 16384\n"
    "s_add_u32 s86, s86, 128\n"
    "v_mfma_f32_16x16x32_bf16 a[24:27], v[184:187], v[192:195], a[24:27]\n"
    "v_mfma_f32_16x16x32_bf16 a[56:59], v[184:187], v[196:199], a[56:59]\n"
    "v_mfma_f32_16x16x32_bf16 a[88:91], v[184:187], v[200:203], a[88:91]\n"
    "v_mfma_f32_16x16x32_bf16 a[120:123], v[184:187], v[204:207], a[120:123]\n"
    "buffer_load_dwordx4 a[252:255], v249, s[48:51], s87 offen offset:1024\n"
    "v_mfma_f32_16x16x32_bf16 a[28:31], v[188:191], v[192:195], a[28:31]\n"
    "v_mfma_f32_16x16x32_bf16 a[60:63], v[188:191], v[196:199], a[60:63]\n"
    "v_mfma_f32_16x16x32_bf16 a[92:95], v[188:191], v[200:203], a[92:95]\n"
    "v_mfma_f32_16x16x32_bf16 a[124:127], v[188:191], v[204:207], a[124:127]\n"
    "s_waitcnt lgkmcnt(0)\n"
    "v_mfma_f32_16x16x32_bf16 v[0:3], v[128:131], v[208:211], v[0:3]\n"
    "s_add_u32 s87, s87, 507904\n"
    "v_mfma_f32_16x16x32_bf16 v[32:35], v[128:131], v[212:215], v[32:35]\n"
    "ds_read_b64_tr_b16 v[160:161], v236 offset:57344\n"
    "ds_read_b64_tr_b16 v[162:163], v237 offset:57344\n"
    "v_mfma_f32_16x16x32_bf16 v[64:67], v[128:131], v[216:219], v[64:67]\n"
    "v_mfma_f32_16x16x32_bf16 v[96:99], v[128:131], v[220:223], v[96:99]\n"
    "ds_read_b64_tr_b16 v[164:165], v236 offset:57352\n"
    "ds_read_b64_tr_b16 v[166:167], v237 offset:57352\n"
    "v_mfma_f32_16x16x32_bf16 v[4:7], v[132:135], v[208:211], v[4:7]\n"
    "buffer_load_dwordx4 a[252:255], v248, s[44:47], s86 offen\n"
    "v_mfma_f32_16x16x32_bf16 v[36:39], v[132:135], v[212:215], v[36:39]\n"
    "ds_read_b64_tr_b16 v[168:169], v236 offset:57856\n"
    "ds_read_b64_tr_b16 v[170:171], v237 offset:57856\n"
    "v_mfma_f32_16x16x32_bf16 v[68:71], v[132:135], v[216:219], v[68:71]\n"
    "v_mfma_f32_16x16x32_bf16 v[100:103], v[132:135], v[220:223], v[100:103]\n"
    "ds_read_b64_tr_b16 v[172:173], v236 offset:57864\n"
    "ds_read_b64_tr_b16 v[174:175], v237 offset:57864\n"
    "v_mfma_f32_16x16x32_bf16 v[8:11], v[136:139], v[208:211], v[8:11]\n"
    "v_mfma_f32_16x16x32_bf16 v[40:43], v[136:139], v[212:215], v[40:43]\n"
    "ds_read_b64_tr_b16 v[176:177], v236 offset:58368\n"
    "ds_read_b64_tr_b16 v[178:179], v237 offset:58368\n"
    "v_mfma_f32_16x16x32_bf16 v[72:75], v[136:139], v[216:219], v[72:75]\n"
    "v_mfma_f32_16x16x32_bf16 v[104:107], v[136:139], v[220:223], v[104:107]\n"
    "ds_read_b64_tr_b16 v[180:181], v236 offset:58376\n"
    "ds_read_b64_tr_b16 v[182:183], v237 offset:58376\n"
    "v_mfma_f32_16x16x32_bf16 v[12:15], v[140:143], v[208:211], v[12:15]\n"
    "v_mfma_f32_16x16x32_bf16 v[44:47], v[140:143], v[212:215], v[44:47]\n"
    "ds_read_b64_tr_b16 v[184:185], v236 offset:58880\n"
    "ds_read_b64_tr_b16 v[186:187], v237 offset:58880\n"
    "v_mfma_f32_16x16x32_bf16 v[76:79], v[140:143], v[216:219], v[76:79]\n"
    "buffer_load_dwordx4 a[252:255], v249, s[48:51], s87 offen offset:1024\n"
    "v_mfma_f32_16x16x32_bf16 v[108:111], v[140:143], v[220:223], v[108:111]\n"
    "ds_read_b64_tr_b16 v[188:189], v236 offset:58888\n"
    "ds_read_b64_tr_b16 v[190:191], v237 offset:58888\n"
    "v_mfma_f32_16x16x32_bf16 v[16:19], v[144:147], v[208:211], v[16:19]\n"
    "v_mfma_f32_16x16x32_bf16 v[48:51], v[144:147], v[212:215], v[48:51]\n"
    "v_mfma_f32_16x16x32_bf16 v[80:83], v[144:147], v[216:219], v[80:83]\n"
    "v_mfma_f32_16x16x32_bf16 v[112:115], v[144:147], v[220:223], v[112:115]\n"
    "s_add_u32 s87, s87, 16384\n"
    "s_add_u32 s86, s86, 128\n"
    "v_mfma_f32_16x16x32_bf16 v[20:23], v[148:151], v[208:211], v[20:23]\n"
    "v_mfma_f32_16x16x32_bf16 v[52:55], v[148:151], v[212:215], v[52:55]\n"
    "v_mfma_f32_16x16x32_bf16 v[84:87], v[148:151], v[216:219], v[84:87]\n"
    "v_mfma_f32_16x16x32_bf16 v[116:119], v[148:151], v[220:223], v[116:119]\n"
    "buffer_load_dwordx4 a[252:255], v249, s[48:51], s87 offen offset:1024\n"
    "v_mfma_f32_16x16x32_bf16 v[24:27], v[152:155], v[208:211], v[24:27]\n"
    "v_mfma_f32_16x16x32_bf16 v[56:59], v[152:155], v[212:215], v[56:59]\n"
    "v_mfma_f32_16x16x32_bf16 v[88:91], v[152:155], v[216:219], v[88:91]\n"
    "v_mfma_f32_16x16x32_bf16 v[120:123], v[152:155], v[220:223], v[120:123]\n"
    "s_add_u32 s87, s87, 507904\n"
    "v_mfma_f32_16x16x32_bf16 v[28:31], v[156:159], v[208:211], v[28:31]\n"
    "v_mfma_f32_16x16x32_bf16 v[60:63], v[156:159], v[212:215], v[60:63]\n"
    "v_mfma_f32_16x16x32_bf16 v[92:95], v[156:159], v[216:219], v[92:95]\n"
    "v_mfma_f32_16x16x32_bf16 v[124:127], v[156:159], v[220:223], v[124:127]\n"
    "s_waitcnt lgkmcnt(0)\n"
    "v_mfma_f32_16x16x32_bf16 a[0:3], v[160:163], v[208:211], a[0:3]\n"
    "s_waitcnt vmcnt(6)\n"
    "s_barrier\n"
    "v_mfma_f32_16x16x32_bf16 a[32:35], v[160:163], v[212:215], a[32:35]\n"
    "ds_read_b128 v[192:195], v238 offset:0\n"
    "ds_read_b128 v[196:199], v238 offset:2048\n"
    "v_mfma_f32_16x16x32_bf16 a[64:67], v[160:163], v[216:219], a[64:67]\n"
    "s_add_u32 m0, s72, 32768\n"
    "s_add_u32 s76, s80, 0\n"
    "v_mfma_f32_16x16x32_bf16 a[96:99], v[160:163], v[220:223], a[96:99]\n"
    "ds_read_b128 v[200:203], v238 offset:4096\n"
    "ds_read_b128 v[204:207], v238 offset:6144\n"
    "v_mfma_f32_16x16x32_bf16 a[4:7], v[164:167], v[208:211], a[4:7]\n"
    "buffer_load_dwordx4 v240, s[48:51], s76 offen offset:0 lds\n"
    "v_mfma_f32_16x16x32_bf16 a[36:39], v[164:167], v[212:215], a[36:39]\n"
    "ds_read_b64_tr_b16 v[128:129], v236 offset:0\n"
    "ds_read_b64_tr_b16 v[130:131], v237 offset:0\n"
    "v_mfma_f32_16x16x32_bf16 a[68:71], v[164:167], v[216:219], a[68:71]\n"
    "v_mfma_f32_16x16x32_bf16 a[100:103], v[164:167], v[220:223], a[100:103]\n"
    "ds_read_b64_tr_b16 v[132:133], v236 offset:8\n"
    "ds_read_b64_tr_b16 v[134:135], v237 offset:8\n"
    "v_mfma_f32_16x16x32_bf16 a[8:11], v[168:171], v[208:211], a[8:11]\n"
    "buffer_load_dwordx4 v241, s[48:51], s76 offen offset:1024 lds\n"
    "v_mfma_f32_16x16x32_bf16 a[40:43], v[168:171], v[212:215], a[40:43]\n"
    "ds_read_b64_tr_b16 v[136:137], v236 offset:512\n"
    "ds_read_b64_tr_b16 v[138:139], v237 offset:512\n"
    "v_mfma_f32_16x16x32_bf16 a[72:75], v[168:171], v[216:219], a[72:75]\n"
    "v_mfma_f32_16x16x32_bf16 a[104:107], v[168:171], v[220:223], a[104:107]\n"
    "ds_read_b64_tr_b16 v[140:141], v236 offset:520\n"
    "ds_read_b64_tr_b16 v[142:143], v237 offset:520\n"
    "v_mfma_f32_16x16x32_bf16 a[12:15], v[172:175], v[208:211], a[12:15]\n"
    "buffer_load_dwordx4 v242, s[48:51], s76 offen offset:2048 lds\n"
    "v_mfma_f32_16x16x32_bf16 a[44:47], v[172:175], v[212:215], a[44:47]\n"
    "ds_read_b64_tr_b16 v[144:145], v236 offset:1024\n"
    "ds_read_b64_tr_b16 v[146:147], v237 offset:1024\n"
    "v_mfma_f32_16x16x32_bf16 a[76:79], v[172:175], v[216:219], a[76:79]\n"
    "v_mfma_f32_16x16x32_bf16 a[108:111], v[172:175], v[220:223], a[108:111]\n"
    "ds_read_b64_tr_b16 v[148:149], v236 offset:1032\n"
    "ds_read_b64_tr_b16 v[150:151], v237 offset:1032\n"
    "v_mfma_f32_16x16x32_bf16 a[16:19], v[176:179], v[208:211], a[16:19]\n"
    "buffer_load_dwordx4 v243, s[48:51], s76 offen offset:3072 lds\n"
    "v_mfma_f32_16x16x32_bf16 a[48:51], v[176:179], v[212:215], a[48:51]\n"
    "ds_read_b64_tr_b16 v[152:153], v236 offset:1536\n"
    "ds_read_b64_tr_b16 v[154:155], v237 offset:1536\n"
    "v_mfma_f32_16x16x32_bf16 a[80:83], v[176:179], v[216:219], a[80:83]\n"
    "v_mfma_f32_16x16x32_bf16 a[112:115], v[176:179], v[220:223], a[112:115]\n"
    "ds_read_b64_tr_b16 v[156:157], v236 offset:1544\n"
    "ds_read_b64_tr_b16 v[158:159], v237 offset:1544\n"
    "v_mfma_f32_16x16x32_bf16 a[20:23], v[180:183], v[208:211], a[20:23]\n"
    "s_add_u32 m0, s72, 49152\n"
    "s_add_u32 s76, s80, 256\n"
    "v_mfma_f32_16x16x32_bf16 a[52:55], v[180:183], v[212:215], a[52:55]\n"
    "v_mfma_f32_16x16x32_bf16 a[84:87], v[180:183], v[216:219], a[84:87]\n"
    "buffer_load_dwordx4 v240, s[48:51], s76 offen offset:0 lds\n"
    "v_mfma_f32_16x16x32_bf16 a[116:119], v[180:183], v[220:223], a[116:119]\n"
    "v_mfma_f32_16x16x32_bf16 a[24:27], v[184:187], v[208:211], a[24:27]\n"
    "buffer_load_dwordx4 v241, s[48:51], s76 offen offset:1024 lds\n"
    "v_mfma_f32_16x16x32_bf16 a[56:59], v[184:187], v[212:215], a[56:59]\n"
    "v_mfma_f32_16x16x32_bf16 a[88:91], v[184:187], v[216:219], a[88:91]\n"
    "buffer_load_dwordx4 v242, s[48:51], s76 offen offset:2048 lds\n"
    "v_mfma_f32_16x16x32_bf16 a[120:123], v[184:187], v[220:223], a[120:123]\n"
    "v_mfma_f32_16x16x32_bf16 a[28:31], v[188:191], v[208:211], a[28:31]\n"
    "buffer_load_dwordx4 v243, s[48:51], s76 offen offset:3072 lds\n"
    "v_mfma_f32_16x16x32_bf16 a[60:63], v[188:191], v[212:215], a[60:63]\n"
    "v_mfma_f32_16x16x32_bf16 a[92:95], v[188:191], v[216:219], a[92:95]\n"
    "v_mfma_f32_16x16x32_bf16 a[124:127], v[188:191], v[220:223], a[124:127]\n"
    "s_waitcnt lgkmcnt(0)\n"
    "v_mfma_f32_16x16x32_bf16 v[0:3], v[128:131], v[192:195], v[0:3]\n"
    "s_add_u32 m0, s81, 32768\n"
    "s_add_u32 s76, s79, 0\n"
    "v_mfma_f32_16x16x32_bf16 v[32:35], v[128:131], v[196:199], v[32:35]\n"
    "ds_read_b64_tr_b16 v[160:161], v236 offset:16384\n"
    "ds_read_b64_tr_b16 v[162:163], v237 offset:16384\n"
    "v_mfma_f32_16x16x32_bf16 v[64:67], v[128:131], v[200:203], v[64:67]\n"
    "buffer_load_dwordx4 v244, s[44:47], s76 offen lds\n"
    "v_mfma_f32_16x16x32_bf16 v[96:99], v[128:131], v[204:207], v[96:99]\n"
    "ds_read_b64_tr_b16 v[164:165], v236 offset:16392\n"
    "ds_read_b64_tr_b16 v[166:167], v237 offset:16392\n"
    "v_mfma_f32_16x16x32_bf16 v[4:7], v[132:135], v[192:195], v[4:7]\n"
    "v_mfma_f32_16x16x32_bf16 v[36:39], v[132:135], v[196:199], v[36:39]\n"
    "ds_read_b64_tr_b16 v[168:169], v236 offset:16896\n"
    "ds_read_b64_tr_b16 v[170:171], v237 offset:16896\n"
    "v_mfma_f32_16x16x32_bf16 v[68:71], v[132:135], v[200:203], v[68:71]\n"
    "s_add_u32 m0, s81, 33792\n"
    "s_add_u32 s76, s79, 10240\n"
    "v_mfma_f32_16x16x32_bf16 v[100:103], v[132:135], v[204:207], v[100:103]\n"
    "ds_read_b64_tr_b16 v[172:173], v236 offset:16904\n"
    "ds_read_b64_tr_b16 v[174:175], v237 offset:16904\n"
    "v_mfma_f32_16x16x32_bf16 v[8:11], v[136:139], v[192:195], v[8:11]\n"
    "buffer_load_dwordx4 v245, s[44:47], s76 offen lds\n"
    "v_mfma_f32_16x16x32_bf16 v[40:43], v[136:139], v[196:199], v[40:43]\n"
    "ds_read_b64_tr_b16 v[176:177], v236 offset:17408\n"
    "ds_read_b64_tr_b16 v[178:179], v237 offset:17408\n"
    "v_mfma_f32_16x16x32_bf16 v[72:75], v[136:139], v[200:203], v[72:75]\n"
    "v_mfma_f32_16x16x32_bf16 v[104:107], v[136:139], v[204:207], v[104:107]\n"
    "ds_read_b64_tr_b16 v[180:181], v236 offset:17416\n"
    "ds_read_b64_tr_b16 v[182:183], v237 offset:17416\n"
    "v_mfma_f32_16x16x32_bf16 v[12:15], v[140:143], v[192:195], v[12:15]\n"
    "s_add_u32 m0, s81, 34816\n"
    "s_add_u32 s76, s79, 20480\n"
    "v_mfma_f32_16x16x32_bf16 v[44:47], v[140:143], v[196:199], v[44:47]\n"
    "ds_read_b64_tr_b16 v[184:185], v236 offset:17920\n"
    "ds_read_b64_tr_b16 v[186:187], v237 offset:17920\n"
    "v_mfma_f32_16x16x32_bf16 v[76:79], v[140:143], v[200:203], v[76:79]\n"
    "v_mfma_f32_16x16x32_bf16 v[108:111], v[140:143], v[204:207], v[108:111]\n"
    "ds_read_b64_tr_b16 v[188:189], v236 offset:17928\n"
    "ds_read_b64_tr_b16 v[190:191], v237 offset:17928\n"
    "v_mfma_f32_16x16x32_bf16 v[16:19], v[144:147], v[192:195], v[16:19]\n"
    "buffer_load_dwordx4 v244, s[44:47], s76 offen lds\n"
    "v_mfma_f32_16x16x32_bf16 v[48:51], v[144:147], v[196:199], v[48:51]\n"
    "s_add_u32 m0, s81, 35840\n"
    "s_add_u32 s76, s79, 30720\n"
    "v_mfma_f32_16x16x32_bf16 v[80:83], v[144:147], v[200:203], v[80:83]\n"
    "v_mfma_f32_16x16x32_bf16 v[112:115], v[144:147], v[204:207], v[112:115]\n"
    "buffer_load_dwordx4 v245, s[44:47], s76 offen lds\n"
    "v_mfma_f32_16x16x32_bf16 v[20:23], v[148:151], v[192:195], v[20:23]\n"
    "s_add_u32 m0, s81, 36864\n"
    "s_add_u32 s76, s79, 40960\n"
    "v_mfma_f32_16x16x32_bf16 v[52:55], v[148:151], v[196:199], v[52:55]\n"
    "v_mfma_f32_16x16x32_bf16 v[84:87], v[148:151], v[200:203], v[84:87]\n"
    "buffer_load_dwordx4 v244, s[44:47], s76 offen lds\n"
    "v_mfma_f32_16x16x32_bf16 v[116:119], v[148:151], v[204:207], v[116:119]\n"
    "v_mfma_f32_16x16x32_bf16 v[24:27], v[152:155], v[192:195], v[24:27]\n"
    "s_add_u32 m0, s81, 37888\n"
    "s_add_u32 s76, s79, 51200\n"
    "v_mfma_f32_16x16x32_bf16 v[56:59], v[152:155], v[196:199], v[56:59]\n"
    "buffer_load_dwordx4 v245, s[44:47], s76 offen lds\n"
    "v_mfma_f32_16x16x32_bf16 v[88:91], v[152:155], v[200:203], v[88:91]\n"
    "v_mfma_f32_16x16x32_bf16 v[120:123], v[152:155], v[204:207], v[120:123]\n"
    "s_add_u32 m0, s81, 38912\n"
    "s_add_u32 s76, s79, 61440\n"
    "v_mfma_f32_16x16x32_bf16 v[28:31], v[156:159], v[192:195], v[28:31]\n"
    "v_mfma_f32_16x16x32_bf16 v[60:63], v[156:159], v[196:199], v[60:63]\n"
    "buffer_load_dwordx4 v244, s[44:47], s76 offen lds\n"
    "v_mfma_f32_16x16x32_bf16 v[92:95], v[156:159], v[200:203], v[92:95]\n"
    "s_add_u32 m0, s81, 39936\n"
    "s_add_u32 s76, s79, 71680\n"
    "v_mfma_f32_16x16x32_bf16 v[124:127], v[156:159], v[204:207], v[124:127]\n"
    "s_waitcnt lgkmcnt(0)\n"
    "v_mfma_f32_16x16x32_bf16 a[0:3], v[160:163], v[192:195], a[0:3]\n"
    "buffer_load_dwordx4 v245, s[44:47], s76 offen lds\n"
    "v_mfma_f32_16x16x32_bf16 a[32:35], v[160:163], v[196:199], a[32:35]\n"
    "ds_read_b128 v[208:211], v238 offset:512\n"
    "ds_read_b128 v[212:215], v238 offset:2560\n"
    "v_mfma_f32_16x16x32_bf16 a[64:67], v[160:163], v[200:203], a[64:67]\n"
    "v_mfma_f32_16x16x32_bf16 a[96:99], v[160:163], v[204:207], a[96:99]\n"
    "ds_read_b128 v[216:219], v238 offset:4608\n"
    "ds_read_b128 v[220:223], v238 offset:6656\n"
    "v_mfma_f32_16x16x32_bf16 a[4:7], v[164:167], v[192:195], a[4:7]\n"
    "s_add_u32 s80, s80, 524288\n"
    "s_add_u32 s79, s79, 128\n"
    "v_mfma_f32_16x16x32_bf16 a[36:39], v[164:167], v[196:199], a[36:39]\n"
    "ds_read_b64_tr_b16 v[128:129], v236 offset:8192\n"
    "ds_read_b64_tr_b16 v[130:131], v237 offset:8192\n"
    "v_mfma_f32_16x16x32_bf16 a[68:71], v[164:167], v[200:203], a[68:71]\n"
    "v_mfma_f32_16x16x32_bf16 a[100:103], v[164:167], v[204:207], a[100:103]\n"
    "ds_read_b64_tr_b16 v[132:133], v236 offset:8200\n"
    "ds_read_b64_tr_b16 v[134:135], v237 offset:8200\n"
    "v_mfma_f32_16x16x32_bf16 a[8:11], v[168:171], v[192:195], a[8:11]\n"
    "v_mfma_f32_16x16x32_bf16 a[40:43], v[168:171], v[196:199], a[40:43]\n"
    "ds_read_b64_tr_b16 v[136:137], v236 offset:8704\n"
    "ds_read_b64_tr_b16 v[138:139], v237 offset:8704\n"
    "v_mfma_f32_16x16x32_bf16 a[72:75], v[168:171], v[200:203], a[72:75]\n"
    "buffer_load_dwordx4 a[252:255], v248, s[44:47], s86 offen\n"
    "v_mfma_f32_16x16x32_bf16 a[104:107], v[168:171], v[204:207], a[104:107]\n"
    "ds_read_b64_tr_b16 v[140:141], v236 offset:8712\n"
    "ds_read_b64_tr_b16 v[142:143], v237 offset:8712\n"
    "v_mfma_f32_16x16x32_bf16 a[12:15], v[172:175], v[192:195], a[12:15]\n"
    "v_mfma_f32_16x16x32_bf16 a[44:47], v[172:175], v[196:199], a[44:47]\n"
    "ds_read_b64_tr_b16 v[144:145], v236 offset:9216\n"
    "ds_read_b64_tr_b16 v[146:147], v237 offset:9216\n"
    "v_mfma_f32_16x16x32_bf16 a[76:79], v[172:175], v[200:203], a[76:79]\n"
    "v_mfma_f32_16x16x32_bf16 a[108:111], v[172:175], v[204:207], a[108:111]\n"
    "ds_read_b64_tr_b16 v[148:149], v236 offset:9224\n"
    "ds_read_b64_tr_b16 v[150:151], v237 offset:9224\n"
    "v_mfma_f32_16x16x32_bf16 a[16:19], v[176:179], v[192:195], a[16:19]\n"
    "buffer_load_dwordx4 a[252:255], v249, s[48:51], s87 offen offset:1024\n"
    "v_mfma_f32_16x16x32_bf16 a[48:51], v[176:179], v[196:199], a[48:51]\n"
    "ds_read_b64_tr_b16 v[152:153], v236 offset:9728\n"
    "ds_read_b64_tr_b16 v[154:155], v237 offset:9728\n"
    "v_mfma_f32_16x16x32_bf16 a[80:83], v[176:179], v[200:203], a[80:83]\n"
    "v_mfma_f32_16x16x32_bf16 a[112:115], v[176:179], v[204:207], a[112:115]\n"
    "ds_read_b64_tr_b16 v[156:157], v236 offset:9736\n"
    "ds_read_b64_tr_b16 v[158:159], v237 offset:9736\n"
    "v_mfma_f32_16x16x32_bf16 a[20:23], v[180:183], v[192:195], a[20:23]\n"
    "v_mfma_f32_16x16x32_bf16 a[52:55], v[180:183], v[196:199], a[52:55]\n"
    "s_add_u32 s87, s87, 16384\n"
    "s_add_u32 s86, s86, 128\n"
    "v_mfma_f32_16x16x32_bf16 a[84:87], v[180:183], v[200:203], a[84:87]\n"
    "v_mfma_f32_16x16x32_bf16 a[116:119], v[180:183], v[204:207], a[116:119]\n"
    "v_mfma_f32_16x16x32_bf16 a[24:27], v[184:187], v[192:195], a[24:27]\n"
    "buffer_load_dwordx4 a[252:255], v249, s[48:51], s87 offen offset:1024\n"
    "v_mfma_f32_16x16x32_bf16 a[56:59], v[184:187], v[196:199], a[56:59]\n"
    "v_mfma_f32_16x16x32_bf16 a[88:91], v[184:187], v[200:203], a[88:91]\n"
    "v_mfma_f32_16x16x32_bf16 a[120:123], v[184:187], v[204:207], a[120:123]\n"
    "s_add_u32 s87, s87, 507904\n"
    "v_mfma_f32_16x16x32_bf16 a[28:31], v[188:191], v[192:195], a[28:31]\n"
    "v_mfma_f32_16x16x32_bf16 a[60:63], v[188:191], v[196:199], a[60:63]\n"
    "s_movk_i32 s77, 0\n"
    "buffer_load_dwordx4 a[252:255], v229, s[60:63], s77 offen offset:0\n"
    "v_mfma_f32_16x16x32_bf16 a[92:95], v[188:191], v[200:203], a[92:95]\n"
    "v_mfma_f32_16x16x32_bf16 a[124:127], v[188:191], v[204:207], a[124:127]\n"
    "s_waitcnt lgkmcnt(0)\n"
    "v_mfma_f32_16x16x32_bf16 v[0:3], v[128:131], v[208:211], v[0:3]\n"
    "v_mfma_f32_16x16x32_bf16 v[32:35], v[128:131], v[212:215], v[32:35]\n"
    "ds_read_b64_tr_b16 v[160:161], v236 offset:24576\n"
    "ds_read_b64_tr_b16 v[162:163], v237 offset:24576\n"
    "v_mfma_f32_16x16x32_bf16 v[64:67], v[128:131], v[216:219], v[64:67]\n"
    "v_mfma_f32_16x16x32_bf16 v[96:99], v[128:131], v[220:223], v[96:99]\n"
    "ds_read_b64_tr_b16 v[164:165], v236 offset:24584\n"
    "ds_read_b64_tr_b16 v[166:167], v237 offset:24584\n"
    "v_mfma_f32_16x16x32_bf16 v[4:7], v[132:135], v[208:211], v[4:7]\n"
    "s_movk_i32 s77, 0\n"
    "buffer_load_dwordx4 a[252:255], v229, s[60:63], s77 offen offset:1024\n"
    "v_mfma_f32_16x16x32_bf16 v[36:39], v[132:135], v[212:215], v[36:39]\n"
    "ds_read_b64_tr_b16 v[168:169], v236 offset:25088\n"
    "ds_read_b64_tr_b16 v[170:171], v237 offset:25088\n"
    "v_mfma_f32_16x16x32_bf16 v[68:71], v[132:135], v[216:219], v[68:71]\n"
    "v_mfma_f32_16x16x32_bf16 v[100:103], v[132:135], v[220:223], v[100:103]\n"
    "ds_read_b64_tr_b16 v[172:173], v236 offset:25096\n"
    "ds_read_b64_tr_b16 v[174:175], v237 offset:25096\n"
    "v_mfma_f32_16x16x32_bf16 v[8:11], v[136:139], v[208:211], v[8:11]\n"
    "v_mfma_f32_16x16x32_bf16 v[40:43], v[136:139], v[212:215], v[40:43]\n"
    "ds_read_b64_tr_b16 v[176:177], v236 offset:25600\n"
    "ds_read_b64_tr_b16 v[178:179], v237 offset:25600\n"
    "v_mfma_f32_16x16x32_bf16 v[72:75], v[136:139], v[216:219], v[72:75]\n"
    "v_mfma_f32_16x16x32_bf16 v[104:107], v[136:139], v[220:223], v[104:107]\n"
    "ds_read_b64_tr_b16 v[180:181], v236 offset:25608\n"
    "ds_read_b64_tr_b16 v[182:183], v237 offset:25608\n"
    "v_mfma_f32_16x16x32_bf16 v[12:15], v[140:143], v[208:211], v[12:15]\n"
    "v_mfma_f32_16x16x32_bf16 v[44:47], v[140:143], v[212:215], v[44:47]\n"
    "ds_read_b64_tr_b16 v[184:185], v236 offset:26112\n"
    "ds_read_b64_tr_b16 v[186:187], v237 offset:26112\n"
    "v_mfma_f32_16x16x32_bf16 v[76:79], v[140:143], v[216:219], v[76:79]\n"
    "s_movk_i32 s77, 0\n"
    "buffer_load_dwordx4 a[252:255], v229, s[60:63], s77 offen offset:2048\n"
    "v_mfma_f32_16x16x32_bf16 v[108:111], v[140:143], v[220:223], v[108:111]\n"
    "ds_read_b64_tr_b16 v[188:189], v236 offset:26120\n"
    "ds_read_b64_tr_b16 v[190:191], v237 offset:26120\n"
    "v_mfma_f32_16x16x32_bf16 v[16:19], v[144:147], v[208:211], v[16:19]\n"
    "v_mfma_f32_16x16x32_bf16 v[48:51], v[144:147], v[212:215], v[48:51]\n"
    "v_mfma_f32_16x16x32_bf16 v[80:83], v[144:147], v[216:219], v[80:83]\n"
    "v_mfma_f32_16x16x32_bf16 v[112:115], v[144:147], v[220:223], v[112:115]\n"
    "v_mfma_f32_16x16x32_bf16 v[20:23], v[148:151], v[208:211], v[20:23]\n"
    "s_movk_i32 s77, 0\n"
    "buffer_load_dwordx4 a[252:255], v229, s[60:63], s77 offen offset:3072\n"
    "v_mfma_f32_16x16x32_bf16 v[52:55], v[148:151], v[212:215], v[52:55]\n"
    "v_mfma_f32_16x16x32_bf16 v[84:87], v[148:151], v[216:219], v[84:87]\n"
    "v_mfma_f32_16x16x32_bf16 v[116:119], v[148:151], v[220:223], v[116:119]\n"
    "v_mfma_f32_16x16x32_bf16 v[24:27], v[152:155], v[208:211], v[24:27]\n"
    "v_mfma_f32_16x16x32_bf16 v[56:59], v[152:155], v[212:215], v[56:59]\n"
    "s_movk_i32 s77, 4096\n"
    "buffer_load_dwordx4 a[252:255], v229, s[60:63], s77 offen offset:0\n"
    "v_mfma_f32_16x16x32_bf16 v[88:91], v[152:155], v[216:219], v[88:91]\n"
    "v_mfma_f32_16x16x32_bf16 v[120:123], v[152:155], v[220:223], v[120:123]\n"
    "v_mfma_f32_16x16x32_bf16 v[28:31], v[156:159], v[208:211], v[28:31]\n"
    "v_mfma_f32_16x16x32_bf16 v[60:63], v[156:159], v[212:215], v[60:63]\n"
    "v_mfma_f32_16x16x32_bf16 v[92:95], v[156:159], v[216:219], v[92:95]\n"
    "v_mfma_f32_16x16x32_bf16 v[124:127], v[156:159], v[220:223], v[124:127]\n"
    "s_waitcnt lgkmcnt(0)\n"
    "v_mfma_f32_16x16x32_bf16 a[0:3], v[160:163], v[208:211], a[0:3]\n"
    "s_waitcnt vmcnt(8)\n"
    "s_barrier\n"
    "v_mfma_f32_16x16x32_bf16 a[32:35], v[160:163], v[212:215], a[32:35]\n"
    "ds_read_b128 v[192:195], v238 offset:32768\n"
    "ds_read_b128 v[196:199], v238 offset:34816\n"
    "v_mfma_f32_16x16x32_bf16 a[64:67], v[160:163], v[216:219], a[64:67]\n"
    "s_add_u32 m0, s72, 0\n"
    "s_add_u32 s76, s80, 0\n"
    "v_mfma_f32_16x16x32_bf16 a[96:99], v[160:163], v[220:223], a[96:99]\n"
    "ds_read_b128 v[200:203], v238 offset:36864\n"
    "ds_read_b128 v[204:207], v238 offset:38912\n"
    "v_mfma_f32_16x16x32_bf16 a[4:7], v[164:167], v[208:211], a[4:7]\n"
    "buffer_load_dwordx4 v240, s[48:51], s76 offen offset:0 lds\n"
    "v_mfma_f32_16x16x32_bf16 a[36:39], v[164:167], v[212:215], a[36:39]\n"
    "ds_read_b64_tr_b16 v[128:129], v236 offset:32768\n"
    "ds_read_b64_tr_b16 v[130:131], v237 offset:32768\n"
    "v_mfma_f32_16x16x32_bf16 a[68:71], v[164:167], v[216:219], a[68:71]\n"
    "v_mfma_f32_16x16x32_bf16 a[100:103], v[164:167], v[220:223], a[100:103]\n"
    "ds_read_b64_tr_b16 v[132:133], v236 offset:32776\n"
    "ds_read_b64_tr_b16 v[134:135], v237 offset:32776\n"
    "v_mfma_f32_16x16x32_bf16 a[8:11], v[168:171], v[208:211], a[8:11]\n"
    "buffer_load_dwordx4 v241, s[48:51], s76 offen offset:1024 lds\n"
    "v_mfma_f32_16x16x32_bf16 a[40:43], v[168:171], v[212:215], a[40:43]\n"
    "ds_read_b64_tr_b16 v[136:137], v236 offset:33280\n"
    "ds_read_b64_tr_b16 v[138:139], v237 offset:33280\n"
    "v_mfma_f32_16x16x32_bf16 a[72:75], v[168:171], v[216:219], a[72:75]\n"
    "v_mfma_f32_16x16x32_bf16 a[104:107], v[168:171], v[220:223], a[104:107]\n"
    "ds_read_b64_tr_b16 v[140:141], v236 offset:33288\n"
    "ds_read_b64_tr_b16 v[142:143], v237 offset:33288\n"
    "v_mfma_f32_16x16x32_bf16 a[12:15], v[172:175], v[208:211], a[12:15]\n"
    "buffer_load_dwordx4 v242, s[48:51], s76 offen offset:2048 lds\n"
    "v_mfma_f32_16x16x32_bf16 a[44:47], v[172:175], v[212:215], a[44:47]\n"
    "ds_read_b64_tr_b16 v[144:145], v236 offset:33792\n"
    "ds_read_b64_tr_b16 v[146:147], v237 offset:33792\n"
    "v_mfma_f32_16x16x32_bf16 a[76:79], v[172:175], v[216:219], a[76:79]\n"
    "v_mfma_f32_16x16x32_bf16 a[108:111], v[172:175], v[220:223], a[108:111]\n"
    "ds_read_b64_tr_b16 v[148:149], v236 offset:33800\n"
    "ds_read_b64_tr_b16 v[150:151], v237 offset:33800\n"
    "v_mfma_f32_16x16x32_bf16 a[16:19], v[176:179], v[208:211], a[16:19]\n"
    "buffer_load_dwordx4 v243, s[48:51], s76 offen offset:3072 lds\n"
    "v_mfma_f32_16x16x32_bf16 a[48:51], v[176:179], v[212:215], a[48:51]\n"
    "ds_read_b64_tr_b16 v[152:153], v236 offset:34304\n"
    "ds_read_b64_tr_b16 v[154:155], v237 offset:34304\n"
    "v_mfma_f32_16x16x32_bf16 a[80:83], v[176:179], v[216:219], a[80:83]\n"
    "v_mfma_f32_16x16x32_bf16 a[112:115], v[176:179], v[220:223], a[112:115]\n"
    "ds_read_b64_tr_b16 v[156:157], v236 offset:34312\n"
    "ds_read_b64_tr_b16 v[158:159], v237 offset:34312\n"
    "v_mfma_f32_16x16x32_bf16 a[20:23], v[180:183], v[208:211], a[20:23]\n"
    "s_add_u32 m0, s72, 16384\n"
    "s_add_u32 s76, s80, 256\n"
    "v_mfma_f32_16x16x32_bf16 a[52:55], v[180:183], v[212:215], a[52:55]\n"
    "v_mfma_f32_16x16x32_bf16 a[84:87], v[180:183], v[216:219], a[84:87]\n"
    "buffer_load_dwordx4 v240, s[48:51], s76 offen offset:0 lds\n"
    "v_mfma_f32_16x16x32_bf16 a[116:119], v[180:183], v[220:223], a[116:119]\n"
    "v_mfma_f32_16x16x32_bf16 a[24:27], v[184:187], v[208:211], a[24:27]\n"
    "buffer_load_dwordx4 v241, s[48:51], s76 offen offset:1024 lds\n"
    "v_mfma_f32_16x16x32_bf16 a[56:59], v[184:187], v[212:215], a[56:59]\n"
    "v_mfma_f32_16x16x32_bf16 a[88:91], v[184:187], v[216:219], a[88:91]\n"
    "buffer_load_dwordx4 v242, s[48:51], s76 offen offset:2048 lds\n"
    "v_mfma_f32_16x16x32_bf16 a[120:123], v[184:187], v[220:223], a[120:123]\n"
    "v_mfma_f32_16x16x32_bf16 a[28:31], v[188:191], v[208:211], a[28:31]\n"
    "buffer_load_dwordx4 v243, s[48:51], s76 offen offset:3072 lds\n"
    "v_mfma_f32_16x16x32_bf16 a[60:63], v[188:191], v[212:215], a[60:63]\n"
    "v_mfma_f32_16x16x32_bf16 a[92:95], v[188:191], v[216:219], a[92:95]\n"
    "v_mfma_f32_16x16x32_bf16 a[124:127], v[188:191], v[220:223], a[124:127]\n"
    "s_waitcnt lgkmcnt(0)\n"
    "s_mov_b32 s74, 2\n"
    "QLP%=:\n"
    "v_mfma_f32_16x16x32_bf16 v[0:3], v[128:131], v[192:195], v[0:3]\n"
    "s_add_u32 m0, s81, 0\n"
    "s_add_u32 s76, s79, 0\n"
    "v_mfma_f32_16x16x32_bf16 v[32:35], v[128:131], v[196:199], v[32:35]\n"
    "ds_read_b64_tr_b16 v[160:161], v236 offset:49152\n"
    "ds_read_b64_tr_b16 v[162:163], v237 offset:49152\n"
    "v_mfma_f32_16x16x32_bf16 v[64:67], v[128:131], v[200:203], v[64:67]\n"
    "buffer_load_dwordx4 v244, s[44:47], s76 offen lds\n"
    "v_mfma_f32_16x16x32_bf16 v[96:99], v[128:131], v[204:207], v[96:99]\n"
    "ds_read_b64_tr_b16 v[164:165], v236 offset:49160\n"
    "ds_read_b64_tr_b16 v[166:167], v237 offset:49160\n"
    "v_mfma_f32_16x16x32_bf16 v[4:7], v[132:135], v[192:195], v[4:7]\n"
    "v_mfma_f32_16x16x32_bf16 v[36:39], v[132:135], v[196:199], v[36:39]\n"
    "ds_read_b64_tr_b16 v[168:169], v236 offset:49664\n"
    "ds_read_b64_tr_b16 v[170:171], v237 offset:49664\n"
    "v_mfma_f32_16x16x32_bf16 v[68:71], v[132:135], v[200:203], v[68:71]\n"
    "s_add_u32 m0, s81, 1024\n"
    "s_add_u32 s76, s79, 10240\n"
    "v_mfma_f32_16x16x32_bf16 v[100:103], v[132:135], v[204:207], v[100:103]\n"
    "ds_read_b64_tr_b16 v[172:173], v236 offset:49672\n"
    "ds_read_b64_tr_b16 v[174:175], v237 offset:49672\n"
    "v_mfma_f32_16x16x32_bf16 v[8:11], v[136:139], v[192:195], v[8:11]\n"
    "buffer_load_dwordx4 v245, s[44:47], s76 offen lds\n"
    "v_mfma_f32_16x16x32_bf16 v[40:43], v[136:139], v[196:199], v[40:43]\n"
    "ds_read_b64_tr_b16 v[176:177], v236 offset:50176\n"
    "ds_read_b64_tr_b16 v[178:179], v237 offset:50176\n"
    "v_mfma_f32_16x16x32_bf16 v[72:75], v[136:139], v[200:203], v[72:75]\n"
    "v_mfma_f32_16x16x32_bf16 v[104:107], v[136:139], v[204:207], v[104:107]\n"
    "ds_read_b64_tr_b16 v[180:181], v236 offset:50184\n"
    "ds_read_b64_tr_b16 v[182:183], v237 offset:50184\n"
    "v_mfma_f32_16x16x32_bf16 v[12:15], v[140:143], v[192:195], v[12:15]\n"
    "s_add_u32 m0, s81, 2048\n"
    "s_add_u32 s76, s79, 20480\n"
    "v_mfma_f32_16x16x32_bf16 v[44:47], v[140:143], v[196:199], v[44:47]\n"
    "ds_read_b64_tr_b16 v[184:185], v236 offset:50688\n"
    "ds_read_b64_tr_b16 v[186:187], v237 offset:50688\n"
    "v_mfma_f32_16x16x32_bf16 v[76:79], v[140:143], v[200:203], v[76:79]\n"
    "v_mfma_f32_16x16x32_bf16 v[108:111], v[140:143], v[204:207], v[108:111]\n"
    "ds_read_b64_tr_b16 v[188:189], v236 offset:50696\n"
    "ds_read_b64_tr_b16 v[190:191], v237 offset:50696\n"
    "v_mfma_f32_16x16x32_bf16 v[16:19], v[144:147], v[192:195], v[16:19]\n"
    "buffer_load_dwordx4 v244, s[44:47], s76 offen lds\n"
    "v_mfma_f32_16x16x32_bf16 v[48:51], v[144:147], v[196:199], v[48:51]\n"
    "s_add_u32 m0, s81, 3072\n"
    "s_add_u32 s76, s79, 30720\n"
    "v_mfma_f32_16x16x32_bf16 v[80:83], v[144:147], v[200:203], v[80:83]\n"
    "v_mfma_f32_16x16x32_bf16 v[112:115], v[144:147], v[204:207], v[112:115]\n"
    "buffer_load_dwordx4 v245, s[44:47], s76 offen lds\n"
    "v_mfma_f32_16x16x32_bf16 v[20:23], v[148:151], v[192:195], v[20:23]\n"
    "s_add_u32 m0, s81, 4096\n"
    "s_add_u32 s76, s79, 40960\n"
    "v_mfma_f32_16x16x32_bf16 v[52:55], v[148:151], v[196:199], v[52:55]\n"
    "v_mfma_f32_16x16x32_bf16 v[84:87], v[148:151], v[200:203], v[84:87]\n"
    "buffer_load_dwordx4 v244, s[44:47], s76 offen lds\n"
    "v_mfma_f32_16x16x32_bf16 v[116:119], v[148:151], v[204:207], v[116:119]\n"
    "v_mfma_f32_16x16x32_bf16 v[24:27], v[152:155], v[192:195], v[24:27]\n"
    "s_add_u32 m0, s81, 5120\n"
    "s_add_u32 s76, s79, 51200\n"
    "v_mfma_f32_16x16x32_bf16 v[56:59], v[152:155], v[196:199], v[56:59]\n"
    "buffer_load_dwordx4 v245, s[44:47], s76 offen lds\n"
    "v_mfma_f32_16x16x32_bf16 v[88:91], v[152:155], v[200:203], v[88:91]\n"
    "v_mfma_f32_16x16x32_bf16 v[120:123], v[152:155], v[204:207], v[120:123]\n"
    "s_add_u32 m0, s81, 6144\n"
    "s_add_u32 s76, s79, 61440\n"
    "v_mfma_f32_16x16x32_bf16 v[28:31], v[156:159], v[192:195], v[28:31]\n"
    "v_mfma_f32_16x16x32_bf16 v[60:63], v[156:159], v[196:199], v[60:63]\n"
    "buffer_load_dwordx4 v244, s[44:47], s76 offen lds\n"
    "v_mfma_f32_16x16x32_bf16 v[92:95], v[156:159], v[200:203], v[92:95]\n"
    "s_add_u32 m0, s81, 7168\n"
    "s_add_u32 s76, s79, 71680\n"
    "v_mfma_f32_16x16x32_bf16 v[124:127], v[156:159], v[204:207], v[124:127]\n"
    "s_waitcnt lgkmcnt(0)\n"
    "v_mfma_f32_16x16x32_bf16 a[0:3], v[160:163], v[192:195], a[0:3]\n"
    "buffer_load_dwordx4 v245, s[44:47], s76 offen lds\n"
    "v_mfma_f32_16x16x32_bf16 a[32:35], v[160:163], v[196:199], a[32:35]\n"
    "ds_read_b128 v[208:211], v238 offset:33280\n"
    "ds_read_b128 v[212:215], v238 offset:35328\n"
    "v_mfma_f32_16x16x32_bf16 a[64:67], v[160:163], v[200:203], a[64:67]\n"
    "v_mfma_f32_16x16x32_bf16 a[96:99], v[160:163], v[204:207], a[96:99]\n"
    "ds_read_b128 v[216:219], v238 offset:37376\n"
    "ds_read_b128 v[220:223], v238 offset:39424\n"
    "v_mfma_f32_16x16x32_bf16 a[4:7], v[164:167], v[192:195], a[4:7]\n"
    "s_add_u32 s80, s80, 524288\n"
    "s_add_u32 s79, s79, 128\n"
    "v_mfma_f32_16x16x32_bf16 a[36:39], v[164:167], v[196:199], a[36:39]\n"
    "ds_read_b64_tr_b16 v[128:129], v236 offset:40960\n"
    "ds_read_b64_tr_b16 v[130:131], v237 offset:40960\n"
    "v_mfma_f32_16x16x32_bf16 a[68:71], v[164:167], v[200:203], a[68:71]\n"
    "v_mfma_f32_16x16x32_bf16 a[100:103], v[164:167], v[204:207], a[100:103]\n"
    "ds_read_b64_tr_b16 v[132:133], v236 offset:40968\n"
    "ds_read_b64_tr_b16 v[134:135], v237 offset:40968\n"
    "v_mfma_f32_16x16x32_bf16 a[8:11], v[168:171], v[192:195], a[8:11]\n"
    "v_mfma_f32_16x16x32_bf16 a[40:43], v[168:171], v[196:199], a[40:43]\n"
    "ds_read_b64_tr_b16 v[136:137], v236 offset:41472\n"
    "ds_read_b64_tr_b16 v[138:139], v237 offset:41472\n"
    "v_mfma_f32_16x16x32_bf16 a[72:75], v[168:171], v[200:203], a[72:75]\n"
    "buffer_load_dwordx4 a[252:255], v248, s[44:47], s86 offen\n"
    "v_mfma_f32_16x16x32_bf16 a[104:107], v[168:171], v[204:207], a[104:107]\n"
    "ds_read_b64_tr_b16 v[140:141], v236 offset:41480\n"
    "ds_read_b64_tr_b16 v[142:143], v237 offset:41480\n"
    "v_mfma_f32_16x16x32_bf16 a[12:15], v[172:175], v[192:195], a[12:15]\n"
    "v_mfma_f32_16x16x32_bf16 a[44:47], v[172:175], v[196:199], a[44:47]\n"
    "ds_read_b64_tr_b16 v[144:145], v236 offset:41984\n"
    "ds_read_b64_tr_b16 v[146:147], v237 offset:41984\n"
    "v_mfma_f32_16x16x32_bf16 a[76:79], v[172:175], v[200:203], a[76:79]\n"
    "v_mfma_f32_16x16x32_bf16 a[108:111], v[172:175], v[204:207], a[108:111]\n"
    "ds_read_b64_tr_b16 v[148:149], v236 offset:41992\n"
    "ds_read_b64_tr_b16 v[150:151], v237 offset:41992\n"
    "v_mfma_f32_16x16x32_bf16 a[16:19], v[176:179], v[192:195], a[16:19]\n"
    "v_mfma_f32_16x16x32_bf16 a[48:51], v[176:179], v[196:199], a[48:51]\n"
    "ds_read_b64_tr_b16 v[152:153], v236 offset:42496\n"
    "ds_read_b64_tr_b16 v[154:155], v237 offset:42496\n"
    "v_mfma_f32_16x16x32_bf16 a[80:83], v[176:179], v[200:203], a[80:83]\n"
    "v_mfma_f32_16x16x32_bf16 a[112:115], v[176:179], v[204:207], a[112:115]\n"
    "ds_read_b64_tr_b16 v[156:157], v236 offset:42504\n"
    "ds_read_b64_tr_b16 v[158:159], v237 offset:42504\n"
    "v_mfma_f32_16x16x32_bf16 a[20:23], v[180:183], v[192:195], a[20:23]\n"
    "v_mfma_f32_16x16x32_bf16 a[52:55], v[180:183], v[196:199], a[52:55]\n"
    "v_mfma_f32_16x16x32_bf16 a[84:87], v[180:183], v[200:203], a[84:87]\n"
    "v_mfma_f32_16x16x32_bf16 a[116:119], v[180:183], v[204:207], a[116:119]\n"
    "buffer_load_dwordx4 a[252:255], v249, s[48:51], s87 offen offset:1024\n"
    "v_mfma_f32_16x16x32_bf16 a[24:27], v[184:187], v[192:195], a[24:27]\n"
    "v_mfma_f32_16x16x32_bf16 a[56:59], v[184:187], v[196:199], a[56:59]\n"
    "v_mfma_f32_16x16x32_bf16 a[88:91], v[184:187], v[200:203], a[88:91]\n"
    "v_mfma_f32_16x16x32_bf16 a[120:123], v[184:187], v[204:207], a[120:123]\n"
    "v_mfma_f32_16x16x32_bf16 a[28:31], v[188:191], v[192:195], a[28:31]\n"
    "v_mfma_f32_16x16x32_bf16 a[60:63], v[188:191], v[196:199], a[60:63]\n"
    "v_mfma_f32_16x16x32_bf16 a[92:95], v[188:191], v[200:203], a[92:95]\n"
    "v_mfma_f32_16x16x32_bf16 a[124:127], v[188:191], v[204:207], a[124:127]\n"
    "s_waitcnt lgkmcnt(0)\n"
    "v_mfma_f32_16x16x32_bf16 v[0:3], v[128:131], v[208:211], v[0:3]\n"
    "s_add_u32 s87, s87, 16384\n"
    "s_add_u32 s86, s86, 128\n"
    "v_mfma_f32_16x16x32_bf16 v[32:35], v[128:131], v[212:215], v[32:35]\n"
    "ds_read_b64_tr_b16 v[160:161], v236 offset:57344\n"
    "ds_read_b64_tr_b16 v[162:163], v237 offset:57344\n"
    "v_mfma_f32_16x16x32_bf16 v[64:67], v[128:131], v[216:219], v[64:67]\n"
    "v_mfma_f32_16x16x32_bf16 v[96:99], v[128:131], v[220:223], v[96:99]\n"
    "ds_read_b64_tr_b16 v[164:165], v236 offset:57352\n"
    "ds_read_b64_tr_b16 v[166:167], v237 offset:57352\n"
    "v_mfma_f32_16x16x32_bf16 v[4:7], v[132:135], v[208:211], v[4:7]\n"
    "v_mfma_f32_16x16x32_bf16 v[36:39], v[132:135], v[212:215], v[36:39]\n"
    "ds_read_b64_tr_b16 v[168:169], v236 offset:57856\n"
    "ds_read_b64_tr_b16 v[170:171], v237 offset:57856\n"
    "v_mfma_f32_16x16x32_bf16 v[68:71], v[132:135], v[216:219], v[68:71]\n"
    "v_mfma_f32_16x16x32_bf16 v[100:103], v[132:135], v[220:223], v[100:103]\n"
    "ds_read_b64_tr_b16 v[172:173], v236 offset:57864\n"
    "ds_read_b64_tr_b16 v[174:175], v237 offset:57864\n"
    "v_mfma_f32_16x16x32_bf16 v[8:11], v[136:139], v[208:211], v[8:11]\n"
    "v_mfma_f32_16x16x32_bf16 v[40:43], v[136:139], v[212:215], v[40:43]\n"
    "ds_read_b64_tr_b16 v[176:177], v236 offset:58368\n"
    "ds_read_b64_tr_b16 v[178:179], v237 offset:58368\n"
    "v_mfma_f32_16x16x32_bf16 v[72:75], v[136:139], v[216:219], v[72:75]\n"
    "v_mfma_f32_16x16x32_bf16 v[104:107], v[136:139], v[220:223], v[104:107]\n"
    "ds_read_b64_tr_b16 v[180:181], v236 offset:58376\n"
    "ds_read_b64_tr_b16 v[182:183], v237 offset:58376\n"
    "v_mfma_f32_16x16x32_bf16 v[12:15], v[140:143], v[208:211], v[12:15]\n"
    "v_mfma_f32_16x16x32_bf16 v[44:47], v[140:143], v[212:215], v[44:47]\n"
    "ds_read_b64_tr_b16 v[184:185], v236 offset:58880\n"
    "ds_read_b64_tr_b16 v[186:187], v237 offset:58880\n"
    "v_mfma_f32_16x16x32_bf16 v[76:79], v[140:143], v[216:219], v[76:79]\n"
    "v_mfma_f32_16x16x32_bf16 v[108:111], v[140:143], v[220:223], v[108:111]\n"
    "ds_read_b64_tr_b16 v[188:189], v236 offset:58888\n"
    "ds_read_b64_tr_b16 v[190:191], v237 offset:58888\n"
    "v_mfma_f32_16x16x32_bf16 v[16:19], v[144:147], v[208:211], v[16:19]\n"
    "v_mfma_f32_16x16x32_bf16 v[48:51], v[144:147], v[212:215], v[48:51]\n"
    "buffer_load_dwordx4 a[252:255], v249, s[48:51], s87 offen offset:1024\n"
    "v_mfma_f32_16x16x32_bf16 v[80:83], v[144:147], v[216:219], v[80:83]\n"
    "v_mfma_f32_16x16x32_bf16 v[112:115], v[144:147], v[220:223], v[112:115]\n"
    "v_mfma_f32_16x16x32_bf16 v[20:23], v[148:151], v[208:211], v[20:23]\n"
    "v_mfma_f32_16x16x32_bf16 v[52:55], v[148:151], v[212:215], v[52:55]\n"
    "v_mfma_f32_16x16x32_bf16 v[84:87], v[148:151], v[216:219], v[84:87]\n"
    "v_mfma_f32_16x16x32_bf16 v[116:119], v[148:151], v[220:223], v[116:119]\n"
    "v_mfma_f32_16x16x32_bf16 v[24:27], v[152:155], v[208:211], v[24:27]\n"
    "v_mfma_f32_16x16x32_bf16 v[56:59], v[152:155], v[212:215], v[56:59]\n"
    "s_add_u32 s87, s87, 507904\n"
    "v_mfma_f32_16x16x32_bf16 v[88:91], v[152:155], v[216:219], v[88:91]\n"
    "v_mfma_f32_16x16x32_bf16 v[120:123], v[152:155], v[220:223], v[120:123]\n"
    "v_mfma_f32_16x16x32_bf16 v[28:31], v[156:159], v[208:211], v[28:31]\n"
    "v_mfma_f32_16x16x32_bf16 v[60:63], v[156:159], v[212:215], v[60:63]\n"
    "v_mfma_f32_16x16x32_bf16 v[92:95], v[156:159], v[216:219], v[92:95]\n"
    "v_mfma_f32_16x16x32_bf16 v[124:127], v[156:159], v[220:223], v[124:127]\n"
    "s_waitcnt lgkmcnt(0)\n"
    "v_mfma_f32_16x16x32_bf16 a[0:3], v[160:163], v[208:211], a[0:3]\n"
    "s_waitcnt vmcnt(3)\n"
    "s_barrier\n"
    "v_mfma_f32_16x16x32_bf16 a[32:35], v[160:163], v[212:215], a[32:35]\n"
    "ds_read_b128 v[192:195], v238 offset:0\n"
    "ds_read_b128 v[196:199], v238 offset:2048\n"
    "v_mfma_f32_16x16x32_bf16 a[64:67], v[160:163], v[216:219], a[64:67]\n"
    "s_add_u32 m0, s72, 32768\n"
    "s_add_u32 s76, s80, 0\n"
    "v_mfma_f32_16x16x32_bf16 a[96:99], v[160:163], v[220:223], a[96:99]\n"
    "ds_read_b128 v[200:203], v238 offset:4096\n"
    "ds_read_b128 v[204:207], v238 offset:6144\n"
    "v_mfma_f32_16x16x32_bf16 a[4:7], v[164:167], v[208:211], a[4:7]\n"
    "buffer_load_dwordx4 v240, s[48:51], s76 offen offset:0 lds\n"
    "v_mfma_f32_16x16x32_bf16 a[36:39], v[164:167], v[212:215], a[36:39]\n"
    "ds_read_b64_tr_b16 v[128:129], v236 offset:0\n"
    "ds_read_b64_tr_b16 v[130:131], v237 offset:0\n"
    "v_mfma_f32_16x16x32_bf16 a[68:71], v[164:167], v[216:219], a[68:71]\n"
    "v_mfma_f32_16x16x32_bf16 a[100:103], v[164:167], v[220:223], a[100:103]\n"
    "ds_read_b64_tr_b16 v[132:133], v236 offset:8\n"
    "ds_read_b64_tr_b16 v[134:135], v237 offset:8\n"
    "v_mfma_f32_16x16x32_bf16 a[8:11], v[168:171], v[208:211], a[8:11]\n"
    "buffer_load_dwordx4 v241, s[48:51], s76 offen offset:1024 lds\n"
    "v_mfma_f32_16x16x32_bf16 a[40:43], v[168:171], v[212:215], a[40:43]\n"
    "ds_read_b64_tr_b16 v[136:137], v236 offset:512\n"
    "ds_read_b64_tr_b16 v[138:139], v237 offset:512\n"
    "v_mfma_f32_16x16x32_bf16 a[72:75], v[168:171], v[216:219], a[72:75]\n"
    "v_mfma_f32_16x16x32_bf16 a[104:107], v[168:171], v[220:223], a[104:107]\n"
    "ds_read_b64_tr_b16 v[140:141], v236 offset:520\n"
    "ds_read_b64_tr_b16 v[142:143], v237 offset:520\n"
    "v_mfma_f32_16x16x32_bf16 a[12:15], v[172:175], v[208:211], a[12:15]\n"
    "buffer_load_dwordx4 v242, s[48:51], s76 offen offset:2048 lds\n"
    "v_mfma_f32_16x16x32_bf16 a[44:47], v[172:175], v[212:215], a[44:47]\n"
    "ds_read_b64_tr_b16 v[144:145], v236 offset:1024\n"
    "ds_read_b64_tr_b16 v[146:147], v237 offset:1024\n"
    "v_mfma_f32_16x16x32_bf16 a[76:79], v[172:175], v[216:219], a[76:79]\n"
    "v_mfma_f32_16x16x32_bf16 a[108:111], v[172:175], v[220:223], a[108:111]\n"
    "ds_read_b64_tr_b16 v[148:149], v236 offset:1032\n"
    "ds_read_b64_tr_b16 v[150:151], v237 offset:1032\n"
    "v_mfma_f32_16x16x32_bf16 a[16:19], v[176:179], v[208:211], a[16:19]\n"
    "buffer_load_dwordx4 v243, s[48:51], s76 offen offset:3072 lds\n"
    "v_mfma_f32_16x16x32_bf16 a[48:51], v[176:179], v[212:215], a[48:51]\n"
    "ds_read_b64_tr_b16 v[152:153], v236 offset:1536\n"
    "ds_read_b64_tr_b16 v[154:155], v237 offset:1536\n"
    "v_mfma_f32_16x16x32_bf16 a[80:83], v[176:179], v[216:219], a[80:83]\n"
    "v_mfma_f32_16x16x32_bf16 a[112:115], v[176:179], v[220:223], a[112:115]\n"
    "ds_read_b64_tr_b16 v[156:157], v236 offset:1544\n"
    "ds_read_b64_tr_b16 v[158:159], v237 offset:1544\n"
    "v_mfma_f32_16x16x32_bf16 a[20:23], v[180:183], v[208:211], a[20:23]\n"
    "s_add_u32 m0, s72, 49152\n"
    "s_add_u32 s76, s80, 256\n"
    "v_mfma_f32_16x16x32_bf16 a[52:55], v[180:183], v[212:215], a[52:55]\n"
    "v_mfma_f32_16x16x32_bf16 a[84:87], v[180:183], v[216:219], a[84:87]\n"
    "buffer_load_dwordx4 v240, s[48:51], s76 offen offset:0 lds\n"
    "v_mfma_f32_16x16x32_bf16 a[116:119], v[180:183], v[220:223], a[116:119]\n"
    "v_mfma_f32_16x16x32_bf16 a[24:27], v[184:187], v[208:211], a[24:27]\n"
    "buffer_load_dwordx4 v241, s[48:51], s76 offen offset:1024 lds\n"
    "v_mfma_f32_16x16x32_bf16 a[56:59], v[184:187], v[212:215], a[56:59]\n"
    "v_mfma_f32_16x16x32_bf16 a[88:91], v[184:187], v[216:219], a[88:91]\n"
    "buffer_load_dwordx4 v242, s[48:51], s76 offen offset:2048 lds\n"
    "v_mfma_f32_16x16x32_bf16 a[120:123], v[184:187], v[220:223], a[120:123]\n"
    "v_mfma_f32_16x16x32_bf16 a[28:31], v[188:191], v[208:211], a[28:31]\n"
    "buffer_load_dwordx4 v243, s[48:51], s76 offen offset:3072 lds\n"
    "v_mfma_f32_16x16x32_bf16 a[60:63], v[188:191], v[212:215], a[60:63]\n"
    "v_mfma_f32_16x16x32_bf16 a[92:95], v[188:191], v[216:219], a[92:95]\n"
    "v_mfma_f32_16x16x32_bf16 a[124:127], v[188:191], v[220:223], a[124:127]\n"
    "s_waitcnt lgkmcnt(0)\n"
    "v_mfma_f32_16x16x32_bf16 v[0:3], v[128:131], v[192:195], v[0:3]\n"
    "s_add_u32 m0, s81, 32768\n"
    "s_add_u32 s76, s79, 0\n"
    "v_mfma_f32_16x16x32_bf16 v[32:35], v[128:131], v[196:199], v[32:35]\n"
    "ds_read_b64_tr_b16 v[160:161], v236 offset:16384\n"
    "ds_read_b64_tr_b16 v[162:163], v237 offset:16384\n"
    "v_mfma_f32_16x16x32_bf16 v[64:67], v[128:131], v[200:203], v[64:67]\n"
    "buffer_load_dwordx4 v244, s[44:47], s76 offen lds\n"
    "v_mfma_f32_16x16x32_bf16 v[96:99], v[128:131], v[204:207], v[96:99]\n"
    "ds_read_b64_tr_b16 v[164:165], v236 offset:16392\n"
    "ds_read_b64_tr_b16 v[166:167], v237 offset:16392\n"
    "v_mfma_f32_16x16x32_bf16 v[4:7], v[132:135], v[192:195], v[4:7]\n"
    "v_mfma_f32_16x16x32_bf16 v[36:39], v[132:135], v[196:199], v[36:39]\n"
    "ds_read_b64_tr_b16 v[168:169], v236 offset:16896\n"
    "ds_read_b64_tr_b16 v[170:171], v237 offset:16896\n"
    "v_mfma_f32_16x16x32_bf16 v[68:71], v[132:135], v[200:203], v[68:71]\n"
    "s_add_u32 m0, s81, 33792\n"
    "s_add_u32 s76, s79, 10240\n"
    "v_mfma_f32_16x16x32_bf16 v[100:103], v[132:135], v[204:207], v[100:103]\n"
    "ds_read_b64_tr_b16 v[172:173], v236 offset:16904\n"
    "ds_read_b64_tr_b16 v[174:175], v237 offset:16904\n"
    "v_mfma_f32_16x16x32_bf16 v[8:11], v[136:139], v[192:195], v[8:11]\n"
    "buffer_load_dwordx4 v245, s[44:47], s76 offen lds\n"
    "v_mfma_f32_16x16x32_bf16 v[40:43], v[136:139], v[196:199], v[40:43]\n"
    "ds_read_b64_tr_b16 v[176:177], v236 offset:17408\n"
    "ds_read_b64_tr_b16 v[178:179], v237 offset:17408\n"
    "v_mfma_f32_16x16x32_bf16 v[72:75], v[136:139], v[200:203], v[72:75]\n"
    "v_mfma_f32_16x16x32_bf16 v[104:107], v[136:139], v[204:207], v[104:107]\n"
    "ds_read_b64_tr_b16 v[180:181], v236 offset:17416\n"
    "ds_read_b64_tr_b16 v[182:183], v237 offset:17416\n"
    "v_mfma_f32_16x16x32_bf16 v[12:15], v[140:143], v[192:195], v[12:15]\n"
    "s_add_u32 m0, s81, 34816\n"
    "s_add_u32 s76, s79, 20480\n"
    "v_mfma_f32_16x16x32_bf16 v[44:47], v[140:143], v[196:199], v[44:47]\n"
    "ds_read_b64_tr_b16 v[184:185], v236 offset:17920\n"
    "ds_read_b64_tr_b16 v[186:187], v237 offset:17920\n"
    "v_mfma_f32_16x16x32_bf16 v[76:79], v[140:143], v[200:203], v[76:79]\n"
    "v_mfma_f32_16x16x32_bf16 v[108:111], v[140:143], v[204:207], v[108:111]\n"
    "ds_read_b64_tr_b16 v[188:189], v236 offset:17928\n"
    "ds_read_b64_tr_b16 v[190:191], v237 offset:17928\n"
    "v_mfma_f32_16x16x32_bf16 v[16:19], v[144:147], v[192:195], v[16:19]\n"
    "buffer_load_dwordx4 v244, s[44:47], s76 offen lds\n"
    "v_mfma_f32_16x16x32_bf16 v[48:51], v[144:147], v[196:199], v[48:51]\n"
    "s_add_u32 m0, s81, 35840\n"
    "s_add_u32 s76, s79, 30720\n"
    "v_mfma_f32_16x16x32_bf16 v[80:83], v[144:147], v[200:203], v[80:83]\n"
    "v_mfma_f32_16x16x32_bf16 v[112:115], v[144:147], v[204:207], v[112:115]\n"
    "buffer_load_dwordx4 v245, s[44:47], s76 offen lds\n"
    "v_mfma_f32_16x16x32_bf16 v[20:23], v[148:151], v[192:195], v[20:23]\n"
    "s_add_u32 m0, s81, 36864\n"
    "s_add_u32 s76, s79, 40960\n"
    "v_mfma_f32_16x16x32_bf16 v[52:55], v[148:151], v[196:199], v[52:55]\n"
    "v_mfma_f32_16x16x32_bf16 v[84:87], v[148:151], v[200:203], v[84:87]\n"
    "buffer_load_dwordx4 v244, s[44:47], s76 offen lds\n"
    "v_mfma_f32_16x16x32_bf16 v[116:119], v[148:151], v[204:207], v[116:119]\n"
    "v_mfma_f32_16x16x32_bf16 v[24:27], v[152:155], v[192:195], v[24:27]\n"
    "s_add_u32 m0, s81, 37888\n"
    "s_add_u32 s76, s79, 51200\n"
    "v_mfma_f32_16x16x32_bf16 v[56:59], v[152:155], v[196:199], v[56:59]\n"
    "buffer_load_dwordx4 v245, s[44:47], s76 offen lds\n"
    "v_mfma_f32_16x16x32_bf16 v[88:91], v[152:155], v[200:203], v[88:91]\n"
    "v_mfma_f32_16x16x32_bf16 v[120:123], v[152:155], v[204:207], v[120:123]\n"
    "s_add_u32 m0, s81, 38912\n"
    "s_add_u32 s76, s79, 61440\n"
    "v_mfma_f32_16x16x32_bf16 v[28:31], v[156:159], v[192:195], v[28:31]\n"
    "v_mfma_f32_16x16x32_bf16 v[60:63], v[156:159], v[196:199], v[60:63]\n"
    "buffer_load_dwordx4 v244, s[44:47], s76 offen lds\n"
    "v_mfma_f32_16x16x32_bf16 v[92:95], v[156:159], v[200:203], v[92:95]\n"
    "s_add_u32 m0, s81, 39936\n"
    "s_add_u32 s76, s79, 71680\n"
    "v_mfma_f32_16x16x32_bf16 v[124:127], v[156:159], v[204:207], v[124:127]\n"
    "s_waitcnt lgkmcnt(0)\n"
    "v_mfma_f32_16x16x32_bf16 a[0:3], v[160:163], v[192:195], a[0:3]\n"
    "buffer_load_dwordx4 v245, s[44:47], s76 offen lds\n"
    "v_mfma_f32_16x16x32_bf16 a[32:35], v[160:163], v[196:199], a[32:35]\n"
    "ds_read_b128 v[208:211], v238 offset:512\n"
    "ds_read_b128 v[212:215], v238 offset:2560\n"
    "v_mfma_f32_16x16x32_bf16 a[64:67], v[160:163], v[200:203], a[64:67]\n"
    "v_mfma_f32_16x16x32_bf16 a[96:99], v[160:163], v[204:207], a[96:99]\n"
    "ds_read_b128 v[216:219], v238 offset:4608\n"
    "ds_read_b128 v[220:223], v238 offset:6656\n"
    "v_mfma_f32_16x16x32_bf16 a[4:7], v[164:167], v[192:195], a[4:7]\n"
    "s_add_u32 s80, s80, 524288\n"
    "s_add_u32 s79, s79, 128\n"
    "v_mfma_f32_16x16x32_bf16 a[36:39], v[164:167], v[196:199], a[36:39]\n"
    "ds_read_b64_tr_b16 v[128:129], v236 offset:8192\n"
    "ds_read_b64_tr_b16 v[130:131], v237 offset:8192\n"
    "v_mfma_f32_16x16x32_bf16 a[68:71], v[164:167], v[200:203], a[68:71]\n"
    "v_mfma_f32_16x16x32_bf16 a[100:103], v[164:167], v[204:207], a[100:103]\n"
    "ds_read_b64_tr_b16 v[132:133], v236 offset:8200\n"
    "ds_read_b64_tr_b16 v[134:135], v237 offset:8200\n"
    "v_mfma_f32_16x16x32_bf16 a[8:11], v[168:171], v[192:195], a[8:11]\n"
    "v_mfma_f32_16x16x32_bf16 a[40:43], v[168:171], v[196:199], a[40:43]\n"
    "ds_read_b64_tr_b16 v[136:137], v236 offset:8704\n"
    "ds_read_b64_tr_b16 v[138:139], v237 offset:8704\n"
    "v_mfma_f32_16x16x32_bf16 a[72:75], v[168:171], v[200:203], a[72:75]\n"
    "v_mfma_f32_16x16x32_bf16 a[104:107], v[168:171], v[204:207], a[104:107]\n"
    "ds_read_b64_tr_b16 v[140:141], v236 offset:8712\n"
    "ds_read_b64_tr_b16 v[142:143], v237 offset:8712\n"
    "v_mfma_f32_16x16x32_bf16 a[12:15], v[172:175], v[192:195], a[12:15]\n"
    "v_mfma_f32_16x16x32_bf16 a[44:47], v[172:175], v[196:199], a[44:47]\n"
    "ds_read_b64_tr_b16 v[144:145], v236 offset:9216\n"
    "ds_read_b64_tr_b16 v[146:147], v237 offset:9216\n"
    "v_mfma_f32_16x16x32_bf16 a[76:79], v[172:175], v[200:203], a[76:79]\n"
    "v_mfma_f32_16x16x32_bf16 a[108:111], v[172:175], v[204:207], a[108:111]\n"
    "ds_read_b64_tr_b16 v[148:149], v236 offset:9224\n"
    "ds_read_b64_tr_b16 v[150:151], v237 offset:9224\n"
    "v_mfma_f32_16x16x32_bf16 a[16:19], v[176:179], v[192:195], a[16:19]\n"
    "v_mfma_f32_16x16x32_bf16 a[48:51], v[176:179], v[196:199], a[48:51]\n"
    "ds_read_b64_tr_b16 v[152:153], v236 offset:9728\n"
    "ds_read_b64_tr_b16 v[154:155], v237 offset:9728\n"
    "v_mfma_f32_16x16x32_bf16 a[80:83], v[176:179], v[200:203], a[80:83]\n"
    "v_mfma_f32_16x16x32_bf16 a[112:115], v[176:179], v[204:207], a[112:115]\n"
    "ds_read_b64_tr_b16 v[156:157], v236 offset:9736\n"
    "ds_read_b64_tr_b16 v[158:159], v237 offset:9736\n"
    "v_mfma_f32_16x16x32_bf16 a[20:23], v[180:183], v[192:195], a[20:23]\n"
    "v_mfma_f32_16x16x32_bf16 a[52:55], v[180:183], v[196:199], a[52:55]\n"
    "v_mfma_f32_16x16x32_bf16 a[84:87], v[180:183], v[200:203], a[84:87]\n"
    "v_mfma_f32_16x16x32_bf16 a[116:119], v[180:183], v[204:207], a[116:119]\n"
    "v_mfma_f32_16x16x32_bf16 a[24:27], v[184:187], v[192:195], a[24:27]\n"
    "v_mfma_f32_16x16x32_bf16 a[56:59], v[184:187], v[196:199], a[56:59]\n"
    "v_mfma_f32_16x16x32_bf16 a[88:91], v[184:187], v[200:203], a[88:91]\n"
    "v_mfma_f32_16x16x32_bf16 a[120:123], v[184:187], v[204:207], a[120:123]\n"
    "v_mfma_f32_16x16x32_bf16 a[28:31], v[188:191], v[192:195], a[28:31]\n"
    "v_mfma_f32_16x16x32_bf16 a[60:63], v[188:191], v[196:199], a[60:63]\n"
    "v_mfma_f32_16x16x32_bf16 a[92:95], v[188:191], v[200:203], a[92:95]\n"
    "v_mfma_f32_16x16x32_bf16 a[124:127], v[188:191], v[204:207], a[124:127]\n"
    "s_waitcnt lgkmcnt(0)\n"
    "v_mfma_f32_16x16x32_bf16 v[0:3], v[128:131], v[208:211], v[0:3]\n"
    "v_mfma_f32_16x16x32_bf16 v[32:35], v[128:131], v[212:215], v[32:35]\n"
    "ds_read_b64_tr_b16 v[160:161], v236 offset:24576\n"
    "ds_read_b64_tr_b16 v[162:163], v237 offset:24576\n"
    "v_mfma_f32_16x16x32_bf16 v[64:67], v[128:131], v[216:219], v[64:67]\n"
    "v_mfma_f32_16x16x32_bf16 v[96:99], v[128:131], v[220:223], v[96:99]\n"
    "ds_read_b64_tr_b16 v[164:165], v236 offset:24584\n"
    "ds_read_b64_tr_b16 v[166:167], v237 offset:24584\n"
    "v_mfma_f32_16x16x32_bf16 v[4:7], v[132:135], v[208:211], v[4:7]\n"
    "v_mfma_f32_16x16x32_bf16 v[36:39], v[132:135], v[212:215], v[36:39]\n"
    "ds_read_b64_tr_b16 v[168:169], v236 offset:25088\n"
    "ds_read_b64_tr_b16 v[170:171], v237 offset:25088\n"
    "v_mfma_f32_16x16x32_bf16 v[68:71], v[132:135], v[216:219], v[68:71]\n"
    "v_mfma_f32_16x16x32_bf16 v[100:103], v[132:135], v[220:223], v[100:103]\n"
    "ds_read_b64_tr_b16 v[172:173], v236 offset:25096\n"
    "ds_read_b64_tr_b16 v[174:175], v237 offset:25096\n"
    "v_mfma_f32_16x16x32_bf16 v[8:11], v[136:139], v[208:211], v[8:11]\n"
    "v_mfma_f32_16x16x32_bf16 v[40:43], v[136:139], v[212:215], v[40:43]\n"
    "ds_read_b64_tr_b16 v[176:177], v236 offset:25600\n"
    "ds_read_b64_tr_b16 v[178:179], v237 offset:25600\n"
    "v_mfma_f32_16x16x32_bf16 v[72:75], v[136:139], v[216:219], v[72:75]\n"
    "v_mfma_f32_16x16x32_bf16 v[104:107], v[136:139], v[220:223], v[104:107]\n"
    "ds_read_b64_tr_b16 v[180:181], v236 offset:25608\n"
    "ds_read_b64_tr_b16 v[182:183], v237 offset:25608\n"
    "v_mfma_f32_16x16x32_bf16 v[12:15], v[140:143], v[208:211], v[12:15]\n"
    "v_mfma_f32_16x16x32_bf16 v[44:47], v[140:143], v[212:215], v[44:47]\n"
    "ds_read_b64_tr_b16 v[184:185], v236 offset:26112\n"
    "ds_read_b64_tr_b16 v[186:187], v237 offset:26112\n"
    "v_mfma_f32_16x16x32_bf16 v[76:79], v[140:143], v[216:219], v[76:79]\n"
    "v_mfma_f32_16x16x32_bf16 v[108:111], v[140:143], v[220:223], v[108:111]\n"
    "ds_read_b64_tr_b16 v[188:189], v236 offset:26120\n"
    "ds_read_b64_tr_b16 v[190:191], v237 offset:26120\n"
    "v_mfma_f32_16x16x32_bf16 v[16:19], v[144:147], v[208:211], v[16:19]\n"
    "v_mfma_f32_16x16x32_bf16 v[48:51], v[144:147], v[212:215], v[48:51]\n"
    "v_mfma_f32_16x16x32_bf16 v[80:83], v[144:147], v[216:219], v[80:83]\n"
    "v_mfma_f32_16x16x32_bf16 v[112:115], v[144:147], v[220:223], v[112:115]\n"
    "v_mfma_f32_16x16x32_bf16 v[20:23], v[148:151], v[208:211], v[20:23]\n"
    "v_mfma_f32_16x16x32_bf16 v[52:55], v[148:151], v[212:215], v[52:55]\n"
    "v_mfma_f32_16x16x32_bf16 v[84:87], v[148:151], v[216:219], v[84:87]\n"
    "v_mfma_f32_16x16x32_bf16 v[116:119], v[148:151], v[220:223], v[116:119]\n"
    "v_mfma_f32_16x16x32_bf16 v[24:27], v[152:155], v[208:211], v[24:27]\n"
    "v_mfma_f32_16x16x32_bf16 v[56:59], v[152:155], v[212:215], v[56:59]\n"
    "v_mfma_f32_16x16x32_bf16 v[88:91], v[152:155], v[216:219], v[88:91]\n"
    "v_mfma_f32_16x16x32_bf16 v[120:123], v[152:155], v[220:223], v[120:123]\n"
    "v_mfma_f32_16x16x32_bf16 v[28:31], v[156:159], v[208:211], v[28:31]\n"
    "v_mfma_f32_16x16x32_bf16 v[60:63], v[156:159], v[212:215], v[60:63]\n"
    "v_mfma_f32_16x16x32_bf16 v[92:95], v[156:159], v[216:219], v[92:95]\n"
    "v_mfma_f32_16x16x32_bf16 v[124:127], v[156:159], v[220:223], v[124:127]\n"
    "s_waitcnt lgkmcnt(0)\n"
    "v_mfma_f32_16x16x32_bf16 a[0:3], v[160:163], v[208:211], a[0:3]\n"
    "s_waitcnt vmcnt(0)\n"
    "s_barrier\n"
    "v_mfma_f32_16x16x32_bf16 a[32:35], v[160:163], v[212:215], a[32:35]\n"
    "ds_read_b128 v[192:195], v238 offset:32768\n"
    "ds_read_b128 v[196:199], v238 offset:34816\n"
    "v_mfma_f32_16x16x32_bf16 a[64:67], v[160:163], v[216:219], a[64:67]\n"
    "s_add_u32 m0, s72, 0\n"
    "s_add_u32 s76, s80, 0\n"
    "v_mfma_f32_16x16x32_bf16 a[96:99], v[160:163], v[220:223], a[96:99]\n"
    "ds_read_b128 v[200:203], v238 offset:36864\n"
    "ds_read_b128 v[204:207], v238 offset:38912\n"
    "v_mfma_f32_16x16x32_bf16 a[4:7], v[164:167], v[208:211], a[4:7]\n"
    "buffer_load_dwordx4 v240, s[48:51], s76 offen offset:0 lds\n"
    "v_mfma_f32_16x16x32_bf16 a[36:39], v[164:167], v[212:215], a[36:39]\n"
    "ds_read_b64_tr_b16 v[128:129], v236 offset:32768\n"
    "ds_read_b64_tr_b16 v[130:131], v237 offset:32768\n"
    "v_mfma_f32_16x16x32_bf16 a[68:71], v[164:167], v[216:219], a[68:71]\n"
    "v_mfma_f32_16x16x32_bf16 a[100:103], v[164:167], v[220:223], a[100:103]\n"
    "ds_read_b64_tr_b16 v[132:133], v236 offset:32776\n"
    "ds_read_b64_tr_b16 v[134:135], v237 offset:32776\n"
    "v_mfma_f32_16x16x32_bf16 a[8:11], v[168:171], v[208:211], a[8:11]\n"
    "buffer_load_dwordx4 v241, s[48:51], s76 offen offset:1024 lds\n"
    "v_mfma_f32_16x16x32_bf16 a[40:43], v[168:171], v[212:215], a[40:43]\n"
    "ds_read_b64_tr_b16 v[136:137], v236 offset:33280\n"
    "ds_read_b64_tr_b16 v[138:139], v237 offset:33280\n"
    "v_mfma_f32_16x16x32_bf16 a[72:75], v[168:171], v[216:219], a[72:75]\n"
    "v_mfma_f32_16x16x32_bf16 a[104:107], v[168:171], v[220:223], a[104:107]\n"
    "ds_read_b64_tr_b16 v[140:141], v236 offset:33288\n"
    "ds_read_b64_tr_b16 v[142:143], v237 offset:33288\n"
    "v_mfma_f32_16x16x32_bf16 a[12:15], v[172:175], v[208:211], a[12:15]\n"
    "buffer_load_dwordx4 v242, s[48:51], s76 offen offset:2048 lds\n"
    "v_mfma_f32_16x16x32_bf16 a[44:47], v[172:175], v[212:215], a[44:47]\n"
    "ds_read_b64_tr_b16 v[144:145], v236 offset:33792\n"
    "ds_read_b64_tr_b16 v[146:147], v237 offset:33792\n"
    "v_mfma_f32_16x16x32_bf16 a[76:79], v[172:175], v[216:219], a[76:79]\n"
    "v_mfma_f32_16x16x32_bf16 a[108:111], v[172:175], v[220:223], a[108:111]\n"
    "ds_read_b64_tr_b16 v[148:149], v236 offset:33800\n"
    "ds_read_b64_tr_b16 v[150:151], v237 offset:33800\n"
    "v_mfma_f32_16x16x32_bf16 a[16:19], v[176:179], v[208:211], a[16:19]\n"
    "buffer_load_dwordx4 v243, s[48:51], s76 offen offset:3072 lds\n"
    "v_mfma_f32_16x16x32_bf16 a[48:51], v[176:179], v[212:215], a[48:51]\n"
    "ds_read_b64_tr_b16 v[152:153], v236 offset:34304\n"
    "ds_read_b64_tr_b16 v[154:155], v237 offset:34304\n"
    "v_mfma_f32_16x16x32_bf16 a[80:83], v[176:179], v[216:219], a[80:83]\n"
    "v_mfma_f32_16x16x32_bf16 a[112:115], v[176:179], v[220:223], a[112:115]\n"
    "ds_read_b64_tr_b16 v[156:157], v236 offset:34312\n"
    "ds_read_b64_tr_b16 v[158:159], v237 offset:34312\n"
    "v_mfma_f32_16x16x32_bf16 a[20:23], v[180:183], v[208:211], a[20:23]\n"
    "s_add_u32 m0, s72, 16384\n"
    "s_add_u32 s76, s80, 256\n"
    "v_mfma_f32_16x16x32_bf16 a[52:55], v[180:183], v[212:215], a[52:55]\n"
    "v_mfma_f32_16x16x32_bf16 a[84:87], v[180:183], v[216:219], a[84:87]\n"
    "buffer_load_dwordx4 v240, s[48:51], s76 offen offset:0 lds\n"
    "v_mfma_f32_16x16x32_bf16 a[116:119], v[180:183], v[220:223], a[116:119]\n"
    "v_mfma_f32_16x16x32_bf16 a[24:27], v[184:187], v[208:211], a[24:27]\n"
    "buffer_load_dwordx4 v241, s[48:51], s76 offen offset:1024 lds\n"
    "v_mfma_f32_16x16x32_bf16 a[56:59], v[184:187], v[212:215], a[56:59]\n"
    "v_mfma_f32_16x16x32_bf16 a[88:91], v[184:187], v[216:219], a[88:91]\n"
    "buffer_load_dwordx4 v242, s[48:51], s76 offen offset:2048 lds\n"
    "v_mfma_f32_16x16x32_bf16 a[120:123], v[184:187], v[220:223], a[120:123]\n"
    "v_mfma_f32_16x16x32_bf16 a[28:31], v[188:191], v[208:211], a[28:31]\n"
    "buffer_load_dwordx4 v243, s[48:51], s76 offen offset:3072 lds\n"
    "v_mfma_f32_16x16x32_bf16 a[60:63], v[188:191], v[212:215], a[60:63]\n"
    "v_mfma_f32_16x16x32_bf16 a[92:95], v[188:191], v[216:219], a[92:95]\n"
    "v_mfma_f32_16x16x32_bf16 a[124:127], v[188:191], v[220:223], a[124:127]\n"
    "s_waitcnt lgkmcnt(0)\n"
    "s_sub_u32 s74, s74, 1\n"
    "s_cmp_lg_u32 s74, 0\n"
    "s_cbranch_scc1 QLP%=\n"
    "v_mfma_f32_16x16x32_bf16 v[0:3], v[128:131], v[192:195], v[0:3]\n"
    "s_add_u32 m0, s81, 0\n"
    "s_add_u32 s76, s79, 0\n"
    "v_mfma_f32_16x16x32_bf16 v[32:35], v[128:131], v[196:199], v[32:35]\n"
    "ds_read_b64_tr_b16 v[160:161], v236 offset:49152\n"
    "ds_read_b64_tr_b16 v[162:163], v237 offset:49152\n"
    "v_mfma_f32_16x16x32_bf16 v[64:67], v[128:131], v[200:203], v[64:67]\n"
    "buffer_load_dwordx4 v244, s[44:47], s76 offen lds\n"
    "v_mfma_f32_16x16x32_bf16 v[96:99], v[128:131], v[204:207], v[96:99]\n"
    "ds_read_b64_tr_b16 v[164:165], v236 offset:49160\n"
    "ds_read_b64_tr_b16 v[166:167], v237 offset:49160\n"
    "v_mfma_f32_16x16x32_bf16 v[4:7], v[132:135], v[192:195], v[4:7]\n"
    "v_mfma_f32_16x16x32_bf16 v[36:39], v[132:135], v[196:199], v[36:39]\n"
    "ds_read_b64_tr_b16 v[168:169], v236 offset:49664\n"
    "ds_read_b64_tr_b16 v[170:171], v237 offset:49664\n"
    "v_mfma_f32_16x16x32_bf16 v[68:71], v[132:135], v[200:203], v[68:71]\n"
    "s_add_u32 m0, s81, 1024\n"
    "s_add_u32 s76, s79, 10240\n"
    "v_mfma_f32_16x16x32_bf16 v[100:103], v[132:135], v[204:207], v[100:103]\n"
    "ds_read_b64_tr_b16 v[172:173], v236 offset:49672\n"
    "ds_read_b64_tr_b16 v[174:175], v237 offset:49672\n"
    "v_mfma_f32_16x16x32_bf16 v[8:11], v[136:139], v[192:195], v[8:11]\n"
    "buffer_load_dwordx4 v245, s[44:47], s76 offen lds\n"
    "v_mfma_f32_16x16x32_bf16 v[40:43], v[136:139], v[196:199], v[40:43]\n"
    "ds_read_b64_tr_b16 v[176:177], v236 offset:50176\n"
    "ds_read_b64_tr_b16 v[178:179], v237 offset:50176\n"
    "v_mfma_f32_16x16x32_bf16 v[72:75], v[136:139], v[200:203], v[72:75]\n"
    "v_mfma_f32_16x16x32_bf16 v[104:107], v[136:139], v[204:207], v[104:107]\n"
    "ds_read_b64_tr_b16 v[180:181], v236 offset:50184\n"
    "ds_read_b64_tr_b16 v[182:183], v237 offset:50184\n"
    "v_mfma_f32_16x16x32_bf16 v[12:15], v[140:143], v[192:195], v[12:15]\n"
    "s_add_u32 m0, s81, 2048\n"
    "s_add_u32 s76, s79, 20480\n"
    "v_mfma_f32_16x16x32_bf16 v[44:47], v[140:143], v[196:199], v[44:47]\n"
    "ds_read_b64_tr_b16 v[184:185], v236 offset:50688\n"
    "ds_read_b64_tr_b16 v[186:187], v237 offset:50688\n"
    "v_mfma_f32_16x16x32_bf16 v[76:79], v[140:143], v[200:203], v[76:79]\n"
    "v_mfma_f32_16x16x32_bf16 v[108:111], v[140:143], v[204:207], v[108:111]\n"
    "ds_read_b64_tr_b16 v[188:189], v236 offset:50696\n"
    "ds_read_b64_tr_b16 v[190:191], v237 offset:50696\n"
    "v_mfma_f32_16x16x32_bf16 v[16:19], v[144:147], v[192:195], v[16:19]\n"
    "buffer_load_dwordx4 v244, s[44:47], s76 offen lds\n"
    "v_mfma_f32_16x16x32_bf16 v[48:51], v[144:147], v[196:199], v[48:51]\n"
    "s_add_u32 m0, s81, 3072\n"
    "s_add_u32 s76, s79, 30720\n"
    "v_mfma_f32_16x16x32_bf16 v[80:83], v[144:147], v[200:203], v[80:83]\n"
    "v_mfma_f32_16x16x32_bf16 v[112:115], v[144:147], v[204:207], v[112:115]\n"
    "buffer_load_dwordx4 v245, s[44:47], s76 offen lds\n"
    "v_mfma_f32_16x16x32_bf16 v[20:23], v[148:151], v[192:195], v[20:23]\n"
    "s_add_u32 m0, s81, 4096\n"
    "s_add_u32 s76, s79, 40960\n"
    "v_mfma_f32_16x16x32_bf16 v[52:55], v[148:151], v[196:199], v[52:55]\n"
    "v_mfma_f32_16x16x32_bf16 v[84:87], v[148:151], v[200:203], v[84:87]\n"
    "buffer_load_dwordx4 v244, s[44:47], s76 offen lds\n"
    "v_mfma_f32_16x16x32_bf16 v[116:119], v[148:151], v[204:207], v[116:119]\n"
    "v_mfma_f32_16x16x32_bf16 v[24:27], v[152:155], v[192:195], v[24:27]\n"
    "s_add_u32 m0, s81, 5120\n"
    "s_add_u32 s76, s79, 51200\n"
    "v_mfma_f32_16x16x32_bf16 v[56:59], v[152:155], v[196:199], v[56:59]\n"
    "buffer_load_dwordx4 v245, s[44:47], s76 offen lds\n"
    "v_mfma_f32_16x16x32_bf16 v[88:91], v[152:155], v[200:203], v[88:91]\n"
    "v_mfma_f32_16x16x32_bf16 v[120:123], v[152:155], v[204:207], v[120:123]\n"
    "s_add_u32 m0, s81, 6144\n"
    "s_add_u32 s76, s79, 61440\n"
    "v_mfma_f32_16x16x32_bf16 v[28:31], v[156:159], v[192:195], v[28:31]\n"
    "v_mfma_f32_16x16x32_bf16 v[60:63], v[156:159], v[196:199], v[60:63]\n"
    "buffer_load_dwordx4 v244, s[44:47], s76 offen lds\n"
    "v_mfma_f32_16x16x32_bf16 v[92:95], v[156:159], v[200:203], v[92:95]\n"
    "s_add_u32 m0, s81, 7168\n"
    "s_add_u32 s76, s79, 71680\n"
    "v_mfma_f32_16x16x32_bf16 v[124:127], v[156:159], v[204:207], v[124:127]\n"
    "s_waitcnt lgkmcnt(0)\n"
    "v_mfma_f32_16x16x32_bf16 a[0:3], v[160:163], v[192:195], a[0:3]\n"
    "buffer_load_dwordx4 v245, s[44:47], s76 offen lds\n"
    "v_mfma_f32_16x16x32_bf16 a[32:35], v[160:163], v[196:199], a[32:35]\n"
    "ds_read_b128 v[208:211], v238 offset:33280\n"
    "ds_read_b128 v[212:215], v238 offset:35328\n"
    "v_mfma_f32_16x16x32_bf16 a[64:67], v[160:163], v[200:203], a[64:67]\n"
    "v_mfma_f32_16x16x32_bf16 a[96:99], v[160:163], v[204:207], a[96:99]\n"
    "ds_read_b128 v[216:219], v238 offset:37376\n"
    "ds_read_b128 v[220:223], v238 offset:39424\n"
    "v_mfma_f32_16x16x32_bf16 a[4:7], v[164:167], v[192:195], a[4:7]\n"
    "s_add_u32 s80, s80, 524288\n"
    "s_add_u32 s79, s79, 128\n"
    "v_mfma_f32_16x16x32_bf16 a[36:39], v[164:167], v[196:199], a[36:39]\n"
    "ds_read_b64_tr_b16 v[128:129], v236 offset:40960\n"
    "ds_read_b64_tr_b16 v[130:131], v237 offset:40960\n"
    "v_mfma_f32_16x16x32_bf16 a[68:71], v[164:167], v[200:203], a[68:71]\n"
    "v_mfma_f32_16x16x32_bf16 a[100:103], v[164:167], v[204:207], a[100:103]\n"
    "ds_read_b64_tr_b16 v[132:133], v236 offset:40968\n"
    "ds_read_b64_tr_b16 v[134:135], v237 offset:40968\n"
    "v_mfma_f32_16x16x32_bf16 a[8:11], v[168:171], v[192:195], a[8:11]\n"
    "v_mfma_f32_16x16x32_bf16 a[40:43], v[168:171], v[196:199], a[40:43]\n"
    "ds_read_b64_tr_b16 v[136:137], v236 offset:41472\n"
    "ds_read_b64_tr_b16 v[138:139], v237 offset:41472\n"
    "v_mfma_f32_16x16x32_bf16 a[72:75], v[168:171], v[200:203], a[72:75]\n"
    "s_movk_i32 s77, 4096\n"
    "buffer_load_dwordx4 a[252:255], v229, s[60:63], s77 offen offset:1024\n"
    "v_mfma_f32_16x16x32_bf16 a[104:107], v[168:171], v[204:207], a[104:107]\n"
    "ds_read_b64_tr_b16 v[140:141], v236 offset:41480\n"
    "ds_read_b64_tr_b16 v[142:143], v237 offset:41480\n"
    "v_mfma_f32_16x16x32_bf16 a[12:15], v[172:175], v[192:195], a[12:15]\n"
    "v_mfma_f32_16x16x32_bf16 a[44:47], v[172:175], v[196:199], a[44:47]\n"
    "ds_read_b64_tr_b16 v[144:145], v236 offset:41984\n"
    "ds_read_b64_tr_b16 v[146:147], v237 offset:41984\n"
    "v_mfma_f32_16x16x32_bf16 a[76:79], v[172:175], v[200:203], a[76:79]\n"
    "v_mfma_f32_16x16x32_bf16 a[108:111], v[172:175], v[204:207], a[108:111]\n"
    "ds_read_b64_tr_b16 v[148:149], v236 offset:41992\n"
    "ds_read_b64_tr_b16 v[150:151], v237 offset:41992\n"
    "v_mfma_f32_16x16x32_bf16 a[16:19], v[176:179], v[192:195], a[16:19]\n"
    "v_mfma_f32_16x16x32_bf16 a[48:51], v[176:179], v[196:199], a[48:51]\n"
    "ds_read_b64_tr_b16 v[152:153], v236 offset:42496\n"
    "ds_read_b64_tr_b16 v[154:155], v237 offset:42496\n"
    "v_mfma_f32_16x16x32_bf16 a[80:83], v[176:179], v[200:203], a[80:83]\n"
    "v_mfma_f32_16x16x32_bf16 a[112:115], v[176:179], v[204:207], a[112:115]\n"
    "ds_read_b64_tr_b16 v[156:157], v236 offset:42504\n"
    "ds_read_b64_tr_b16 v[158:159], v237 offset:42504\n"
    "v_mfma_f32_16x16x32_bf16 a[20:23], v[180:183], v[192:195], a[20:23]\n"
    "v_mfma_f32_16x16x32_bf16 a[52:55], v[180:183], v[196:199], a[52:55]\n"
    "v_mfma_f32_16x16x32_bf16 a[84:87], v[180:183], v[200:203], a[84:87]\n"
    "s_movk_i32 s77, 4096\n"
    "buffer_load_dwordx4 a[252:255], v229, s[60:63], s77 offen offset:2048\n"
    "v_mfma_f32_16x16x32_bf16 a[116:119], v[180:183], v[204:207], a[116:119]\n"
    "v_mfma_f32_16x16x32_bf16 a[24:27], v[184:187], v[192:195], a[24:27]\n"
    "v_mfma_f32_16x16x32_bf16 a[56:59], v[184:187], v[196:199], a[56:59]\n"
    "v_mfma_f32_16x16x32_bf16 a[88:91], v[184:187], v[200:203], a[88:91]\n"
    "v_mfma_f32_16x16x32_bf16 a[120:123], v[184:187], v[204:207], a[120:123]\n"
    "v_mfma_f32_16x16x32_bf16 a[28:31], v[188:191], v[192:195], a[28:31]\n"
    "v_mfma_f32_16x16x32_bf16 a[60:63], v[188:191], v[196:199], a[60:63]\n"
    "v_mfma_f32_16x16x32_bf16 a[92:95], v[188:191], v[200:203], a[92:95]\n"
    "s_movk_i32 s77, 4096\n"
    "buffer_load_dwordx4 a[252:255], v229, s[60:63], s77 offen offset:3072\n"
    "v_mfma_f32_16x16x32_bf16 a[124:127], v[188:191], v[204:207], a[124:127]\n"
    "s_waitcnt lgkmcnt(0)\n"
    "v_mfma_f32_16x16x32_bf16 v[0:3], v[128:131], v[208:211], v[0:3]\n"
    "v_mfma_f32_16x16x32_bf16 v[32:35], v[128:131], v[212:215], v[32:35]\n"
    "ds_read_b64_tr_b16 v[160:161], v236 offset:57344\n"
    "ds_read_b64_tr_b16 v[162:163], v237 offset:57344\n"
    "v_mfma_f32_16x16x32_bf16 v[64:67], v[128:131], v[216:219], v[64:67]\n"
    "v_mfma_f32_16x16x32_bf16 v[96:99], v[128:131], v[220:223], v[96:99]\n"
    "ds_read_b64_tr_b16 v[164:165], v236 offset:57352\n"
    "ds_read_b64_tr_b16 v[166:167], v237 offset:57352\n"
    "v_mfma_f32_16x16x32_bf16 v[4:7], v[132:135], v[208:211], v[4:7]\n"
    "v_mfma_f32_16x16x32_bf16 v[36:39], v[132:135], v[212:215], v[36:39]\n"
    "ds_read_b64_tr_b16 v[168:169], v236 offset:57856\n"
    "ds_read_b64_tr_b16 v[170:171], v237 offset:57856\n"
    "v_mfma_f32_16x16x32_bf16 v[68:71], v[132:135], v[216:219], v[68:71]\n"
    "v_mfma_f32_16x16x32_bf16 v[100:103], v[132:135], v[220:223], v[100:103]\n"
    "ds_read_b64_tr_b16 v[172:173], v236 offset:57864\n"
    "ds_read_b64_tr_b16 v[174:175], v237 offset:57864\n"
    "v_mfma_f32_16x16x32_bf16 v[8:11], v[136:139], v[208:211], v[8:11]\n"
    "v_mfma_f32_16x16x32_bf16 v[40:43], v[136:139], v[212:215], v[40:43]\n"
    "ds_read_b64_tr_b16 v[176:177], v236 offset:58368\n"
    "ds_read_b64_tr_b16 v[178:179], v237 offset:58368\n"
    "v_mfma_f32_16x16x32_bf16 v[72:75], v[136:139], v[216:219], v[72:75]\n"
    "v_mfma_f32_16x16x32_bf16 v[104:107], v[136:139], v[220:223], v[104:107]\n"
    "ds_read_b64_tr_b16 v[180:181], v236 offset:58376\n"
    "ds_read_b64_tr_b16 v[182:183], v237 offset:58376\n"
    "v_mfma_f32_16x16x32_bf16 v[12:15], v[140:143], v[208:211], v[12:15]\n"
    "s_movk_i32 s77, 8192\n"
    "buffer_load_dwordx4 a[252:255], v229, s[60:63], s77 offen offset:0\n"
    "v_mfma_f32_16x16x32_bf16 v[44:47], v[140:143], v[212:215], v[44:47]\n"
    "ds_read_b64_tr_b16 v[184:185], v236 offset:58880\n"
    "ds_read_b64_tr_b16 v[186:187], v237 offset:58880\n"
    "v_mfma_f32_16x16x32_bf16 v[76:79], v[140:143], v[216:219], v[76:79]\n"
    "v_mfma_f32_16x16x32_bf16 v[108:111], v[140:143], v[220:223], v[108:111]\n"
    "ds_read_b64_tr_b16 v[188:189], v236 offset:58888\n"
    "ds_read_b64_tr_b16 v[190:191], v237 offset:58888\n"
    "v_mfma_f32_16x16x32_bf16 v[16:19], v[144:147], v[208:211], v[16:19]\n"
    "v_mfma_f32_16x16x32_bf16 v[48:51], v[144:147], v[212:215], v[48:51]\n"
    "v_mfma_f32_16x16x32_bf16 v[80:83], v[144:147], v[216:219], v[80:83]\n"
    "v_mfma_f32_16x16x32_bf16 v[112:115], v[144:147], v[220:223], v[112:115]\n"
    "v_mfma_f32_16x16x32_bf16 v[20:23], v[148:151], v[208:211], v[20:23]\n"
    "v_mfma_f32_16x16x32_bf16 v[52:55], v[148:151], v[212:215], v[52:55]\n"
    "v_mfma_f32_16x16x32_bf16 v[84:87], v[148:151], v[216:219], v[84:87]\n"
    "s_movk_i32 s77, 8192\n"
    "buffer_load_dwordx4 a[252:255], v229, s[60:63], s77 offen offset:1024\n"
    "v_mfma_f32_16x16x32_bf16 v[116:119], v[148:151], v[220:223], v[116:119]\n"
    "v_mfma_f32_16x16x32_bf16 v[24:27], v[152:155], v[208:211], v[24:27]\n"
    "v_mfma_f32_16x16x32_bf16 v[56:59], v[152:155], v[212:215], v[56:59]\n"
    "v_mfma_f32_16x16x32_bf16 v[88:91], v[152:155], v[216:219], v[88:91]\n"
    "v_mfma_f32_16x16x32_bf16 v[120:123], v[152:155], v[220:223], v[120:123]\n"
    "v_mfma_f32_16x16x32_bf16 v[28:31], v[156:159], v[208:211], v[28:31]\n"
    "v_mfma_f32_16x16x32_bf16 v[60:63], v[156:159], v[212:215], v[60:63]\n"
    "v_mfma_f32_16x16x32_bf16 v[92:95], v[156:159], v[216:219], v[92:95]\n"
    "v_mfma_f32_16x16x32_bf16 v[124:127], v[156:159], v[220:223], v[124:127]\n"
    "s_waitcnt lgkmcnt(0)\n"
    "v_mfma_f32_16x16x32_bf16 a[0:3], v[160:163], v[208:211], a[0:3]\n"
    "s_waitcnt vmcnt(5)\n"
    "s_barrier\n"
    "v_mfma_f32_16x16x32_bf16 a[32:35], v[160:163], v[212:215], a[32:35]\n"
    "ds_read_b128 v[192:195], v238 offset:0\n"
    "ds_read_b128 v[196:199], v238 offset:2048\n"
    "v_mfma_f32_16x16x32_bf16 a[64:67], v[160:163], v[216:219], a[64:67]\n"
    "s_add_u32 m0, s72, 32768\n"
    "s_add_u32 s76, s80, 0\n"
    "v_mfma_f32_16x16x32_bf16 a[96:99], v[160:163], v[220:223], a[96:99]\n"
    "ds_read_b128 v[200:203], v238 offset:4096\n"
    "ds_read_b128 v[204:207], v238 offset:6144\n"
    "v_mfma_f32_16x16x32_bf16 a[4:7], v[164:167], v[208:211], a[4:7]\n"
    "buffer_load_dwordx4 v240, s[48:51], s76 offen offset:0 lds\n"
    "v_mfma_f32_16x16x32_bf16 a[36:39], v[164:167], v[212:215], a[36:39]\n"
    "ds_read_b64_tr_b16 v[128:129], v236 offset:0\n"
    "ds_read_b64_tr_b16 v[130:131], v237 offset:0\n"
    "v_mfma_f32_16x16x32_bf16 a[68:71], v[164:167], v[216:219], a[68:71]\n"
    "v_mfma_f32_16x16x32_bf16 a[100:103], v[164:167], v[220:223], a[100:103]\n"
    "ds_read_b64_tr_b16 v[132:133], v236 offset:8\n"
    "ds_read_b64_tr_b16 v[134:135], v237 offset:8\n"
    "v_mfma_f32_16x16x32_bf16 a[8:11], v[168:171], v[208:211], a[8:11]\n"
    "buffer_load_dwordx4 v241, s[48:51], s76 offen offset:1024 lds\n"
    "v_mfma_f32_16x16x32_bf16 a[40:43], v[168:171], v[212:215], a[40:43]\n"
    "ds_read_b64_tr_b16 v[136:137], v236 offset:512\n"
    "ds_read_b64_tr_b16 v[138:139], v237 offset:512\n"
    "v_mfma_f32_16x16x32_bf16 a[72:75], v[168:171], v[216:219], a[72:75]\n"
    "v_mfma_f32_16x16x32_bf16 a[104:107], v[168:171], v[220:223], a[104:107]\n"
    "ds_read_b64_tr_b16 v[140:141], v236 offset:520\n"
    "ds_read_b64_tr_b16 v[142:143], v237 offset:520\n"
    "v_mfma_f32_16x16x32_bf16 a[12:15], v[172:175], v[208:211], a[12:15]\n"
    "buffer_load_dwordx4 v242, s[48:51], s76 offen offset:2048 lds\n"
    "v_mfma_f32_16x16x32_bf16 a[44:47], v[172:175], v[212:215], a[44:47]\n"
    "ds_read_b64_tr_b16 v[144:145], v236 offset:1024\n"
    "ds_read_b64_tr_b16 v[146:147], v237 offset:1024\n"
    "v_mfma_f32_16x16x32_bf16 a[76:79], v[172:175], v[216:219], a[76:79]\n"
    "v_mfma_f32_16x16x32_bf16 a[108:111], v[172:175], v[220:223], a[108:111]\n"
    "ds_read_b64_tr_b16 v[148:149], v236 offset:1032\n"
    "ds_read_b64_tr_b16 v[150:151], v237 offset:1032\n"
    "v_mfma_f32_16x16x32_bf16 a[16:19], v[176:179], v[208:211], a[16:19]\n"
    "buffer_load_dwordx4 v243, s[48:51], s76 offen offset:3072 lds\n"
    "v_mfma_f32_16x16x32_bf16 a[48:51], v[176:179], v[212:215], a[48:51]\n"
    "ds_read_b64_tr_b16 v[152:153], v236 offset:1536\n"
    "ds_read_b64_tr_b16 v[154:155], v237 offset:1536\n"
    "v_mfma_f32_16x16x32_bf16 a[80:83], v[176:179], v[216:219], a[80:83]\n"
    "v_mfma_f32_16x16x32_bf16 a[112:115], v[176:179], v[220:223], a[112:115]\n"
    "ds_read_b64_tr_b16 v[156:157], v236 offset:1544\n"
    "ds_read_b64_tr_b16 v[158:159], v237 offset:1544\n"
    "v_mfma_f32_16x16x32_bf16 a[20:23], v[180:183], v[208:211], a[20:23]\n"
    "s_add_u32 m0, s72, 49152\n"
    "s_add_u32 s76, s80, 256\n"
    "v_mfma_f32_16x16x32_bf16 a[52:55], v[180:183], v[212:215], a[52:55]\n"
    "v_mfma_f32_16x16x32_bf16 a[84:87], v[180:183], v[216:219], a[84:87]\n"
    "buffer_load_dwordx4 v240, s[48:51], s76 offen offset:0 lds\n"
    "v_mfma_f32_16x16x32_bf16 a[116:119], v[180:183], v[220:223], a[116:119]\n"
    "v_mfma_f32_16x16x32_bf16 a[24:27], v[184:187], v[208:211], a[24:27]\n"
    "buffer_load_dwordx4 v241, s[48:51], s76 offen offset:1024 lds\n"
    "v_mfma_f32_16x16x32_bf16 a[56:59], v[184:187], v[212:215], a[56:59]\n"
    "v_mfma_f32_16x16x32_bf16 a[88:91], v[184:187], v[216:219], a[88:91]\n"
    "buffer_load_dwordx4 v242, s[48:51], s76 offen offset:2048 lds\n"
    "v_mfma_f32_16x16x32_bf16 a[120:123], v[184:187], v[220:223], a[120:123]\n"
    "v_mfma_f32_16x16x32_bf16 a[28:31], v[188:191], v[208:211], a[28:31]\n"
    "buffer_load_dwordx4 v243, s[48:51], s76 offen offset:3072 lds\n"
    "v_mfma_f32_16x16x32_bf16 a[60:63], v[188:191], v[212:215], a[60:63]\n"
    "v_mfma_f32_16x16x32_bf16 a[92:95], v[188:191], v[216:219], a[92:95]\n"
    "v_mfma_f32_16x16x32_bf16 a[124:127], v[188:191], v[220:223], a[124:127]\n"
    "s_waitcnt lgkmcnt(0)\n"
    "v_mfma_f32_16x16x32_bf16 v[0:3], v[128:131], v[192:195], v[0:3]\n"
    "s_add_u32 m0, s81, 32768\n"
    "s_add_u32 s76, s79, 0\n"
    "v_mfma_f32_16x16x32_bf16 v[32:35], v[128:131], v[196:199], v[32:35]\n"
    "ds_read_b64_tr_b16 v[160:161], v236 offset:16384\n"
    "ds_read_b64_tr_b16 v[162:163], v237 offset:16384\n"
    "v_mfma_f32_16x16x32_bf16 v[64:67], v[128:131], v[200:203], v[64:67]\n"
    "buffer_load_dwordx4 v244, s[44:47], s76 offen lds\n"
    "v_mfma_f32_16x16x32_bf16 v[96:99], v[128:131], v[204:207], v[96:99]\n"
    "ds_read_b64_tr_b16 v[164:165], v236 offset:16392\n"
    "ds_read_b64_tr_b16 v[166:167], v237 offset:16392\n"
    "v_mfma_f32_16x16x32_bf16 v[4:7], v[132:135], v[192:195], v[4:7]\n"
    "v_mfma_f32_16x16x32_bf16 v[36:39], v[132:135], v[196:199], v[36:39]\n"
    "ds_read_b64_tr_b16 v[168:169], v236 offset:16896\n"
    "ds_read_b64_tr_b16 v[170:171], v237 offset:16896\n"
    "v_mfma_f32_16x16x32_bf16 v[68:71], v[132:135], v[200:203], v[68:71]\n"
    "s_add_u32 m0, s81, 33792\n"
    "s_add_u32 s76, s79, 10240\n"
    "v_mfma_f32_16x16x32_bf16 v[100:103], v[132:135], v[204:207], v[100:103]\n"
    "ds_read_b64_tr_b16 v[172:173], v236 offset:16904\n"
    "ds_read_b64_tr_b16 v[174:175], v237 offset:16904\n"
    "v_mfma_f32_16x16x32_bf16 v[8:11], v[136:139], v[192:195], v[8:11]\n"
    "buffer_load_dwordx4 v245, s[44:47], s76 offen lds\n"
    "v_mfma_f32_16x16x32_bf16 v[40:43], v[136:139], v[196:199], v[40:43]\n"
    "ds_read_b64_tr_b16 v[176:177], v236 offset:17408\n"
    "ds_read_b64_tr_b16 v[178:179], v237 offset:17408\n"
    "v_mfma_f32_16x16x32_bf16 v[72:75], v[136:139], v[200:203], v[72:75]\n"
    "v_mfma_f32_16x16x32_bf16 v[104:107], v[136:139], v[204:207], v[104:107]\n"
    "ds_read_b64_tr_b16 v[180:181], v236 offset:17416\n"
    "ds_read_b64_tr_b16 v[182:183], v237 offset:17416\n"
    "v_mfma_f32_16x16x32_bf16 v[12:15], v[140:143], v[192:195], v[12:15]\n"
    "s_add_u32 m0, s81, 34816\n"
    "s_add_u32 s76, s79, 20480\n"
    "v_mfma_f32_16x16x32_bf16 v[44:47], v[140:143], v[196:199], v[44:47]\n"
    "ds_read_b64_tr_b16 v[184:185], v236 offset:17920\n"
    "ds_read_b64_tr_b16 v[186:187], v237 offset:17920\n"
    "v_mfma_f32_16x16x32_bf16 v[76:79], v[140:143], v[200:203], v[76:79]\n"
    "v_mfma_f32_16x16x32_bf16 v[108:111], v[140:143], v[204:207], v[108:111]\n"
    "ds_read_b64_tr_b16 v[188:189], v236 offset:17928\n"
    "ds_read_b64_tr_b16 v[190:191], v237 offset:17928\n"
    "v_mfma_f32_16x16x32_bf16 v[16:19], v[144:147], v[192:195], v[16:19]\n"
    "buffer_load_dwordx4 v244, s[44:47], s76 offen lds\n"
    "v_mfma_f32_16x16x32_bf16 v[48:51], v[144:147], v[196:199], v[48:51]\n"
    "s_add_u32 m0, s81, 35840\n"
    "s_add_u32 s76, s79, 30720\n"
    "v_mfma_f32_16x16x32_bf16 v[80:83], v[144:147], v[200:203], v[80:83]\n"
    "v_mfma_f32_16x16x32_bf16 v[112:115], v[144:147], v[204:207], v[112:115]\n"
    "buffer_load_dwordx4 v245, s[44:47], s76 offen lds\n"
    "v_mfma_f32_16x16x32_bf16 v[20:23], v[148:151], v[192:195], v[20:23]\n"
    "s_add_u32 m0, s81, 36864\n"
    "s_add_u32 s76, s79, 40960\n"
    "v_mfma_f32_16x16x32_bf16 v[52:55], v[148:151], v[196:199], v[52:55]\n"
    "v_mfma_f32_16x16x32_bf16 v[84:87], v[148:151], v[200:203], v[84:87]\n"
    "buffer_load_dwordx4 v244, s[44:47], s76 offen lds\n"
    "v_mfma_f32_16x16x32_bf16 v[116:119], v[148:151], v[204:207], v[116:119]\n"
    "v_mfma_f32_16x16x32_bf16 v[24:27], v[152:155], v[192:195], v[24:27]\n"
    "s_add_u32 m0, s81, 37888\n"
    "s_add_u32 s76, s79, 51200\n"
    "v_mfma_f32_16x16x32_bf16 v[56:59], v[152:155], v[196:199], v[56:59]\n"
    "buffer_load_dwordx4 v245, s[44:47], s76 offen lds\n"
    "v_mfma_f32_16x16x32_bf16 v[88:91], v[152:155], v[200:203], v[88:91]\n"
    "v_mfma_f32_16x16x32_bf16 v[120:123], v[152:155], v[204:207], v[120:123]\n"
    "s_add_u32 m0, s81, 38912\n"
    "s_add_u32 s76, s79, 61440\n"
    "v_mfma_f32_16x16x32_bf16 v[28:31], v[156:159], v[192:195], v[28:31]\n"
    "v_mfma_f32_16x16x32_bf16 v[60:63], v[156:159], v[196:199], v[60:63]\n"
    "buffer_load_dwordx4 v244, s[44:47], s76 offen lds\n"
    "v_mfma_f32_16x16x32_bf16 v[92:95], v[156:159], v[200:203], v[92:95]\n"
    "s_add_u32 m0, s81, 39936\n"
    "s_add_u32 s76, s79, 71680\n"
    "v_mfma_f32_16x16x32_bf16 v[124:127], v[156:159], v[204:207], v[124:127]\n"
    "s_waitcnt lgkmcnt(0)\n"
    "v_mfma_f32_16x16x32_bf16 a[0:3], v[160:163], v[192:195], a[0:3]\n"
    "buffer_load_dwordx4 v245, s[44:47], s76 offen lds\n"
    "v_mfma_f32_16x16x32_bf16 a[32:35], v[160:163], v[196:199], a[32:35]\n"
    "ds_read_b128 v[208:211], v238 offset:512\n"
    "ds_read_b128 v[212:215], v238 offset:2560\n"
    "v_mfma_f32_16x16x32_bf16 a[64:67], v[160:163], v[200:203], a[64:67]\n"
    "v_mfma_f32_16x16x32_bf16 a[96:99], v[160:163], v[204:207], a[96:99]\n"
    "ds_read_b128 v[216:219], v238 offset:4608\n"
    "ds_read_b128 v[220:223], v238 offset:6656\n"
    "v_mfma_f32_16x16x32_bf16 a[4:7], v[164:167], v[192:195], a[4:7]\n"
    "s_add_u32 s80, s80, 524288\n"
    "s_add_u32 s79, s79, 128\n"
    "v_mfma_f32_16x16x32_bf16 a[36:39], v[164:167], v[196:199], a[36:39]\n"
    "ds_read_b64_tr_b16 v[128:129], v236 offset:8192\n"
    "ds_read_b64_tr_b16 v[130:131], v237 offset:8192\n"
    "v_mfma_f32_16x16x32_bf16 a[68:71], v[164:167], v[200:203], a[68:71]\n"
    "v_mfma_f32_16x16x32_bf16 a[100:103], v[164:167], v[204:207], a[100:103]\n"
    "ds_read_b64_tr_b16 v[132:133], v236 offset:8200\n"
    "ds_read_b64_tr_b16 v[134:135], v237 offset:8200\n"
    "v_mfma_f32_16x16x32_bf16 a[8:11], v[168:171], v[192:195], a[8:11]\n"
    "v_mfma_f32_16x16x32_bf16 a[40:43], v[168:171], v[196:199], a[40:43]\n"
    "ds_read_b64_tr_b16 v[136:137], v236 offset:8704\n"
    "ds_read_b64_tr_b16 v[138:139], v237 offset:8704\n"
    "v_mfma_f32_16x16x32_bf16 a[72:75], v[168:171], v[200:203], a[72:75]\n"
    "v_mfma_f32_16x16x32_bf16 a[104:107], v[168:171], v[204:207], a[104:107]\n"
    "ds_read_b64_tr_b16 v[140:141], v236 offset:8712\n"
    "ds_read_b64_tr_b16 v[142:143], v237 offset:8712\n"
    "v_mfma_f32_16x16x32_bf16 a[12:15], v[172:175], v[192:195], a[12:15]\n"
    "v_mfma_f32_16x16x32_bf16 a[44:47], v[172:175], v[196:199], a[44:47]\n"
    "ds_read_b64_tr_b16 v[144:145], v236 offset:9216\n"
    "ds_read_b64_tr_b16 v[146:147], v237 offset:9216\n"
    "v_mfma_f32_16x16x32_bf16 a[76:79], v[172:175], v[200:203], a[76:79]\n"
    "v_mfma_f32_16x16x32_bf16 a[108:111], v[172:175], v[204:207], a[108:111]\n"
    "ds_read_b64_tr_b16 v[148:149], v236 offset:9224\n"
    "ds_read_b64_tr_b16 v[150:151], v237 offset:9224\n"
    "v_mfma_f32_16x16x32_bf16 a[16:19], v[176:179], v[192:195], a[16:19]\n"
    "v_mfma_f32_16x16x32_bf16 a[48:51], v[176:179], v[196:199], a[48:51]\n"
    "ds_read_b64_tr_b16 v[152:153], v236 offset:9728\n"
    "ds_read_b64_tr_b16 v[154:155], v237 offset:9728\n"
    "v_mfma_f32_16x16x32_bf16 a[80:83], v[176:179], v[200:203], a[80:83]\n"
    "v_mfma_f32_16x16x32_bf16 a[112:115], v[176:179], v[204:207], a[112:115]\n"
    "ds_read_b64_tr_b16 v[156:157], v236 offset:9736\n"
    "ds_read_b64_tr_b16 v[158:159], v237 offset:9736\n"
    "v_mfma_f32_16x16x32_bf16 a[20:23], v[180:183], v[192:195], a[20:23]\n"
    "v_mfma_f32_16x16x32_bf16 a[52:55], v[180:183], v[196:199], a[52:55]\n"
    "v_mfma_f32_16x16x32_bf16 a[84:87], v[180:183], v[200:203], a[84:87]\n"
    "v_mfma_f32_16x16x32_bf16 a[116:119], v[180:183], v[204:207], a[116:119]\n"
    "v_mfma_f32_16x16x32_bf16 a[24:27], v[184:187], v[192:195], a[24:27]\n"
    "v_mfma_f32_16x16x32_bf16 a[56:59], v[184:187], v[196:199], a[56:59]\n"
    "v_mfma_f32_16x16x32_bf16 a[88:91], v[184:187], v[200:203], a[88:91]\n"
    "v_mfma_f32_16x16x32_bf16 a[120:123], v[184:187], v[204:207], a[120:123]\n"
    "v_mfma_f32_16x16x32_bf16 a[28:31], v[188:191], v[192:195], a[28:31]\n"
    "v_mfma_f32_16x16x32_bf16 a[60:63], v[188:191], v[196:199], a[60:63]\n"
    "v_mfma_f32_16x16x32_bf16 a[92:95], v[188:191], v[200:203], a[92:95]\n"
    "v_mfma_f32_16x16x32_bf16 a[124:127], v[188:191], v[204:207], a[124:127]\n"
    "s_waitcnt lgkmcnt(0)\n"
    "v_mfma_f32_16x16x32_bf16 v[0:3], v[128:131], v[208:211], v[0:3]\n"
    "v_mfma_f32_16x16x32_bf16 v[32:35], v[128:131], v[212:215], v[32:35]\n"
    "ds_read_b64_tr_b16 v[160:161], v236 offset:24576\n"
    "ds_read_b64_tr_b16 v[162:163], v237 offset:24576\n"
    "v_mfma_f32_16x16x32_bf16 v[64:67], v[128:131], v[216:219], v[64:67]\n"
    "v_mfma_f32_16x16x32_bf16 v[96:99], v[128:131], v[220:223], v[96:99]\n"
    "ds_read_b64_tr_b16 v[164:165], v236 offset:24584\n"
    "ds_read_b64_tr_b16 v[166:167], v237 offset:24584\n"
    "v_mfma_f32_16x16x32_bf16 v[4:7], v[132:135], v[208:211], v[4:7]\n"
    "v_mfma_f32_16x16x32_bf16 v[36:39], v[132:135], v[212:215], v[36:39]\n"
    "ds_read_b64_tr_b16 v[168:169], v236 offset:25088\n"
    "ds_read_b64_tr_b16 v[170:171], v237 offset:25088\n"
    "v_mfma_f32_16x16x32_bf16 v[68:71], v[132:135], v[216:219], v[68:71]\n"
    "v_mfma_f32_16x16x32_bf16 v[100:103], v[132:135], v[220:223], v[100:103]\n"
    "ds_read_b64_tr_b16 v[172:173], v236 offset:25096\n"
    "ds_read_b64_tr_b16 v[174:175], v237 offset:25096\n"
    "v_mfma_f32_16x16x32_bf16 v[8:11], v[136:139], v[208:211], v[8:11]\n"
    "v_mfma_f32_16x16x32_bf16 v[40:43], v[136:139], v[212:215], v[40:43]\n"
    "ds_read_b64_tr_b16 v[176:177], v236 offset:25600\n"
    "ds_read_b64_tr_b16 v[178:179], v237 offset:25600\n"
    "v_mfma_f32_16x16x32_bf16 v[72:75], v[136:139], v[216:219], v[72:75]\n"
    "v_mfma_f32_16x16x32_bf16 v[104:107], v[136:139], v[220:223], v[104:107]\n"
    "ds_read_b64_tr_b16 v[180:181], v236 offset:25608\n"
    "ds_read_b64_tr_b16 v[182:183], v237 offset:25608\n"
    "v_mfma_f32_16x16x32_bf16 v[12:15], v[140:143], v[208:211], v[12:15]\n"
    "v_mfma_f32_16x16x32_bf16 v[44:47], v[140:143], v[212:215], v[44:47]\n"
    "ds_read_b64_tr_b16 v[184:185], v236 offset:26112\n"
    "ds_read_b64_tr_b16 v[186:187], v237 offset:26112\n"
    "v_mfma_f32_16x16x32_bf16 v[76:79], v[140:143], v[216:219], v[76:79]\n"
    "v_mfma_f32_16x16x32_bf16 v[108:111], v[140:143], v[220:223], v[108:111]\n"
    "ds_read_b64_tr_b16 v[188:189], v236 offset:26120\n"
    "ds_read_b64_tr_b16 v[190:191], v237 offset:26120\n"
    "v_mfma_f32_16x16x32_bf16 v[16:19], v[144:147], v[208:211], v[16:19]\n"
    "v_mfma_f32_16x16x32_bf16 v[48:51], v[144:147], v[212:215], v[48:51]\n"
    "v_mfma_f32_16x16x32_bf16 v[80:83], v[144:147], v[216:219], v[80:83]\n"
    "v_mfma_f32_16x16x32_bf16 v[112:115], v[144:147], v[220:223], v[112:115]\n"
    "v_mfma_f32_16x16x32_bf16 v[20:23], v[148:151], v[208:211], v[20:23]\n"
    "v_mfma_f32_16x16x32_bf16 v[52:55], v[148:151], v[212:215], v[52:55]\n"
    "v_mfma_f32_16x16x32_bf16 v[84:87], v[148:151], v[216:219], v[84:87]\n"
    "v_mfma_f32_16x16x32_bf16 v[116:119], v[148:151], v[220:223], v[116:119]\n"
    "v_mfma_f32_16x16x32_bf16 v[24:27], v[152:155], v[208:211], v[24:27]\n"
    "v_mfma_f32_16x16x32_bf16 v[56:59], v[152:155], v[212:215], v[56:59]\n"
    "v_mfma_f32_16x16x32_bf16 v[88:91], v[152:155], v[216:219], v[88:91]\n"
    "v_mfma_f32_16x16x32_bf16 v[120:123], v[152:155], v[220:223], v[120:123]\n"
    "v_mfma_f32_16x16x32_bf16 v[28:31], v[156:159], v[208:211], v[28:31]\n"
    "v_mfma_f32_16x16x32_bf16 v[60:63], v[156:159], v[212:215], v[60:63]\n"
    "v_mfma_f32_16x16x32_bf16 v[92:95], v[156:159], v[216:219], v[92:95]\n"
    "v_mfma_f32_16x16x32_bf16 v[124:127], v[156:159], v[220:223], v[124:127]\n"
    "s_waitcnt lgkmcnt(0)\n"
    "v_mfma_f32_16x16x32_bf16 a[0:3], v[160:163], v[208:211], a[0:3]\n"
    "s_waitcnt vmcnt(0)\n"
    "s_barrier\n"
    "v_mfma_f32_16x16x32_bf16 a[32:35], v[160:163], v[212:215], a[32:35]\n"
    "ds_read_b128 v[192:195], v238 offset:32768\n"
    "ds_read_b128 v[196:199], v238 offset:34816\n"
    "v_mfma_f32_16x16x32_bf16 a[64:67], v[160:163], v[216:219], a[64:67]\n"
    "s_add_u32 m0, s72, 0\n"
    "s_add_u32 s76, s71, 0\n"
    "v_mfma_f32_16x16x32_bf16 a[96:99], v[160:163], v[220:223], a[96:99]\n"
    "ds_read_b128 v[200:203], v238 offset:36864\n"
    "ds_read_b128 v[204:207], v238 offset:38912\n"
    "v_mfma_f32_16x16x32_bf16 a[4:7], v[164:167], v[208:211], a[4:7]\n"
    "buffer_load_dwordx4 v224, s[36:39], s76 offen offset:0 nt lds\n"
    "v_mfma_f32_16x16x32_bf16 a[36:39], v[164:167], v[212:215], a[36:39]\n"
    "ds_read_b64_tr_b16 v[128:129], v236 offset:32768\n"
    "ds_read_b64_tr_b16 v[130:131], v237 offset:32768\n"
    "v_mfma_f32_16x16x32_bf16 a[68:71], v[164:167], v[216:219], a[68:71]\n"
    "v_mfma_f32_16x16x32_bf16 a[100:103], v[164:167], v[220:223], a[100:103]\n"
    "ds_read_b64_tr_b16 v[132:133], v236 offset:32776\n"
    "ds_read_b64_tr_b16 v[134:135], v237 offset:32776\n"
    "v_mfma_f32_16x16x32_bf16 a[8:11], v[168:171], v[208:211], a[8:11]\n"
    "buffer_load_dwordx4 v225, s[36:39], s76 offen offset:1024 nt lds\n"
    "v_mfma_f32_16x16x32_bf16 a[40:43], v[168:171], v[212:215], a[40:43]\n"
    "ds_read_b64_tr_b16 v[136:137], v236 offset:33280\n"
    "ds_read_b64_tr_b16 v[138:139], v237 offset:33280\n"
    "v_mfma_f32_16x16x32_bf16 a[72:75], v[168:171], v[216:219], a[72:75]\n"
    "v_mfma_f32_16x16x32_bf16 a[104:107], v[168:171], v[220:223], a[104:107]\n"
    "ds_read_b64_tr_b16 v[140:141], v236 offset:33288\n"
    "ds_read_b64_tr_b16 v[142:143], v237 offset:33288\n"
    "v_mfma_f32_16x16x32_bf16 a[12:15], v[172:175], v[208:211], a[12:15]\n"
    "buffer_load_dwordx4 v226, s[36:39], s76 offen offset:2048 nt lds\n"
    "v_mfma_f32_16x16x32_bf16 a[44:47], v[172:175], v[212:215], a[44:47]\n"
    "ds_read_b64_tr_b16 v[144:145], v236 offset:33792\n"
    "ds_read_b64_tr_b16 v[146:147], v237 offset:33792\n"
    "v_mfma_f32_16x16x32_bf16 a[76:79], v[172:175], v[216:219], a[76:79]\n"
    "v_mfma_f32_16x16x32_bf16 a[108:111], v[172:175], v[220:223], a[108:111]\n"
    "ds_read_b64_tr_b16 v[148:149], v236 offset:33800\n"
    "ds_read_b64_tr_b16 v[150:151], v237 offset:33800\n"
    "v_mfma_f32_16x16x32_bf16 a[16:19], v[176:179], v[208:211], a[16:19]\n"
    "buffer_load_dwordx4 v227, s[36:39], s76 offen offset:3072 nt lds\n"
    "v_mfma_f32_16x16x32_bf16 a[48:51], v[176:179], v[212:215], a[48:51]\n"
    "ds_read_b64_tr_b16 v[152:153], v236 offset:34304\n"
    "ds_read_b64_tr_b16 v[154:155], v237 offset:34304\n"
    "v_mfma_f32_16x16x32_bf16 a[80:83], v[176:179], v[216:219], a[80:83]\n"
    "v_mfma_f32_16x16x32_bf16 a[112:115], v[176:179], v[220:223], a[112:115]\n"
    "ds_read_b64_tr_b16 v[156:157], v236 offset:34312\n"
    "ds_read_b64_tr_b16 v[158:159], v237 offset:34312\n"
    "v_mfma_f32_16x16x32_bf16 a[20:23], v[180:183], v[208:211], a[20:23]\n"
    "s_add_u32 m0, s72, 16384\n"
    "s_add_u32 s76, s71, 16384\n"
    "v_mfma_f32_16x16x32_bf16 a[52:55], v[180:183], v[212:215], a[52:55]\n"
    "v_mfma_f32_16x16x32_bf16 a[84:87], v[180:183], v[216:219], a[84:87]\n"
    "buffer_load_dwordx4 v224, s[36:39], s76 offen offset:0 nt lds\n"
    "v_mfma_f32_16x16x32_bf16 a[116:119], v[180:183], v[220:223], a[116:119]\n"
    "v_mfma_f32_16x16x32_bf16 a[24:27], v[184:187], v[208:211], a[24:27]\n"
    "buffer_load_dwordx4 v225, s[36:39], s76 offen offset:1024 nt lds\n"
    "v_mfma_f32_16x16x32_bf16 a[56:59], v[184:187], v[212:215], a[56:59]\n"
    "v_mfma_f32_16x16x32_bf16 a[88:91], v[184:187], v[216:219], a[88:91]\n"
    "buffer_load_dwordx4 v226, s[36:39], s76 offen offset:2048 nt lds\n"
    "v_mfma_f32_16x16x32_bf16 a[120:123], v[184:187], v[220:223], a[120:123]\n"
    "v_mfma_f32_16x16x32_bf16 a[28:31], v[188:191], v[208:211], a[28:31]\n"
    "buffer_load_dwordx4 v227, s[36:39], s76 offen offset:3072 nt lds\n"
    "v_mfma_f32_16x16x32_bf16 a[60:63], v[188:191], v[212:215], a[60:63]\n"
    "v_mfma_f32_16x16x32_bf16 a[92:95], v[188:191], v[216:219], a[92:95]\n"
    "v_mfma_f32_16x16x32_bf16 a[124:127], v[188:191], v[220:223], a[124:127]\n"
    "s_waitcnt lgkmcnt(0)\n"
    "v_mfma_f32_16x16x32_bf16 v[0:3], v[128:131], v[192:195], v[0:3]\n"
    "s_add_u32 m0, s73, 0\n"
    "s_add_u32 s76, s71, 0\n"
    "v_mfma_f32_16x16x32_bf16 v[32:35], v[128:131], v[196:199], v[32:35]\n"
    "ds_read_b64_tr_b16 v[160:161], v236 offset:49152\n"
    "ds_read_b64_tr_b16 v[162:163], v237 offset:49152\n"
    "v_mfma_f32_16x16x32_bf16 v[64:67], v[128:131], v[200:203], v[64:67]\n"
    "v_mfma_f32_16x16x32_bf16 v[96:99], v[128:131], v[204:207], v[96:99]\n"
    "ds_read_b64_tr_b16 v[164:165], v236 offset:49160\n"
    "ds_read_b64_tr_b16 v[166:167], v237 offset:49160\n"
    "v_mfma_f32_16x16x32_bf16 v[4:7], v[132:135], v[192:195], v[4:7]\n"
    "buffer_load_dwordx4 v224, s[40:43], s76 offen offset:0 nt lds\n"
    "v_mfma_f32_16x16x32_bf16 v[36:39], v[132:135], v[196:199], v[36:39]\n"
    "ds_read_b64_tr_b16 v[168:169], v236 offset:49664\n"
    "ds_read_b64_tr_b16 v[170:171], v237 offset:49664\n"
    "v_mfma_f32_16x16x32_bf16 v[68:71], v[132:135], v[200:203], v[68:71]\n"
    "v_mfma_f32_16x16x32_bf16 v[100:103], v[132:135], v[204:207], v[100:103]\n"
    "ds_read_b64_tr_b16 v[172:173], v236 offset:49672\n"
    "ds_read_b64_tr_b16 v[174:175], v237 offset:49672\n"
    "v_mfma_f32_16x16x32_bf16 v[8:11], v[136:139], v[192:195], v[8:11]\n"
    "v_mfma_f32_16x16x32_bf16 v[40:43], v[136:139], v[196:199], v[40:43]\n"
    "ds_read_b64_tr_b16 v[176:177], v236 offset:50176\n"
    "ds_read_b64_tr_b16 v[178:179], v237 offset:50176\n"
    "v_mfma_f32_16x16x32_bf16 v[72:75], v[136:139], v[200:203], v[72:75]\n"
    "buffer_load_dwordx4 v225, s[40:43], s76 offen offset:1024 nt lds\n"
    "v_mfma_f32_16x16x32_bf16 v[104:107], v[136:139], v[204:207], v[104:107]\n"
    "ds_read_b64_tr_b16 v[180:181], v236 offset:50184\n"
    "ds_read_b64_tr_b16 v[182:183], v237 offset:50184\n"
    "v_mfma_f32_16x16x32_bf16 v[12:15], v[140:143], v[192:195], v[12:15]\n"
    "v_mfma_f32_16x16x32_bf16 v[44:47], v[140:143], v[196:199], v[44:47]\n"
    "ds_read_b64_tr_b16 v[184:185], v236 offset:50688\n"
    "ds_read_b64_tr_b16 v[186:187], v237 offset:50688\n"
    "v_mfma_f32_16x16x32_bf16 v[76:79], v[140:143], v[200:203], v[76:79]\n"
    "v_mfma_f32_16x16x32_bf16 v[108:111], v[140:143], v[204:207], v[108:111]\n"
    "ds_read_b64_tr_b16 v[188:189], v236 offset:50696\n"
    "ds_read_b64_tr_b16 v[190:191], v237 offset:50696\n"
    "v_mfma_f32_16x16x32_bf16 v[16:19], v[144:147], v[192:195], v[16:19]\n"
    "buffer_load_dwordx4 v226, s[40:43], s76 offen offset:2048 nt lds\n"
    "v_mfma_f32_16x16x32_bf16 v[48:51], v[144:147], v[196:199], v[48:51]\n"
    "v_mfma_f32_16x16x32_bf16 v[80:83], v[144:147], v[200:203], v[80:83]\n"
    "buffer_load_dwordx4 v227, s[40:43], s76 offen offset:3072 nt lds\n"
    "v_mfma_f32_16x16x32_bf16 v[112:115], v[144:147], v[204:207], v[112:115]\n"
    "v_mfma_f32_16x16x32_bf16 v[20:23], v[148:151], v[192:195], v[20:23]\n"
    "v_mfma_f32_16x16x32_bf16 v[52:55], v[148:151], v[196:199], v[52:55]\n"
    "s_add_u32 m0, s73, 16384\n"
    "s_add_u32 s76, s71, 16384\n"
    "v_mfma_f32_16x16x32_bf16 v[84:87], v[148:151], v[200:203], v[84:87]\n"
    "v_mfma_f32_16x16x32_bf16 v[116:119], v[148:151], v[204:207], v[116:119]\n"
    "v_mfma_f32_16x16x32_bf16 v[24:27], v[152:155], v[192:195], v[24:27]\n"
    "buffer_load_dwordx4 v224, s[40:43], s76 offen offset:0 nt lds\n"
    "v_mfma_f32_16x16x32_bf16 v[56:59], v[152:155], v[196:199], v[56:59]\n"
    "v_mfma_f32_16x16x32_bf16 v[88:91], v[152:155], v[200:203], v[88:91]\n"
    "v_mfma_f32_16x16x32_bf16 v[120:123], v[152:155], v[204:207], v[120:123]\n"
    "buffer_load_dwordx4 v225, s[40:43], s76 offen offset:1024 nt lds\n"
    "v_mfma_f32_16x16x32_bf16 v[28:31], v[156:159], v[192:195], v[28:31]\n"
    "v_mfma_f32_16x16x32_bf16 v[60:63], v[156:159], v[196:199], v[60:63]\n"
    "buffer_load_dwordx4 v226, s[40:43], s76 offen offset:2048 nt lds\n"
    "v_mfma_f32_16x16x32_bf16 v[92:95], v[156:159], v[200:203], v[92:95]\n"
    "v_mfma_f32_16x16x32_bf16 v[124:127], v[156:159], v[204:207], v[124:127]\n"
    "s_waitcnt lgkmcnt(0)\n"
    "v_mfma_f32_16x16x32_bf16 a[0:3], v[160:163], v[192:195], a[0:3]\n"
    "v_mfma_f32_16x16x32_bf16 a[32:35], v[160:163], v[196:199], a[32:35]\n"
    "ds_read_b128 v[208:211], v238 offset:33280\n"
    "ds_read_b128 v[212:215], v238 offset:35328\n"
    "v_mfma_f32_16x16x32_bf16 a[64:67], v[160:163], v[200:203], a[64:67]\n"
    "buffer_load_dwordx4 v227, s[40:43], s76 offen offset:3072 nt lds\n"
    "v_mfma_f32_16x16x32_bf16 a[96:99], v[160:163], v[204:207], a[96:99]\n"
    "ds_read_b128 v[216:219], v238 offset:37376\n"
    "ds_read_b128 v[220:223], v238 offset:39424\n"
    "v_mfma_f32_16x16x32_bf16 a[4:7], v[164:167], v[192:195], a[4:7]\n"
    "v_mfma_f32_16x16x32_bf16 a[36:39], v[164:167], v[196:199], a[36:39]\n"
    "ds_read_b64_tr_b16 v[128:129], v236 offset:40960\n"
    "ds_read_b64_tr_b16 v[130:131], v237 offset:40960\n"
    "v_mfma_f32_16x16x32_bf16 a[68:71], v[164:167], v[200:203], a[68:71]\n"
    "v_mfma_f32_16x16x32_bf16 a[100:103], v[164:167], v[204:207], a[100:103]\n"
    "ds_read_b64_tr_b16 v[132:133], v236 offset:40968\n"
    "ds_read_b64_tr_b16 v[134:135], v237 offset:40968\n"
    "v_mfma_f32_16x16x32_bf16 a[8:11], v[168:171], v[192:195], a[8:11]\n"
    "v_mfma_f32_16x16x32_bf16 a[40:43], v[168:171], v[196:199], a[40:43]\n"
    "ds_read_b64_tr_b16 v[136:137], v236 offset:41472\n"
    "ds_read_b64_tr_b16 v[138:139], v237 offset:41472\n"
    "v_mfma_f32_16x16x32_bf16 a[72:75], v[168:171], v[200:203], a[72:75]\n"
    "v_mfma_f32_16x16x32_bf16 a[104:107], v[168:171], v[204:207], a[104:107]\n"
    "ds_read_b64_tr_b16 v[140:141], v236 offset:41480\n"
    "ds_read_b64_tr_b16 v[142:143], v237 offset:41480\n"
    "v_mfma_f32_16x16x32_bf16 a[12:15], v[172:175], v[192:195], a[12:15]\n"
    "v_mfma_f32_16x16x32_bf16 a[44:47], v[172:175], v[196:199], a[44:47]\n"
    "ds_read_b64_tr_b16 v[144:145], v236 offset:41984\n"
    "ds_read_b64_tr_b16 v[146:147], v237 offset:41984\n"
    "v_mfma_f32_16x16x32_bf16 a[76:79], v[172:175], v[200:203], a[76:79]\n"
    "v_mfma_f32_16x16x32_bf16 a[108:111], v[172:175], v[204:207], a[108:111]\n"
    "ds_read_b64_tr_b16 v[148:149], v236 offset:41992\n"
    "ds_read_b64_tr_b16 v[150:151], v237 offset:41992\n"
    "v_mfma_f32_16x16x32_bf16 a[16:19], v[176:179], v[192:195], a[16:19]\n"
    "v_mfma_f32_16x16x32_bf16 a[48:51], v[176:179], v[196:199], a[48:51]\n"
    "ds_read_b64_tr_b16 v[152:153], v236 offset:42496\n"
    "ds_read_b64_tr_b16 v[154:155], v237 offset:42496\n"
    "v_mfma_f32_16x16x32_bf16 a[80:83], v[176:179], v[200:203], a[80:83]\n"
    "v_mfma_f32_16x16x32_bf16 a[112:115], v[176:179], v[204:207], a[112:115]\n"
    "ds_read_b64_tr_b16 v[156:157], v236 offset:42504\n"
    "ds_read_b64_tr_b16 v[158:159], v237 offset:42504\n"
    "v_mfma_f32_16x16x32_bf16 a[20:23], v[180:183], v[192:195], a[20:23]\n"
    "v_mfma_f32_16x16x32_bf16 a[52:55], v[180:183], v[196:199], a[52:55]\n"
    "v_mfma_f32_16x16x32_bf16 a[84:87], v[180:183], v[200:203], a[84:87]\n"
    "v_mfma_f32_16x16x32_bf16 a[116:119], v[180:183], v[204:207], a[116:119]\n"
    "v_mfma_f32_16x16x32_bf16 a[24:27], v[184:187], v[192:195], a[24:27]\n"
    "v_mfma_f32_16x16x32_bf16 a[56:59], v[184:187], v[196:199], a[56:59]\n"
    "v_mfma_f32_16x16x32_bf16 a[88:91], v[184:187], v[200:203], a[88:91]\n"
    "v_mfma_f32_16x16x32_bf16 a[120:123], v[184:187], v[204:207], a[120:123]\n"
    "v_mfma_f32_16x16x32_bf16 a[28:31], v[188:191], v[192:195], a[28:31]\n"
    "v_mfma_f32_16x16x32_bf16 a[60:63], v[188:191], v[196:199], a[60:63]\n"
    "v_mfma_f32_16x16x32_bf16 a[92:95], v[188:191], v[200:203], a[92:95]\n"
    "v_mfma_f32_16x16x32_bf16 a[124:127], v[188:191], v[204:207], a[124:127]\n"
    "s_waitcnt lgkmcnt(0)\n"
    "v_mfma_f32_16x16x32_bf16 v[0:3], v[128:131], v[208:211], v[0:3]\n"
    "v_mfma_f32_16x16x32_bf16 v[32:35], v[128:131], v[212:215], v[32:35]\n"
    "ds_read_b64_tr_b16 v[160:161], v236 offset:57344\n"
    "ds_read_b64_tr_b16 v[162:163], v237 offset:57344\n"
    "v_mfma_f32_16x16x32_bf16 v[64:67], v[128:131], v[216:219], v[64:67]\n"
    "v_mfma_f32_16x16x32_bf16 v[96:99], v[128:131], v[220:223], v[96:99]\n"
    "ds_read_b64_tr_b16 v[164:165], v236 offset:57352\n"
    "ds_read_b64_tr_b16 v[166:167], v237 offset:57352\n"
    "v_mfma_f32_16x16x32_bf16 v[4:7], v[132:135], v[208:211], v[4:7]\n"
    "v_mfma_f32_16x16x32_bf16 v[36:39], v[132:135], v[212:215], v[36:39]\n"
    "ds_read_b64_tr_b16 v[168:169], v236 offset:57856\n"
    "ds_read_b64_tr_b16 v[170:171], v237 offset:57856\n"
    "v_mfma_f32_16x16x32_bf16 v[68:71], v[132:135], v[216:219], v[68:71]\n"
    "v_mfma_f32_16x16x32_bf16 v[100:103], v[132:135], v[220:223], v[100:103]\n"
    "ds_read_b64_tr_b16 v[172:173], v236 offset:57864\n"
    "ds_read_b64_tr_b16 v[174:175], v237 offset:57864\n"
    "v_mfma_f32_16x16x32_bf16 v[8:11], v[136:139], v[208:211], v[8:11]\n"
    "v_mfma_f32_16x16x32_bf16 v[40:43], v[136:139], v[212:215], v[40:43]\n"
    "ds_read_b64_tr_b16 v[176:177], v236 offset:58368\n"
    "ds_read_b64_tr_b16 v[178:179], v237 offset:58368\n"
    "v_mfma_f32_16x16x32_bf16 v[72:75], v[136:139], v[216:219], v[72:75]\n"
    "v_mfma_f32_16x16x32_bf16 v[104:107], v[136:139], v[220:223], v[104:107]\n"
    "ds_read_b64_tr_b16 v[180:181], v236 offset:58376\n"
    "ds_read_b64_tr_b16 v[182:183], v237 offset:58376\n"
    "v_mfma_f32_16x16x32_bf16 v[12:15], v[140:143], v[208:211], v[12:15]\n"
    "v_mfma_f32_16x16x32_bf16 v[44:47], v[140:143], v[212:215], v[44:47]\n"
    "ds_read_b64_tr_b16 v[184:185], v236 offset:58880\n"
    "ds_read_b64_tr_b16 v[186:187], v237 offset:58880\n"
    "v_mfma_f32_16x16x32_bf16 v[76:79], v[140:143], v[216:219], v[76:79]\n"
    "v_mfma_f32_16x16x32_bf16 v[108:111], v[140:143], v[220:223], v[108:111]\n"
    "ds_read_b64_tr_b16 v[188:189], v236 offset:58888\n"
    "ds_read_b64_tr_b16 v[190:191], v237 offset:58888\n"
    "v_mfma_f32_16x16x32_bf16 v[16:19], v[144:147], v[208:211], v[16:19]\n"
    "v_mfma_f32_16x16x32_bf16 v[48:51], v[144:147], v[212:215], v[48:51]\n"
    "v_mfma_f32_16x16x32_bf16 v[80:83], v[144:147], v[216:219], v[80:83]\n"
    "v_mfma_f32_16x16x32_bf16 v[112:115], v[144:147], v[220:223], v[112:115]\n"
    "v_mfma_f32_16x16x32_bf16 v[20:23], v[148:151], v[208:211], v[20:23]\n"
    "v_mfma_f32_16x16x32_bf16 v[52:55], v[148:151], v[212:215], v[52:55]\n"
    "v_mfma_f32_16x16x32_bf16 v[84:87], v[148:151], v[216:219], v[84:87]\n"
    "v_mfma_f32_16x16x32_bf16 v[116:119], v[148:151], v[220:223], v[116:119]\n"
    "v_mfma_f32_16x16x32_bf16 v[24:27], v[152:155], v[208:211], v[24:27]\n"
    "v_mfma_f32_16x16x32_bf16 v[56:59], v[152:155], v[212:215], v[56:59]\n"
    "v_mfma_f32_16x16x32_bf16 v[88:91], v[152:155], v[216:219], v[88:91]\n"
    "v_mfma_f32_16x16x32_bf16 v[120:123], v[152:155], v[220:223], v[120:123]\n"
    "v_mfma_f32_16x16x32_bf16 v[28:31], v[156:159], v[208:211], v[28:31]\n"
    "v_mfma_f32_16x16x32_bf16 v[60:63], v[156:159], v[212:215], v[60:63]\n"
    "v_mfma_f32_16x16x32_bf16 v[92:95], v[156:159], v[216:219], v[92:95]\n"
    "v_mfma_f32_16x16x32_bf16 v[124:127], v[156:159], v[220:223], v[124:127]\n"
    "s_waitcnt lgkmcnt(0)\n"
    "v_mfma_f32_16x16x32_bf16 a[0:3], v[160:163], v[208:211], a[0:3]\n"
    "v_mfma_f32_16x16x32_bf16 a[32:35], v[160:163], v[212:215], a[32:35]\n"
    "v_mfma_f32_16x16x32_bf16 a[64:67], v[160:163], v[216:219], a[64:67]\n"
    "v_mfma_f32_16x16x32_bf16 a[96:99], v[160:163], v[220:223], a[96:99]\n"
    "v_mfma_f32_16x16x32_bf16 a[4:7], v[164:167], v[208:211], a[4:7]\n"
    "v_mfma_f32_16x16x32_bf16 a[36:39], v[164:167], v[212:215], a[36:39]\n"
    "v_mfma_f32_16x16x32_bf16 a[68:71], v[164:167], v[216:219], a[68:71]\n"
    "v_mfma_f32_16x16x32_bf16 a[100:103], v[164:167], v[220:223], a[100:103]\n"
    "v_mfma_f32_16x16x32_bf16 a[8:11], v[168:171], v[208:211], a[8:11]\n"
    "v_mfma_f32_16x16x32_bf16 a[40:43], v[168:171], v[212:215], a[40:43]\n"
    "v_mfma_f32_16x16x32_bf16 a[72:75], v[168:171], v[216:219], a[72:75]\n"
    "v_mfma_f32_16x16x32_bf16 a[104:107], v[168:171], v[220:223], a[104:107]\n"
    "v_mfma_f32_16x16x32_bf16 a[12:15], v[172:175], v[208:211], a[12:15]\n"
    "v_mfma_f32_16x16x32_bf16 a[44:47], v[172:175], v[212:215], a[44:47]\n"
    "v_mfma_f32_16x16x32_bf16 a[76:79], v[172:175], v[216:219], a[76:79]\n"
    "v_mfma_f32_16x16x32_bf16 a[108:111], v[172:175], v[220:223], a[108:111]\n"
    "v_mfma_f32_16x16x32_bf16 a[16:19], v[176:179], v[208:211], a[16:19]\n"
    "v_mfma_f32_16x16x32_bf16 a[48:51], v[176:179], v[212:215], a[48:51]\n"
    "v_mfma_f32_16x16x32_bf16 a[80:83], v[176:179], v[216:219], a[80:83]\n"
    "v_mfma_f32_16x16x32_bf16 a[112:115], v[176:179], v[220:223], a[112:115]\n"
    "v_mfma_f32_16x16x32_bf16 a[20:23], v[180:183], v[208:211], a[20:23]\n"
    "v_mfma_f32_16x16x32_bf16 a[52:55], v[180:183], v[212:215], a[52:55]\n"
    "v_mfma_f32_16x16x32_bf16 a[84:87], v[180:183], v[216:219], a[84:87]\n"
    "v_mfma_f32_16x16x32_bf16 a[116:119], v[180:183], v[220:223], a[116:119]\n"
    "v_mfma_f32_16x16x32_bf16 a[24:27], v[184:187], v[208:211], a[24:27]\n"
    "v_mfma_f32_16x16x32_bf16 a[56:59], v[184:187], v[212:215], a[56:59]\n"
    "v_mfma_f32_16x16x32_bf16 a[88:91], v[184:187], v[216:219], a[88:91]\n"
    "v_mfma_f32_16x16x32_bf16 a[120:123], v[184:187], v[220:223], a[120:123]\n"
    "v_mfma_f32_16x16x32_bf16 a[28:31], v[188:191], v[208:211], a[28:31]\n"
    "v_mfma_f32_16x16x32_bf16 a[60:63], v[188:191], v[212:215], a[60:63]\n"
    "v_mfma_f32_16x16x32_bf16 a[92:95], v[188:191], v[216:219], a[92:95]\n"
    "v_mfma_f32_16x16x32_bf16 a[124:127], v[188:191], v[220:223], a[124:127]\n"
    "s_waitcnt lgkmcnt(0)\n"
    "s_barrier\n"
    "s_add_u32 m0, s72, 32768\n"
    "s_add_u32 s76, s71, 32768\n"
    "buffer_load_dwordx4 v224, s[36:39], s76 offen offset:0 nt lds\n"
    "buffer_load_dwordx4 v225, s[36:39], s76 offen offset:1024 nt lds\n"
    "buffer_load_dwordx4 v226, s[36:39], s76 offen offset:2048 nt lds\n"
    "buffer_load_dwordx4 v227, s[36:39], s76 offen offset:3072 nt lds\n"
    "v_and_b32 v252, 1, v252\n"
    "v_cmp_ne_u32 vcc, 0, v252\n"
    "s_cmp_lg_u32 s66, 0\n"
    "s_cselect_b64 s[64:65], vcc, s[64:65]\n"
    "s_mov_b32 s66, 0\n"
    "s_add_u32 s70, s71, 49152\n"
    "s_add_u32 s71, s71, 32768\n"
    "v_mov_b32 v252, 0x53800000\n"
    "v_mov_b32 v253, v228\n"
    "s_mov_b32 s99, 0\n"
    "s_mov_b32 s83, 32768\n"
    "s_lshl_b32 s76, s68, 14\n"
    "s_cselect_b32 s77, 2, 1\n"
    "s_mul_i32 s77, s77, 49152\n"
    "s_add_u32 s76, s76, s77\n"
    "s_add_u32 s76, s76, s69\n"
    "v_add_u32 v255, s76, v229\n"
    "v_pk_mul_f32 v[0:1], v[0:1], s[84:85]\n"
    "v_pk_mul_f32 v[2:3], v[2:3], s[84:85]\n"
    "v_pk_mul_f32 v[4:5], v[4:5], s[84:85]\n"
    "v_pk_mul_f32 v[6:7], v[6:7], s[84:85]\n"
    "v_cvt_pk_bf16_f32 v0, v0, v1\n"
    "v_cvt_pk_bf16_f32 v1, v2, v3\n"
    "v_cvt_pk_bf16_f32 v2, v4, v5\n"
    "v_cvt_pk_bf16_f32 v3, v6, v7\n"
    "v_accvgpr_write_b32 a128, v0\n"
    "v_accvgpr_write_b32 a129, v1\n"
    "v_accvgpr_write_b32 a130, v2\n"
    "v_accvgpr_write_b32 a131, v3\n"
    "v_pk_mul_f32 v[8:9], v[8:9], s[84:85]\n"
    "v_pk_mul_f32 v[10:11], v[10:11], s[84:85]\n"
    "v_pk_mul_f32 v[12:13], v[12:13], s[84:85]\n"
    "v_pk_mul_f32 v[14:15], v[14:15], s[84:85]\n"
    "v_cvt_pk_bf16_f32 v8, v8, v9\n"
    "v_cvt_pk_bf16_f32 v9, v10, v11\n"
    "v_cvt_pk_bf16_f32 v10, v12, v13\n"
    "v_cvt_pk_bf16_f32 v11, v14, v15\n"
    "v_accvgpr_write_b32 a132, v8\n"
    "v_accvgpr_write_b32 a133, v9\n"
    "v_accvgpr_write_b32 a134, v10\n"
    "v_accvgpr_write_b32 a135, v11\n"
    "v_pk_mul_f32 v[16:17], v[16:17], s[84:85]\n"
    "v_pk_mul_f32 v[18:19], v[18:19], s[84:85]\n"
    "v_pk_mul_f32 v[20:21], v[20:21], s[84:85]\n"
    "v_pk_mul_f32 v[22:23], v[22:23], s[84:85]\n"
    "v_cvt_pk_bf16_f32 v16, v16, v17\n"
    "v_cvt_pk_bf16_f32 v17, v18, v19\n"
    "v_cvt_pk_bf16_f32 v18, v20, v21\n"
    "v_cvt_pk_bf16_f32 v19, v22, v23\n"
    "v_accvgpr_write_b32 a136, v16\n"
    "v_accvgpr_write_b32 a137, v17\n"
    "v_accvgpr_write_b32 a138, v18\n"
    "v_accvgpr_write_b32 a139, v19\n"
    "v_pk_mul_f32 v[24:25], v[24:25], s[84:85]\n"
    "v_pk_mul_f32 v[26:27], v[26:27], s[84:85]\n"
    "v_pk_mul_f32 v[28:29], v[28:29], s[84:85]\n"
    "v_pk_mul_f32 v[30:31], v[30:31], s[84:85]\n"
    "v_cvt_pk_bf16_f32 v24, v24, v25\n"
    "v_cvt_pk_bf16_f32 v25, v26, v27\n"
    "v_cvt_pk_bf16_f32 v26, v28, v29\n"
    "v_cvt_pk_bf16_f32 v27, v30, v31\n"
    "v_accvgpr_write_b32 a140, v24\n"
    "v_accvgpr_write_b32 a141, v25\n"
    "v_accvgpr_write_b32 a142, v26\n"
    "v_accvgpr_write_b32 a143, v27\n"
    "v_pk_mul_f32 v[32:33], v[32:33], s[84:85]\n"
    "v_pk_mul_f32 v[34:35], v[34:35], s[84:85]\n"
    "v_pk_mul_f32 v[36:37], v[36:37], s[84:85]\n"
    "v_pk_mul_f32 v[38:39], v[38:39], s[84:85]\n"
    "v_cvt_pk_bf16_f32 v32, v32, v33\n"
    "v_cvt_pk_bf16_f32 v33, v34, v35\n"
    "v_cvt_pk_bf16_f32 v34, v36, v37\n"
    "v_cvt_pk_bf16_f32 v35, v38, v39\n"
    "v_accvgpr_write_b32 a144, v32\n"
    "v_accvgpr_write_b32 a145, v33\n"
    "v_accvgpr_write_b32 a146, v34\n"
    "v_accvgpr_write_b32 a147, v35\n"
    "v_pk_mul_f32 v[40:41], v[40:41], s[84:85]\n"
    "v_pk_mul_f32 v[42:43], v[42:43], s[84:85]\n"
    "v_pk_mul_f32 v[44:45], v[44:45], s[84:85]\n"
    "v_pk_mul_f32 v[46:47], v[46:47], s[84:85]\n"
    "v_cvt_pk_bf16_f32 v40, v40, v41\n"
    "v_cvt_pk_bf16_f32 v41, v42, v43\n"
    "v_cvt_pk_bf16_f32 v42, v44, v45\n"
    "v_cvt_pk_bf16_f32 v43, v46, v47\n"
    "v_accvgpr_write_b32 a148, v40\n"
    "v_accvgpr_write_b32 a149, v41\n"
    "v_accvgpr_write_b32 a150, v42\n"
    "v_accvgpr_write_b32 a151, v43\n"
    "v_pk_mul_f32 v[48:49], v[48:49], s[84:85]\n"
    "v_pk_mul_f32 v[50:51], v[50:51], s[84:85]\n"
    "v_pk_mul_f32 v[52:53], v[52:53], s[84:85]\n"
    "v_pk_mul_f32 v[54:55], v[54:55], s[84:85]\n"
    "v_cvt_pk_bf16_f32 v48, v48, v49\n"
    "v_cvt_pk_bf16_f32 v49, v50, v51\n"
    "v_cvt_pk_bf16_f32 v50, v52, v53\n"
    "v_cvt_pk_bf16_f32 v51, v54, v55\n"
    "v_accvgpr_write_b32 a152, v48\n"
    "v_accvgpr_write_b32 a153, v49\n"
    "v_accvgpr_write_b32 a154, v50\n"
    "v_accvgpr_write_b32 a155, v51\n"
    "v_pk_mul_f32 v[56:57], v[56:57], s[84:85]\n"
    "v_pk_mul_f32 v[58:59], v[58:59], s[84:85]\n"
    "v_pk_mul_f32 v[60:61], v[60:61], s[84:85]\n"
    "v_pk_mul_f32 v[62:63], v[62:63], s[84:85]\n"
    "v_cvt_pk_bf16_f32 v56, v56, v57\n"
    "v_cvt_pk_bf16_f32 v57, v58, v59\n"
    "v_cvt_pk_bf16_f32 v58, v60, v61\n"
    "v_cvt_pk_bf16_f32 v59, v62, v63\n"
    "v_accvgpr_write_b32 a156, v56\n"
    "v_accvgpr_write_b32 a157, v57\n"
    "v_accvgpr_write_b32 a158, v58\n"
    "v_accvgpr_write_b32 a159, v59\n"
    "v_pk_mul_f32 v[64:65], v[64:65], s[84:85]\n"
    "v_pk_mul_f32 v[66:67], v[66:67], s[84:85]\n"
    "v_pk_mul_f32 v[68:69], v[68:69], s[84:85]\n"
    "v_pk_mul_f32 v[70:71], v[70:71], s[84:85]\n"
    "v_cvt_pk_bf16_f32 v64, v64, v65\n"
    "v_cvt_pk_bf16_f32 v65, v66, v67\n"
    "v_cvt_pk_bf16_f32 v66, v68, v69\n"
    "v_cvt_pk_bf16_f32 v67, v70, v71\n"
    "v_accvgpr_write_b32 a160, v64\n"
    "v_accvgpr_write_b32 a161, v65\n"
    "v_accvgpr_write_b32 a162, v66\n"
    "v_accvgpr_write_b32 a163, v67\n"
    "v_pk_mul_f32 v[72:73], v[72:73], s[84:85]\n"
    "v_pk_mul_f32 v[74:75], v[74:75], s[84:85]\n"
    "v_pk_mul_f32 v[76:77], v[76:77], s[84:85]\n"
    "v_pk_mul_f32 v[78:79], v[78:79], s[84:85]\n"
    "v_cvt_pk_bf16_f32 v72, v72, v73\n"
    "v_cvt_pk_bf16_f32 v73, v74, v75\n"
    "v_cvt_pk_bf16_f32 v74, v76, v77\n"
    "v_cvt_pk_bf16_f32 v75, v78, v79\n"
    "v_accvgpr_write_b32 a164, v72\n"
    "v_accvgpr_write_b32 a165, v73\n"
    "v_accvgpr_write_b32 a166, v74\n"
    "v_accvgpr_write_b32 a167, v75\n"
    "v_pk_mul_f32 v[80:81], v[80:81], s[84:85]\n"
    "v_pk_mul_f32 v[82:83], v[82:83], s[84:85]\n"
    "v_pk_mul_f32 v[84:85], v[84:85], s[84:85]\n"
    "v_pk_mul_f32 v[86:87], v[86:87], s[84:85]\n"
    "v_cvt_pk_bf16_f32 v80, v80, v81\n"
    "v_cvt_pk_bf16_f32 v81, v82, v83\n"
    "v_cvt_pk_bf16_f32 v82, v84, v85\n"
    "v_cvt_pk_bf16_f32 v83, v86, v87\n"
    "v_accvgpr_write_b32 a168, v80\n"
    "v_accvgpr_write_b32 a169, v81\n"
    "v_accvgpr_write_b32 a170, v82\n"
    "v_accvgpr_write_b32 a171, v83\n"
    "v_pk_mul_f32 v[88:89], v[88:89], s[84:85]\n"
    "v_pk_mul_f32 v[90:91], v[90:91], s[84:85]\n"
    "v_pk_mul_f32 v[92:93], v[92:93], s[84:85]\n"
    "v_pk_mul_f32 v[94:95], v[94:95], s[84:85]\n"
    "v_cvt_pk_bf16_f32 v88, v88, v89\n"
    "v_cvt_pk_bf16_f32 v89, v90, v91\n"
    "v_cvt_pk_bf16_f32 v90, v92, v93\n"
    "v_cvt_pk_bf16_f32 v91, v94, v95\n"
    "v_accvgpr_write_b32 a172, v88\n"
    "v_accvgpr_write_b32 a173, v89\n"
    "v_accvgpr_write_b32 a174, v90\n"
    "v_accvgpr_write_b32 a175, v91\n"
    "v_pk_mul_f32 v[96:97], v[96:97], s[84:85]\n"
    "v_pk_mul_f32 v[98:99], v[98:99], s[84:85]\n"
    "v_pk_mul_f32 v[100:101], v[100:101], s[84:85]\n"
    "v_pk_mul_f32 v[102:103], v[102:103], s[84:85]\n"
    "v_cvt_pk_bf16_f32 v96, v96, v97\n"
    "v_cvt_pk_bf16_f32 v97, v98, v99\n"
    "v_cvt_pk_bf16_f32 v98, v100, v101\n"
    "v_cvt_pk_bf16_f32 v99, v102, v103\n"
    "v_accvgpr_write_b32 a176, v96\n"
    "v_accvgpr_write_b32 a177, v97\n"
    "v_accvgpr_write_b32 a178, v98\n"
    "v_accvgpr_write_b32 a179, v99\n"
    "v_pk_mul_f32 v[104:105], v[104:105], s[84:85]\n"
    "v_pk_mul_f32 v[106:107], v[106:107], s[84:85]\n"
    "v_pk_mul_f32 v[108:109], v[108:109], s[84:85]\n"
    "v_pk_mul_f32 v[110:111], v[110:111], s[84:85]\n"
    "v_cvt_pk_bf16_f32 v104, v104, v105\n"
    "v_cvt_pk_bf16_f32 v105, v106, v107\n"
    "v_cvt_pk_bf16_f32 v106, v108, v109\n"
    "v_cvt_pk_bf16_f32 v107, v110, v111\n"
    "v_accvgpr_write_b32 a180, v104\n"
    "v_accvgpr_write_b32 a181, v105\n"
    "v_accvgpr_write_b32 a182, v106\n"
    "v_accvgpr_write_b32 a183, v107\n"
    "v_pk_mul_f32 v[112:113], v[112:113], s[84:85]\n"
    "v_pk_mul_f32 v[114:115], v[114:115], s[84:85]\n"
    "v_pk_mul_f32 v[116:117], v[116:117], s[84:85]\n"
    "v_pk_mul_f32 v[118:119], v[118:119], s[84:85]\n"
    "v_cvt_pk_bf16_f32 v112, v112, v113\n"
    "v_cvt_pk_bf16_f32 v113, v114, v115\n"
    "v_cvt_pk_bf16_f32 v114, v116, v117\n"
    "v_cvt_pk_bf16_f32 v115, v118, v119\n"
    "v_accvgpr_write_b32 a184, v112\n"
    "v_accvgpr_write_b32 a185, v113\n"
    "v_accvgpr_write_b32 a186, v114\n"
    "v_accvgpr_write_b32 a187, v115\n"
    "v_pk_mul_f32 v[120:121], v[120:121], s[84:85]\n"
    "v_pk_mul_f32 v[122:123], v[122:123], s[84:85]\n"
    "v_pk_mul_f32 v[124:125], v[124:125], s[84:85]\n"
    "v_pk_mul_f32 v[126:127], v[126:127], s[84:85]\n"
    "v_cvt_pk_bf16_f32 v120, v120, v121\n"
    "v_cvt_pk_bf16_f32 v121, v122, v123\n"
    "v_cvt_pk_bf16_f32 v122, v124, v125\n"
    "v_cvt_pk_bf16_f32 v123, v126, v127\n"
    "v_accvgpr_write_b32 a188, v120\n"
    "v_accvgpr_write_b32 a189, v121\n"
    "v_accvgpr_write_b32 a190, v122\n"
    "v_accvgpr_write_b32 a191, v123\n"
    "s_waitcnt vmcnt(16)\n"
    "s_barrier\n"
    "ds_read_b128 a[192:195], v253 offset:0\n"
    "ds_read_b128 a[196:199], v253 offset:4096\n"
    "ds_read_b128 a[200:203], v253 offset:8192\n"
    "ds_read_b128 a[204:207], v253 offset:12288\n"
    "ds_read_b128 a[208:211], v253 offset:512\n"
    "ds_read_b128 a[212:215], v253 offset:4608\n"
    "ds_read_b128 a[216:219], v253 offset:8704\n"
    "ds_read_b128 a[220:223], v253 offset:12800\n"
    "ds_read_b128 a[224:227], v253 offset:1024\n"
    "ds_read_b128 a[228:231], v253 offset:5120\n"
    "ds_read_b128 a[232:235], v253 offset:9216\n"
    "ds_read_b128 a[236:239], v253 offset:13312\n"
    "ds_read_b128 a[240:243], v253 offset:1536\n"
    "ds_read_b128 a[244:247], v253 offset:5632\n"
    "ds_read_b128 a[248:251], v253 offset:9728\n"
    "ds_read_b128 a[252:255], v253 offset:13824\n"
    "v_accvgpr_read_b32 v128, a0\n"
    "v_accvgpr_read_b32 v129, a1\n"
    "v_accvgpr_read_b32 v130, a2\n"
    "v_accvgpr_read_b32 v131, a3\n"
    "v_accvgpr_read_b32 v132, a4\n"
    "v_accvgpr_read_b32 v133, a5\n"
    "v_accvgpr_read_b32 v134, a6\n"
    "v_accvgpr_read_b32 v135, a7\n"
    "v_pk_mul_f32 v[128:129], v[128:129], s[84:85]\n"
    "v_pk_mul_f32 v[130:131], v[130:131], s[84:85]\n"
    "v_pk_mul_f32 v[132:133], v[132:133], s[84:85]\n"
    "v_pk_mul_f32 v[134:135], v[134:135], s[84:85]\n"
    "v_cvt_pk_bf16_f32 v128, v128, v129\n"
    "v_cvt_pk_bf16_f32 v129, v130, v131\n"
    "v_cvt_pk_bf16_f32 v130, v132, v133\n"
    "v_cvt_pk_bf16_f32 v131, v134, v135\n"
    "ds_write_b128 v255, v[128:131] offset:0\n"
    "v_accvgpr_read_b32 v136, a8\n"
    "v_accvgpr_read_b32 v137, a9\n"
    "v_accvgpr_read_b32 v138, a10\n"
    "v_accvgpr_read_b32 v139, a11\n"
    "v_accvgpr_read_b32 v140, a12\n"
    "v_accvgpr_read_b32 v141, a13\n"
    "v_accvgpr_read_b32 v142, a14\n"
    "v_accvgpr_read_b32 v143, a15\n"
    "v_pk_mul_f32 v[136:137], v[136:137], s[84:85]\n"
    "v_pk_mul_f32 v[138:139], v[138:139], s[84:85]\n"
    "v_pk_mul_f32 v[140:141], v[140:141], s[84:85]\n"
    "v_pk_mul_f32 v[142:143], v[142:143], s[84:85]\n"
    "v_cvt_pk_bf16_f32 v136, v136, v137\n"
    "v_cvt_pk_bf16_f32 v137, v138, v139\n"
    "v_cvt_pk_bf16_f32 v138, v140, v141\n"
    "v_cvt_pk_bf16_f32 v139, v142, v143\n"
    "ds_write_b128 v255, v[136:139] offset:1024\n"
    "v_accvgpr_read_b32 v144, a16\n"
    "v_accvgpr_read_b32 v145, a17\n"
    "v_accvgpr_read_b32 v146, a18\n"
    "v_accvgpr_read_b32 v147, a19\n"
    "v_accvgpr_read_b32 v148, a20\n"
    "v_accvgpr_read_b32 v149, a21\n"
    "v_accvgpr_read_b32 v150, a22\n"
    "v_accvgpr_read_b32 v151, a23\n"
    "v_pk_mul_f32 v[144:145], v[144:145], s[84:85]\n"
    "v_pk_mul_f32 v[146:147], v[146:147], s[84:85]\n"
    "v_pk_mul_f32 v[148:149], v[148:149], s[84:85]\n"
    "v_pk_mul_f32 v[150:151], v[150:151], s[84:85]\n"
    "v_cvt_pk_bf16_f32 v144, v144, v145\n"
    "v_cvt_pk_bf16_f32 v145, v146, v147\n"
    "v_cvt_pk_bf16_f32 v146, v148, v149\n"
    "v_cvt_pk_bf16_f32 v147, v150, v151\n"
    "ds_write_b128 v255, v[144:147] offset:2048\n"
    "v_accvgpr_read_b32 v152, a24\n"
    "v_accvgpr_read_b32 v153, a25\n"
    "v_accvgpr_read_b32 v154, a26\n"
    "v_accvgpr_read_b32 v155, a27\n"
    "v_accvgpr_read_b32 v156, a28\n"
    "v_accvgpr_read_b32 v157, a29\n"
    "v_accvgpr_read_b32 v158, a30\n"
    "v_accvgpr_read_b32 v159, a31\n"
    "v_pk_mul_f32 v[152:153], v[152:153], s[84:85]\n"
    "v_pk_mul_f32 v[154:155], v[154:155], s[84:85]\n"
    "v_pk_mul_f32 v[156:157], v[156:157], s[84:85]\n"
    "v_pk_mul_f32 v[158:159], v[158:159], s[84:85]\n"
    "v_cvt_pk_bf16_f32 v152, v152, v153\n"
    "v_cvt_pk_bf16_f32 v153, v154, v155\n"
    "v_cvt_pk_bf16_f32 v154, v156, v157\n"
    "v_cvt_pk_bf16_f32 v155, v158, v159\n"
    "ds_write_b128 v255, v[152:155] offset:3072\n"
    "v_accvgpr_read_b32 v160, a32\n"
    "v_accvgpr_read_b32 v161, a33\n"
    "v_accvgpr_read_b32 v162, a34\n"
    "v_accvgpr_read_b32 v163, a35\n"
    "v_accvgpr_read_b32 v164, a36\n"
    "v_accvgpr_read_b32 v165, a37\n"
    "v_accvgpr_read_b32 v166, a38\n"
    "v_accvgpr_read_b32 v167, a39\n"
    "v_pk_mul_f32 v[160:161], v[160:161], s[84:85]\n"
    "v_pk_mul_f32 v[162:163], v[162:163], s[84:85]\n"
    "v_pk_mul_f32 v[164:165], v[164:165], s[84:85]\n"
    "v_pk_mul_f32 v[166:167], v[166:167], s[84:85]\n"
    "v_cvt_pk_bf16_f32 v160, v160, v161\n"
    "v_cvt_pk_bf16_f32 v161, v162, v163\n"
    "v_cvt_pk_bf16_f32 v162, v164, v165\n"
    "v_cvt_pk_bf16_f32 v163, v166, v167\n"
    "ds_write_b128 v255, v[160:163] offset:4096\n"
    "v_accvgpr_read_b32 v168, a40\n"
    "v_accvgpr_read_b32 v169, a41\n"
    "v_accvgpr_read_b32 v170, a42\n"
    "v_accvgpr_read_b32 v171, a43\n"
    "v_accvgpr_read_b32 v172, a44\n"
    "v_accvgpr_read_b32 v173, a45\n"
    "v_accvgpr_read_b32 v174, a46\n"
    "v_accvgpr_read_b32 v175, a47\n"
    "v_pk_mul_f32 v[168:169], v[168:169], s[84:85]\n"
    "v_pk_mul_f32 v[170:171], v[170:171], s[84:85]\n"
    "v_pk_mul_f32 v[172:173], v[172:173], s[84:85]\n"
    "v_pk_mul_f32 v[174:175], v[174:175], s[84:85]\n"
    "v_cvt_pk_bf16_f32 v168, v168, v169\n"
    "v_cvt_pk_bf16_f32 v169, v170, v171\n"
    "v_cvt_pk_bf16_f32 v170, v172, v173\n"
    "v_cvt_pk_bf16_f32 v171, v174, v175\n"
    "ds_write_b128 v255, v[168:171] offset:5120\n"
    "v_accvgpr_read_b32 v176, a48\n"
    "v_accvgpr_read_b32 v177, a49\n"
    "v_accvgpr_read_b32 v178, a50\n"
    "v_accvgpr_read_b32 v179, a51\n"
    "v_accvgpr_read_b32 v180, a52\n"
    "v_accvgpr_read_b32 v181, a53\n"
    "v_accvgpr_read_b32 v182, a54\n"
    "v_accvgpr_read_b32 v183, a55\n"
    "v_pk_mul_f32 v[176:177], v[176:177], s[84:85]\n"
    "v_pk_mul_f32 v[178:179], v[178:179], s[84:85]\n"
    "v_pk_mul_f32 v[180:181], v[180:181], s[84:85]\n"
    "v_pk_mul_f32 v[182:183], v[182:183], s[84:85]\n"
    "v_cvt_pk_bf16_f32 v176, v176, v177\n"
    "v_cvt_pk_bf16_f32 v177, v178, v179\n"
    "v_cvt_pk_bf16_f32 v178, v180, v181\n"
    "v_cvt_pk_bf16_f32 v179, v182, v183\n"
    "ds_write_b128 v255, v[176:179] offset:6144\n"
    "v_accvgpr_read_b32 v184, a56\n"
    "v_accvgpr_read_b32 v185, a57\n"
    "v_accvgpr_read_b32 v186, a58\n"
    "v_accvgpr_read_b32 v187, a59\n"
    "v_accvgpr_read_b32 v188, a60\n"
    "v_accvgpr_read_b32 v189, a61\n"
    "v_accvgpr_read_b32 v190, a62\n"
    "v_accvgpr_read_b32 v191, a63\n"
    "v_pk_mul_f32 v[184:185], v[184:185], s[84:85]\n"
    "v_pk_mul_f32 v[186:187], v[186:187], s[84:85]\n"
    "v_pk_mul_f32 v[188:189], v[188:189], s[84:85]\n"
    "v_pk_mul_f32 v[190:191], v[190:191], s[84:85]\n"
    "v_cvt_pk_bf16_f32 v184, v184, v185\n"
    "v_cvt_pk_bf16_f32 v185, v186, v187\n"
    "v_cvt_pk_bf16_f32 v186, v188, v189\n"
    "v_cvt_pk_bf16_f32 v187, v190, v191\n"
    "ds_write_b128 v255, v[184:187] offset:7168\n"
    "v_accvgpr_read_b32 v128, a64\n"
    "v_accvgpr_read_b32 v129, a65\n"
    "v_accvgpr_read_b32 v130, a66\n"
    "v_accvgpr_read_b32 v131, a67\n"
    "v_accvgpr_read_b32 v132, a68\n"
    "v_accvgpr_read_b32 v133, a69\n"
    "v_accvgpr_read_b32 v134, a70\n"
    "v_accvgpr_read_b32 v135, a71\n"
    "v_pk_mul_f32 v[128:129], v[128:129], s[84:85]\n"
    "v_pk_mul_f32 v[130:131], v[130:131], s[84:85]\n"
    "v_pk_mul_f32 v[132:133], v[132:133], s[84:85]\n"
    "v_pk_mul_f32 v[134:135], v[134:135], s[84:85]\n"
    "v_cvt_pk_bf16_f32 v128, v128, v129\n"
    "v_cvt_pk_bf16_f32 v129, v130, v131\n"
    "v_cvt_pk_bf16_f32 v130, v132, v133\n"
    "v_cvt_pk_bf16_f32 v131, v134, v135\n"
    "ds_write_b128 v255, v[128:131] offset:8192\n"
    "v_accvgpr_read_b32 v136, a72\n"
    "v_accvgpr_read_b32 v137, a73\n"
    "v_accvgpr_read_b32 v138, a74\n"
    "v_accvgpr_read_b32 v139, a75\n"
    "v_accvgpr_read_b32 v140, a76\n"
    "v_accvgpr_read_b32 v141, a77\n"
    "v_accvgpr_read_b32 v142, a78\n"
    "v_accvgpr_read_b32 v143, a79\n"
    "v_pk_mul_f32 v[136:137], v[136:137], s[84:85]\n"
    "v_pk_mul_f32 v[138:139], v[138:139], s[84:85]\n"
    "v_pk_mul_f32 v[140:141], v[140:141], s[84:85]\n"
    "v_pk_mul_f32 v[142:143], v[142:143], s[84:85]\n"
    "v_cvt_pk_bf16_f32 v136, v136, v137\n"
    "v_cvt_pk_bf16_f32 v137, v138, v139\n"
    "v_cvt_pk_bf16_f32 v138, v140, v141\n"
    "v_cvt_pk_bf16_f32 v139, v142, v143\n"
    "ds_write_b128 v255, v[136:139] offset:9216\n"
    "v_accvgpr_read_b32 v144, a80\n"
    "v_accvgpr_read_b32 v145, a81\n"
    "v_accvgpr_read_b32 v146, a82\n"
    "v_accvgpr_read_b32 v147, a83\n"
    "v_accvgpr_read_b32 v148, a84\n"
    "v_accvgpr_read_b32 v149, a85\n"
    "v_accvgpr_read_b32 v150, a86\n"
    "v_accvgpr_read_b32 v151, a87\n"
    "v_pk_mul_f32 v[144:145], v[144:145], s[84:85]\n"
    "v_pk_mul_f32 v[146:147], v[146:147], s[84:85]\n"
    "v_pk_mul_f32 v[148:149], v[148:149], s[84:85]\n"
    "v_pk_mul_f32 v[150:151], v[150:151], s[84:85]\n"
    "v_cvt_pk_bf16_f32 v144, v144, v145\n"
    "v_cvt_pk_bf16_f32 v145, v146, v147\n"
    "v_cvt_pk_bf16_f32 v146, v148, v149\n"
    "v_cvt_pk_bf16_f32 v147, v150, v151\n"
    "ds_write_b128 v255, v[144:147] offset:10240\n"
    "v_accvgpr_read_b32 v152, a88\n"
    "v_accvgpr_read_b32 v153, a89\n"
    "v_accvgpr_read_b32 v154, a90\n"
    "v_accvgpr_read_b32 v155, a91\n"
    "v_accvgpr_read_b32 v156, a92\n"
    "v_accvgpr_read_b32 v157, a93\n"
    "v_accvgpr_read_b32 v158, a94\n"
    "v_accvgpr_read_b32 v159, a95\n"
    "v_pk_mul_f32 v[152:153], v[152:153], s[84:85]\n"
    "v_pk_mul_f32 v[154:155], v[154:155], s[84:85]\n"
    "v_pk_mul_f32 v[156:157], v[156:157], s[84:85]\n"
    "v_pk_mul_f32 v[158:159], v[158:159], s[84:85]\n"
    "v_cvt_pk_bf16_f32 v152, v152, v153\n"
    "v_cvt_pk_bf16_f32 v153, v154, v155\n"
    "v_cvt_pk_bf16_f32 v154, v156, v157\n"
    "v_cvt_pk_bf16_f32 v155, v158, v159\n"
    "ds_write_b128 v255, v[152:155] offset:11264\n"
    "v_accvgpr_read_b32 v160, a96\n"
    "v_accvgpr_read_b32 v161, a97\n"
    "v_accvgpr_read_b32 v162, a98\n"
    "v_accvgpr_read_b32 v163, a99\n"
    "v_accvgpr_read_b32 v164, a100\n"
    "v_accvgpr_read_b32 v165, a101\n"
    "v_accvgpr_read_b32 v166, a102\n"
    "v_accvgpr_read_b32 v167, a103\n"
    "v_pk_mul_f32 v[160:161], v[160:161], s[84:85]\n"
    "v_pk_mul_f32 v[162:163], v[162:163], s[84:85]\n"
    "v_pk_mul_f32 v[164:165], v[164:165], s[84:85]\n"
    "v_pk_mul_f32 v[166:167], v[166:167], s[84:85]\n"
    "v_cvt_pk_bf16_f32 v160, v160, v161\n"
    "v_cvt_pk_bf16_f32 v161, v162, v163\n"
    "v_cvt_pk_bf16_f32 v162, v164, v165\n"
    "v_cvt_pk_bf16_f32 v163, v166, v167\n"
    "ds_write_b128 v255, v[160:163] offset:12288\n"
    "v_accvgpr_read_b32 v168, a104\n"
    "v_accvgpr_read_b32 v169, a105\n"
    "v_accvgpr_read_b32 v170, a106\n"
    "v_accvgpr_read_b32 v171, a107\n"
    "v_accvgpr_read_b32 v172, a108\n"
    "v_accvgpr_read_b32 v173, a109\n"
    "v_accvgpr_read_b32 v174, a110\n"
    "v_accvgpr_read_b32 v175, a111\n"
    "v_pk_mul_f32 v[168:169], v[168:169], s[84:85]\n"
    "v_pk_mul_f32 v[170:171], v[170:171], s[84:85]\n"
    "v_pk_mul_f32 v[172:173], v[172:173], s[84:85]\n"
    "v_pk_mul_f32 v[174:175], v[174:175], s[84:85]\n"
    "v_cvt_pk_bf16_f32 v168, v168, v169\n"
    "v_cvt_pk_bf16_f32 v169, v170, v171\n"
    "v_cvt_pk_bf16_f32 v170, v172, v173\n"
    "v_cvt_pk_bf16_f32 v171, v174, v175\n"
    "ds_write_b128 v255, v[168:171] offset:13312\n"
    "v_accvgpr_read_b32 v176, a112\n"
    "v_accvgpr_read_b32 v177, a113\n"
    "v_accvgpr_read_b32 v178, a114\n"
    "v_accvgpr_read_b32 v179, a115\n"
    "v_accvgpr_read_b32 v180, a116\n"
    "v_accvgpr_read_b32 v181, a117\n"
    "v_accvgpr_read_b32 v182, a118\n"
    "v_accvgpr_read_b32 v183, a119\n"
    "v_pk_mul_f32 v[176:177], v[176:177], s[84:85]\n"
    "v_pk_mul_f32 v[178:179], v[178:179], s[84:85]\n"
    "v_pk_mul_f32 v[180:181], v[180:181], s[84:85]\n"
    "v_pk_mul_f32 v[182:183], v[182:183], s[84:85]\n"
    "v_cvt_pk_bf16_f32 v176, v176, v177\n"
    "v_cvt_pk_bf16_f32 v177, v178, v179\n"
    "v_cvt_pk_bf16_f32 v178, v180, v181\n"
    "v_cvt_pk_bf16_f32 v179, v182, v183\n"
    "ds_write_b128 v255, v[176:179] offset:14336\n"
    "v_accvgpr_read_b32 v184, a120\n"
    "v_accvgpr_read_b32 v185, a121\n"
    "v_accvgpr_read_b32 v186, a122\n"
    "v_accvgpr_read_b32 v187, a123\n"
    "v_accvgpr_read_b32 v188, a124\n"
    "v_accvgpr_read_b32 v189, a125\n"
    "v_accvgpr_read_b32 v190, a126\n"
    "v_accvgpr_read_b32 v191, a127\n"
    "v_pk_mul_f32 v[184:185], v[184:185], s[84:85]\n"
    "v_pk_mul_f32 v[186:187], v[186:187], s[84:85]\n"
    "v_pk_mul_f32 v[188:189], v[188:189], s[84:85]\n"
    "v_pk_mul_f32 v[190:191], v[190:191], s[84:85]\n"
    "v_cvt_pk_bf16_f32 v184, v184, v185\n"
    "v_cvt_pk_bf16_f32 v185, v186, v187\n"
    "v_cvt_pk_bf16_f32 v186, v188, v189\n"
    "v_cvt_pk_bf16_f32 v187, v190, v191\n"
    "ds_write_b128 v255, v[184:187] offset:15360\n"
    "s_waitcnt lgkmcnt(0)\n"
    "s_cmp_lg_u32 s75, 0\n"
    "s_cbranch_scc1 SAFE%=\n"
    "v_mfma_f32_16x16x32_bf16 v[0:3], a[192:195], a[128:131], 0\n"
    "v_mfma_f32_16x16x32_bf16 v[8:11], a[192:195], a[144:147], 0\n"
    "v_mfma_f32_16x16x32_bf16 v[16:19], a[192:195], a[160:163], 0\n"
    "v_mfma_f32_16x16x32_bf16 v[24:27], a[192:195], a[176:179], 0\n"
    "v_mfma_f32_16x16x32_bf16 v[4:7], a[196:199], a[128:131], 0\n"
    "v_mfma_f32_16x16x32_bf16 v[12:15], a[196:199], a[144:147], 0\n"
    "v_mfma_f32_16x16x32_bf16 v[20:23], a[196:199], a[160:163], 0\n"
    "v_mfma_f32_16x16x32_bf16 v[28:31], a[196:199], a[176:179], 0\n"
    "v_mfma_f32_16x16x32_bf16 v[32:35], a[200:203], a[128:131], 0\n"
    "v_mfma_f32_16x16x32_bf16 v[40:43], a[200:203], a[144:147], 0\n"
    "v_mfma_f32_16x16x32_bf16 v[48:51], a[200:203], a[160:163], 0\n"
    "v_mfma_f32_16x16x32_bf16 v[56:59], a[200:203], a[176:179], 0\n"
    "v_mfma_f32_16x16x32_bf16 v[36:39], a[204:207], a[128:131], 0\n"
    "v_mfma_f32_16x16x32_bf16 v[44:47], a[204:207], a[144:147], 0\n"
    "v_mfma_f32_16x16x32_bf16 v[52:55], a[204:207], a[160:163], 0\n"
    "v_mfma_f32_16x16x32_bf16 v[60:63], a[204:207], a[176:179], 0\n"
    "v_mfma_f32_16x16x32_bf16 v[0:3], a[208:211], a[132:135], v[0:3]\n"
    "v_mfma_f32_16x16x32_bf16 v[8:11], a[208:211], a[148:151], v[8:11]\n"
    "v_mfma_f32_16x16x32_bf16 v[16:19], a[208:211], a[164:167], v[16:19]\n"
    "v_mfma_f32_16x16x32_bf16 v[24:27], a[208:211], a[180:183], v[24:27]\n"
    "v_mfma_f32_16x16x32_bf16 v[4:7], a[212:215], a[132:135], v[4:7]\n"
    "v_mfma_f32_16x16x32_bf16 v[12:15], a[212:215], a[148:151], v[12:15]\n"
    "v_mfma_f32_16x16x32_bf16 v[20:23], a[212:215], a[164:167], v[20:23]\n"
    "v_mfma_f32_16x16x32_bf16 v[28:31], a[212:215], a[180:183], v[28:31]\n"
    "v_mfma_f32_16x16x32_bf16 v[32:35], a[216:219], a[132:135], v[32:35]\n"
    "v_mfma_f32_16x16x32_bf16 v[40:43], a[216:219], a[148:151], v[40:43]\n"
    "v_mfma_f32_16x16x32_bf16 v[48:51], a[216:219], a[164:167], v[48:51]\n"
    "v_mfma_f32_16x16x32_bf16 v[56:59], a[216:219], a[180:183], v[56:59]\n"
    "v_mfma_f32_16x16x32_bf16 v[36:39], a[220:223], a[132:135], v[36:39]\n"
    "v_mfma_f32_16x16x32_bf16 v[44:47], a[220:223], a[148:151], v[44:47]\n"
    "v_mfma_f32_16x16x32_bf16 v[52:55], a[220:223], a[164:167], v[52:55]\n"
    "v_mfma_f32_16x16x32_bf16 v[60:63], a[220:223], a[180:183], v[60:63]\n"
    "v_mfma_f32_16x16x32_bf16 v[0:3], a[224:227], a[136:139], v[0:3]\n"
    "v_mfma_f32_16x16x32_bf16 v[8:11], a[224:227], a[152:155], v[8:11]\n"
    "v_mfma_f32_16x16x32_bf16 v[16:19], a[224:227], a[168:171], v[16:19]\n"
    "v_mfma_f32_16x16x32_bf16 v[24:27], a[224:227], a[184:187], v[24:27]\n"
    "v_mfma_f32_16x16x32_bf16 v[4:7], a[228:231], a[136:139], v[4:7]\n"
    "v_mfma_f32_16x16x32_bf16 v[12:15], a[228:231], a[152:155], v[12:15]\n"
    "v_mfma_f32_16x16x32_bf16 v[20:23], a[228:231], a[168:171], v[20:23]\n"
    "v_mfma_f32_16x16x32_bf16 v[28:31], a[228:231], a[184:187], v[28:31]\n"
    "v_mfma_f32_16x16x32_bf16 v[32:35], a[232:235], a[136:139], v[32:35]\n"
    "v_mfma_f32_16x16x32_bf16 v[40:43], a[232:235], a[152:155], v[40:43]\n"
    "v_mfma_f32_16x16x32_bf16 v[48:51], a[232:235], a[168:171], v[48:51]\n"
    "v_mfma_f32_16x16x32_bf16 v[56:59], a[232:235], a[184:187], v[56:59]\n"
    "v_mfma_f32_16x16x32_bf16 v[36:39], a[236:239], a[136:139], v[36:39]\n"
    "v_mfma_f32_16x16x32_bf16 v[44:47], a[236:239], a[152:155], v[44:47]\n"
    "v_mfma_f32_16x16x32_bf16 v[52:55], a[236:239], a[168:171], v[52:55]\n"
    "v_mfma_f32_16x16x32_bf16 v[60:63], a[236:239], a[184:187], v[60:63]\n"
    "v_mfma_f32_16x16x32_bf16 v[0:3], a[240:243], a[140:143], v[0:3]\n"
    "v_mfma_f32_16x16x32_bf16 v[8:11], a[240:243], a[156:159], v[8:11]\n"
    "v_mfma_f32_16x16x32_bf16 v[16:19], a[240:243], a[172:175], v[16:19]\n"
    "v_mfma_f32_16x16x32_bf16 v[24:27], a[240:243], a[188:191], v[24:27]\n"
    "v_mfma_f32_16x16x32_bf16 v[4:7], a[244:247], a[140:143], v[4:7]\n"
    "v_mfma_f32_16x16x32_bf16 v[12:15], a[244:247], a[156:159], v[12:15]\n"
    "v_mfma_f32_16x16x32_bf16 v[20:23], a[244:247], a[172:175], v[20:23]\n"
    "v_mfma_f32_16x16x32_bf16 v[28:31], a[244:247], a[188:191], v[28:31]\n"
    "v_mfma_f32_16x16x32_bf16 v[32:35], a[248:251], a[140:143], v[32:35]\n"
    "v_mfma_f32_16x16x32_bf16 v[40:43], a[248:251], a[156:159], v[40:43]\n"
    "v_mfma_f32_16x16x32_bf16 v[48:51], a[248:251], a[172:175], v[48:51]\n"
    "v_mfma_f32_16x16x32_bf16 v[56:59], a[248:251], a[188:191], v[56:59]\n"
    "v_mfma_f32_16x16x32_bf16 v[36:39], a[252:255], a[140:143], v[36:39]\n"
    "v_mfma_f32_16x16x32_bf16 v[44:47], a[252:255], a[156:159], v[44:47]\n"
    "v_mfma_f32_16x16x32_bf16 v[52:55], a[252:255], a[172:175], v[52:55]\n"
    "v_mfma_f32_16x16x32_bf16 v[60:63], a[252:255], a[188:191], v[60:63]\n"
    "s_waitcnt vmcnt(8) lgkmcnt(0)\n"
    "s_barrier\n"
    "s_nop 7\n"
    "s_add_u32 m0, s72, s99\n"
    "s_nop 0\n"
    "buffer_load_dwordx4 v224, s[36:39], s70 offen offset:0 nt lds\n"
    "buffer_load_dwordx4 v225, s[36:39], s70 offen offset:1024 nt lds\n"
    "v_max3_f32 v240, v0, v1, v2\n"
    "v_max3_f32 v241, v8, v9, v10\n"
    "v_max3_f32 v242, v16, v17, v18\n"
    "v_max3_f32 v243, v24, v25, v26\n"
    "v_max3_f32 v240, v240, v3, v4\n"
    "v_max3_f32 v241, v241, v11, v12\n"
    "buffer_load_dwordx4 v226, s[36:39], s70 offen offset:2048 nt lds\n"
    "v_max3_f32 v242, v242, v19, v20\n"
    "v_max3_f32 v243, v243, v27, v28\n"
    "v_max3_f32 v240, v240, v5, v6\n"
    "v_max3_f32 v241, v241, v13, v14\n"
    "v_max3_f32 v242, v242, v21, v22\n"
    "v_max3_f32 v243, v243, v29, v30\n"
    "buffer_load_dwordx4 v227, s[36:39], s70 offen offset:3072 nt lds\n"
    "v_max3_f32 v240, v240, v7, v32\n"
    "v_max3_f32 v241, v241, v15, v40\n"
    "s_add_u32 m0, s73, s83\n"
    "s_add_u32 s70, s70, 16384\n"
    "v_max3_f32 v242, v242, v23, v48\n"
    "v_max3_f32 v243, v243, v31, v56\n"
    "buffer_load_dwordx4 v224, s[40:43], s71 offen offset:0 nt lds\n"
    "v_max3_f32 v240, v240, v33, v34\n"
    "v_max3_f32 v241, v241, v41, v42\n"
    "v_add_u32 v250, s99, v230\n"
    "v_add_u32 v251, s99, v231\n"
    "v_max3_f32 v242, v242, v49, v50\n"
    "v_max3_f32 v243, v243, v57, v58\n"
    "buffer_load_dwordx4 v225, s[40:43], s71 offen offset:1024 nt lds\n"
    "v_max3_f32 v240, v240, v35, v36\n"
    "v_max3_f32 v241, v241, v43, v44\n"
    "s_mov_b32 s83, s99\n"
    "s_add_u32 s99, s99, 16384\n"
    "v_max3_f32 v242, v242, v51, v52\n"
    "v_max3_f32 v243, v243, v59, v60\n"
    "buffer_load_dwordx4 v226, s[40:43], s71 offen offset:2048 nt lds\n"
    "v_max3_f32 v240, v240, v37, v38\n"
    "v_max3_f32 v241, v241, v45, v46\n"
    "s_cmp_eq_u32 s99, 49152\n"
    "s_cselect_b32 s99, 0, s99\n"
    "v_max3_f32 v242, v242, v53, v54\n"
    "v_max3_f32 v243, v243, v61, v62\n"
    "buffer_load_dwordx4 v227, s[40:43], s71 offen offset:3072 nt lds\n"
    "v_max3_f32 v240, v240, v39, v39\n"
    "v_max3_f32 v241, v241, v47, v47\n"
    "s_add_u32 s71, s71, 16384\n"
    "v_add_u32 v253, s99, v228\n"
    "v_max3_f32 v242, v242, v55, v55\n"
    "v_max3_f32 v243, v243, v63, v63\n"
    "ds_read_b128 a[192:195], v253 offset:0\n"
    "ds_read_b128 a[196:199], v253 offset:4096\n"
    "v_mov_b32 v236, v240\n"
    "v_mov_b32 v237, v241\n"
    "v_mov_b32 v238, v242\n"
    "v_mov_b32 v239, v243\n"
    "s_nop 0\n"
    "v_permlane32_swap_b32 v240, v236\n"
    "v_permlane32_swap_b32 v241, v237\n"
    "v_permlane32_swap_b32 v242, v238\n"
    "v_permlane32_swap_b32 v243, v239\n"
    "s_nop 0\n"
    "v_max_f32 v240, v240, v236\n"
    "v_max_f32 v241, v241, v237\n"
    "v_max_f32 v242, v242, v238\n"
    "v_max_f32 v243, v243, v239\n"
    "v_mov_b32 v236, v240\n"
    "v_mov_b32 v237, v241\n"
    "v_mov_b32 v238, v242\n"
    "v_mov_b32 v239, v243\n"
    "ds_read_b128 a[200:203], v253 offset:8192\n"
    "ds_read_b128 a[204:207], v253 offset:12288\n"
    "s_nop 0\n"
    "v_permlane16_swap_b32 v240, v236\n"
    "v_permlane16_swap_b32 v241, v237\n"
    "v_permlane16_swap_b32 v242, v238\n"
    "v_permlane16_swap_b32 v243, v239\n"
    "s_nop 0\n"
    "v_max_f32 v240, v240, v236\n"
    "v_max_f32 v241, v241, v237\n"
    "v_max_f32 v242, v242, v238\n"
    "v_max_f32 v243, v243, v239\n"
    "v_sub_f32 v0, v0, v240\n"
    "v_sub_f32 v1, v1, v240\n"
    "v_sub_f32 v2, v2, v240\n"
    "v_sub_f32 v3, v3, v240\n"
    "v_sub_f32 v4, v4, v240\n"
    "v_sub_f32 v5, v5, v240\n"
    "v_sub_f32 v6, v6, v240\n"
    "v_sub_f32 v7, v7, v240\n"
    "ds_read_b128 a[208:211], v253 offset:512\n"
    "ds_read_b128 a[212:215], v253 offset:4608\n"
    "v_sub_f32 v8, v8, v241\n"
    "v_sub_f32 v9, v9, v241\n"
    "v_sub_f32 v10, v10, v241\n"
    "v_sub_f32 v11, v11, v241\n"
    "v_sub_f32 v12, v12, v241\n"
    "v_sub_f32 v13, v13, v241\n"
    "v_sub_f32 v14, v14, v241\n"
    "v_sub_f32 v15, v15, v241\n"
    "v_sub_f32 v16, v16, v242\n"
    "v_sub_f32 v17, v17, v242\n"
    "v_sub_f32 v18, v18, v242\n"
    "v_sub_f32 v19, v19, v242\n"
    "v_sub_f32 v20, v20, v242\n"
    "v_sub_f32 v21, v21, v242\n"
    "v_sub_f32 v22, v22, v242\n"
    "v_sub_f32 v23, v23, v242\n"
    "v_sub_f32 v24, v24, v243\n"
    "v_sub_f32 v25, v25, v243\n"
    "ds_read_b128 a[216:219], v253 offset:8704\n"
    "ds_read_b128 a[220:223], v253 offset:12800\n"
    "v_sub_f32 v26, v26, v243\n"
    "v_sub_f32 v27, v27, v243\n"
    "v_sub_f32 v28, v28, v243\n"
    "v_sub_f32 v29, v29, v243\n"
    "v_sub_f32 v30, v30, v243\n"
    "v_sub_f32 v31, v31, v243\n"
    "v_sub_f32 v32, v32, v240\n"
    "v_sub_f32 v33, v33, v240\n"
    "v_sub_f32 v34, v34, v240\n"
    "v_sub_f32 v35, v35, v240\n"
    "v_sub_f32 v36, v36, v240\n"
    "v_sub_f32 v37, v37, v240\n"
    "v_sub_f32 v38, v38, v240\n"
    "v_sub_f32 v39, v39, v240\n"
    "v_sub_f32 v40, v40, v241\n"
    "v_sub_f32 v41, v41, v241\n"
    "v_sub_f32 v42, v42, v241\n"
    "v_sub_f32 v43, v43, v241\n"
    "ds_read_b128 a[224:227], v253 offset:1024\n"
    "ds_read_b128 a[228:231], v253 offset:5120\n"
    "v_sub_f32 v44, v44, v241\n"
    "v_sub_f32 v45, v45, v241\n"
    "v_sub_f32 v46, v46, v241\n"
    "v_sub_f32 v47, v47, v241\n"
    "v_sub_f32 v48, v48, v242\n"
    "v_sub_f32 v49, v49, v242\n"
    "v_sub_f32 v50, v50, v242\n"
    "v_sub_f32 v51, v51, v242\n"
    "v_sub_f32 v52, v52, v242\n"
    "v_sub_f32 v53, v53, v242\n"
    "v_sub_f32 v54, v54, v242\n"
    "v_sub_f32 v55, v55, v242\n"
    "v_sub_f32 v56, v56, v243\n"
    "v_sub_f32 v57, v57, v243\n"
    "v_sub_f32 v58, v58, v243\n"
    "v_sub_f32 v59, v59, v243\n"
    "v_sub_f32 v60, v60, v243\n"
    "v_sub_f32 v61, v61, v243\n"
    "v_sub_f32 v62, v62, v243\n"
    "v_sub_f32 v63, v63, v243\n"
    "v_sub_f32 v192, 0, v240\n"
    "v_sub_f32 v193, 0, v240\n"
    "v_sub_f32 v194, 0, v240\n"
    "v_sub_f32 v195, 0, v240\n"
    "v_sub_f32 v196, 0, v241\n"
    "v_sub_f32 v197, 0, v241\n"
    "v_sub_f32 v198, 0, v241\n"
    "v_sub_f32 v199, 0, v241\n"
    "v_sub_f32 v200, 0, v242\n"
    "v_sub_f32 v201, 0, v242\n"
    "v_sub_f32 v202, 0, v242\n"
    "v_sub_f32 v203, 0, v242\n"
    "v_sub_f32 v204, 0, v243\n"
    "v_sub_f32 v205, 0, v243\n"
    "v_sub_f32 v206, 0, v243\n"
    "v_sub_f32 v207, 0, v243\n"
    "v_exp_f32 v0, v0\n"
    "v_exp_f32 v1, v1\n"
    "v_exp_f32 v2, v2\n"
    "v_exp_f32 v3, v3\n"
    "v_exp_f32 v4, v4\n"
    "v_cvt_pk_bf16_f32 v0, v0, v1\n"
    "v_cvt_pk_bf16_f32 v1, v2, v3\n"
    "v_exp_f32 v5, v5\n"
    "ds_read_b128 a[232:235], v253 offset:9216\n"
    "ds_read_b128 a[236:239], v253 offset:13312\n"
    "v_exp_f32 v6, v6\n"
    "v_exp_f32 v7, v7\n"
    "v_exp_f32 v8, v8\n"
    "v_cvt_pk_bf16_f32 v2, v4, v5\n"
    "v_cvt_pk_bf16_f32 v3, v6, v7\n"
    "v_exp_f32 v9, v9\n"
    "v_exp_f32 v10, v10\n"
    "v_exp_f32 v11, v11\n"
    "ds_read_b128 a[240:243], v253 offset:1536\n"
    "ds_read_b128 a[244:247], v253 offset:5632\n"
    "v_exp_f32 v12, v12\n"
    "v_cvt_pk_bf16_f32 v8, v8, v9\n"
    "v_cvt_pk_bf16_f32 v9, v10, v11\n"
    "v_exp_f32 v13, v13\n"
    "v_exp_f32 v14, v14\n"
    "v_exp_f32 v15, v15\n"
    "v_exp_f32 v16, v16\n"
    "v_cvt_pk_bf16_f32 v10, v12, v13\n"
    "v_cvt_pk_bf16_f32 v11, v14, v15\n"
    "v_exp_f32 v17, v17\n"
    "ds_read_b128 a[248:251], v253 offset:9728\n"
    "ds_read_b128 a[252:255], v253 offset:13824\n"
    "v_exp_f32 v18, v18\n"
    "v_exp_f32 v19, v19\n"
    "v_exp_f32 v20, v20\n"
    "v_cvt_pk_bf16_f32 v16, v16, v17\n"
    "v_cvt_pk_bf16_f32 v17, v18, v19\n"
    "v_exp_f32 v21, v21\n"
    "v_exp_f32 v22, v22\n"
    "v_exp_f32 v23, v23\n"
    "v_exp_f32 v24, v24\n"
    "v_cvt_pk_bf16_f32 v18, v20, v21\n"
    "v_cvt_pk_bf16_f32 v19, v22, v23\n"
    "v_exp_f32 v25, v25\n"
    "v_exp_f32 v26, v26\n"
    "v_exp_f32 v27, v27\n"
    "v_exp_f32 v28, v28\n"
    "v_exp_f32 v29, v29\n"
    "v_cvt_pk_bf16_f32 v24, v24, v25\n"
    "v_cvt_pk_bf16_f32 v25, v26, v27\n"
    "v_exp_f32 v30, v30\n"
    "v_exp_f32 v31, v31\n"
    "v_exp_f32 v32, v32\n"
    "v_exp_f32 v33, v33\n"
    "v_exp_f32 v34, v34\n"
    "v_cvt_pk_bf16_f32 v26, v28, v29\n"
    "v_cvt_pk_bf16_f32 v27, v30, v31\n"
    "v_exp_f32 v35, v35\n"
    "v_exp_f32 v36, v36\n"
    "v_exp_f32 v37, v37\n"
    "v_exp_f32 v38, v38\n"
    "v_exp_f32 v39, v39\n"
    "s_waitcnt lgkmcnt(0)\n"
    "s_mov_b32 s54, 0\n"
    "s_movk_i32 s74, 0x107\n"
    "s_branch P1F%=\n"
    "PWF%=:\n"
    "v_mfma_f32_16x16x32_bf16 v[0:3], a[192:195], a[128:131], 0\n"
    "v_exp_f32 v104, v104\n"
    "v_mfma_f32_16x16x32_bf16 v[8:11], a[192:195], a[144:147], 0\n"
    "ds_read_b64_tr_b16 v[128:129], v250 offset:0\n"
    "ds_read_b64_tr_b16 v[130:131], v250 offset:4096\n"
    "v_mfma_f32_16x16x32_bf16 v[16:19], a[192:195], a[160:163], 0\n"
    "v_cvt_pk_bf16_f32 v96, v96, v97\n"
    "v_cvt_pk_bf16_f32 v97, v98, v99\n"
    "v_mfma_f32_16x16x32_bf16 v[24:27], a[192:195], a[176:179], 0\n"
    "v_exp_f32 v105, v105\n"
    "v_mfma_f32_16x16x32_bf16 v[4:7], a[196:199], a[128:131], 0\n"
    "ds_read_b64_tr_b16 v[132:133], v251 offset:0\n"
    "ds_read_b64_tr_b16 v[134:135], v251 offset:4096\n"
    "v_mfma_f32_16x16x32_bf16 v[12:15], a[196:199], a[144:147], 0\n"
    "v_mfma_f32_16x16x32_bf16 v[20:23], a[196:199], a[160:163], 0\n"
    "v_exp_f32 v106, v106\n"
    "v_mfma_f32_16x16x32_bf16 v[28:31], a[196:199], a[176:179], 0\n"
    "ds_read_b64_tr_b16 v[136:137], v250 offset:512\n"
    "ds_read_b64_tr_b16 v[138:139], v250 offset:4608\n"
    "v_mfma_f32_16x16x32_bf16 v[32:35], a[200:203], a[128:131], 0\n"
    "v_cvt_pk_bf16_f32 v98, v100, v101\n"
    "v_cvt_pk_bf16_f32 v99, v102, v103\n"
    "v_mfma_f32_16x16x32_bf16 v[40:43], a[200:203], a[144:147], 0\n"
    "v_mfma_f32_16x16x32_bf16 v[48:51], a[200:203], a[160:163], 0\n"
    "ds_read_b64_tr_b16 v[140:141], v251 offset:512\n"
    "ds_read_b64_tr_b16 v[142:143], v251 offset:4608\n"
    "v_mfma_f32_16x16x32_bf16 v[56:59], a[200:203], a[176:179], 0\n"
    "v_exp_f32 v107, v107\n"
    "v_mfma_f32_16x16x32_bf16 v[36:39], a[204:207], a[128:131], 0\n"
    "v_exp_f32 v108, v108\n"
    "v_mfma_f32_16x16x32_bf16 v[44:47], a[204:207], a[144:147], 0\n"
    "ds_read_b64_tr_b16 v[144:145], v250 offset:1024\n"
    "ds_read_b64_tr_b16 v[146:147], v250 offset:5120\n"
    "v_mfma_f32_16x16x32_bf16 v[52:55], a[204:207], a[160:163], 0\n"
    "v_exp_f32 v109, v109\n"
    "v_mfma_f32_16x16x32_bf16 v[60:63], a[204:207], a[176:179], 0\n"
    "v_mfma_f32_16x16x32_bf16 v[0:3], a[208:211], a[132:135], v[0:3]\n"
    "ds_read_b64_tr_b16 v[148:149], v251 offset:1024\n"
    "ds_read_b64_tr_b16 v[150:151], v251 offset:5120\n"
    "v_mfma_f32_16x16x32_bf16 v[8:11], a[208:211], a[148:151], v[8:11]\n"
    "v_cvt_pk_bf16_f32 v104, v104, v105\n"
    "v_cvt_pk_bf16_f32 v105, v106, v107\n"
    "v_mfma_f32_16x16x32_bf16 v[16:19], a[208:211], a[164:167], v[16:19]\n"
    "v_exp_f32 v110, v110\n"
    "v_mfma_f32_16x16x32_bf16 v[24:27], a[208:211], a[180:183], v[24:27]\n"
    "ds_read_b64_tr_b16 v[152:153], v250 offset:1536\n"
    "ds_read_b64_tr_b16 v[154:155], v250 offset:5632\n"
    "v_mfma_f32_16x16x32_bf16 v[4:7], a[212:215], a[132:135], v[4:7]\n"
    "v_mfma_f32_16x16x32_bf16 v[12:15], a[212:215], a[148:151], v[12:15]\n"
    "v_exp_f32 v111, v111\n"
    "v_mfma_f32_16x16x32_bf16 v[20:23], a[212:215], a[164:167], v[20:23]\n"
    "ds_read_b64_tr_b16 v[156:157], v251 offset:1536\n"
    "ds_read_b64_tr_b16 v[158:159], v251 offset:5632\n"
    "v_mfma_f32_16x16x32_bf16 v[28:31], a[212:215], a[180:183], v[28:31]\n"
    "v_exp_f32 v112, v112\n"
    "v_mfma_f32_16x16x32_bf16 v[32:35], a[216:219], a[132:135], v[32:35]\n"
    "v_cvt_pk_bf16_f32 v106, v108, v109\n"
    "v_cvt_pk_bf16_f32 v107, v110, v111\n"
    "v_mfma_f32_16x16x32_bf16 v[40:43], a[216:219], a[148:151], v[40:43]\n"
    "v_mfma_f32_16x16x32_bf16 v[48:51], a[216:219], a[164:167], v[48:51]\n"
    "ds_read_b64_tr_b16 v[160:161], v250 offset:8192\n"
    "ds_read_b64_tr_b16 v[162:163], v250 offset:12288\n"
    "v_mfma_f32_16x16x32_bf16 v[56:59], a[216:219], a[180:183], v[56:59]\n"
    "v_exp_f32 v113, v113\n"
    "v_mfma_f32_16x16x32_bf16 v[36:39], a[220:223], a[132:135], v[36:39]\n"
    "v_exp_f32 v114, v114\n"
    "v_mfma_f32_16x16x32_bf16 v[44:47], a[220:223], a[148:151], v[44:47]\n"
    "ds_read_b64_tr_b16 v[164:165], v251 offset:8192\n"
    "ds_read_b64_tr_b16 v[166:167], v251 offset:12288\n"
    "v_mfma_f32_16x16x32_bf16 v[52:55], a[220:223], a[164:167], v[52:55]\n"
    "v_mfma_f32_16x16x32_bf16 v[60:63], a[220:223], a[180:183], v[60:63]\n"
    "v_exp_f32 v115, v115\n"
    "v_mfma_f32_16x16x32_bf16 v[0:3], a[224:227], a[136:139], v[0:3]\n"
    "ds_read_b64_tr_b16 v[168:169], v250 offset:8704\n"
    "ds_read_b64_tr_b16 v[170:171], v250 offset:12800\n"
    "v_mfma_f32_16x16x32_bf16 v[8:11], a[224:227], a[152:155], v[8:11]\n"
    "v_exp_f32 v116, v116\n"
    "v_mfma_f32_16x16x32_bf16 v[16:19], a[224:227], a[168:171], v[16:19]\n"
    "v_mfma_f32_16x16x32_bf16 v[24:27], a[224:227], a[184:187], v[24:27]\n"
    "ds_read_b64_tr_b16 v[172:173], v251 offset:8704\n"
    "ds_read_b64_tr_b16 v[174:175], v251 offset:12800\n"
    "v_mfma_f32_16x16x32_bf16 v[4:7], a[228:231], a[136:139], v[4:7]\n"
    "v_cvt_pk_bf16_f32 v112, v112, v113\n"
    "v_cvt_pk_bf16_f32 v113, v114, v115\n"
    "v_mfma_f32_16x16x32_bf16 v[12:15], a[228:231], a[152:155], v[12:15]\n"
    "v_exp_f32 v117, v117\n"
    "v_mfma_f32_16x16x32_bf16 v[20:23], a[228:231], a[168:171], v[20:23]\n"
    "ds_read_b64_tr_b16 v[176:177], v250 offset:9216\n"
    "ds_read_b64_tr_b16 v[178:179], v250 offset:13312\n"
    "v_mfma_f32_16x16x32_bf16 v[28:31], a[228:231], a[184:187], v[28:31]\n"
    "v_exp_f32 v118, v118\n"
    "v_mfma_f32_16x16x32_bf16 v[32:35], a[232:235], a[136:139], v[32:35]\n"
    "v_mfma_f32_16x16x32_bf16 v[40:43], a[232:235], a[152:155], v[40:43]\n"
    "ds_read_b64_tr_b16 v[180:181], v251 offset:9216\n"
    "ds_read_b64_tr_b16 v[182:183], v251 offset:13312\n"
    "v_mfma_f32_16x16x32_bf16 v[48:51], a[232:235], a[168:171], v[48:51]\n"
    "v_exp_f32 v119, v119\n"
    "v_mfma_f32_16x16x32_bf16 v[56:59], a[232:235], a[184:187], v[56:59]\n"
    "v_exp_f32 v120, v120\n"
    "v_mfma_f32_16x16x32_bf16 v[36:39], a[236:239], a[136:139], v[36:39]\n"
    "ds_read_b64_tr_b16 v[184:185], v250 offset:9728\n"
    "ds_read_b64_tr_b16 v[186:187], v250 offset:13824\n"
    "v_mfma_f32_16x16x32_bf16 v[44:47], a[236:239], a[152:155], v[44:47]\n"
    "v_mfma_f32_16x16x32_bf16 v[52:55], a[236:239], a[168:171], v[52:55]\n"
    "v_cvt_pk_bf16_f32 v114, v116, v117\n"
    "v_cvt_pk_bf16_f32 v115, v118, v119\n"
    "v_mfma_f32_16x16x32_bf16 v[60:63], a[236:239], a[184:187], v[60:63]\n"
    "ds_read_b64_tr_b16 v[188:189], v251 offset:9728\n"
    "ds_read_b64_tr_b16 v[190:191], v251 offset:13824\n"
    "v_mfma_f32_16x16x32_bf16 v[0:3], a[240:243], a[140:143], v[0:3]\n"
    "v_exp_f32 v121, v121\n"
    "v_mfma_f32_16x16x32_bf16 v[8:11], a[240:243], a[156:159], v[8:11]\n"
    "v_exp_f32 v122, v122\n"
    "v_mfma_f32_16x16x32_bf16 v[16:19], a[240:243], a[172:175], v[16:19]\n"
    "v_mfma_f32_16x16x32_bf16 v[24:27], a[240:243], a[188:191], v[24:27]\n"
    "v_exp_f32 v123, v123\n"
    "v_mfma_f32_16x16x32_bf16 v[4:7], a[244:247], a[140:143], v[4:7]\n"
    "v_exp_f32 v124, v124\n"
    "v_mfma_f32_16x16x32_bf16 v[12:15], a[244:247], a[156:159], v[12:15]\n"
    "v_mfma_f32_16x16x32_bf16 v[20:23], a[244:247], a[172:175], v[20:23]\n"
    "v_cvt_pk_bf16_f32 v120, v120, v121\n"
    "v_cvt_pk_bf16_f32 v121, v122, v123\n"
    "v_mfma_f32_16x16x32_bf16 v[28:31], a[244:247], a[188:191], v[28:31]\n"
    "v_exp_f32 v125, v125\n"
    "v_mfma_f32_16x16x32_bf16 v[32:35], a[248:251], a[140:143], v[32:35]\n"
    "v_mfma_f32_16x16x32_bf16 v[40:43], a[248:251], a[156:159], v[40:43]\n"
    "v_exp_f32 v126, v126\n"
    "v_mfma_f32_16x16x32_bf16 v[48:51], a[248:251], a[172:175], v[48:51]\n"
    "v_exp_f32 v127, v127\n"
    "v_mfma_f32_16x16x32_bf16 v[56:59], a[248:251], a[188:191], v[56:59]\n"
    "s_nop 0\n"
    "v_mfma_f32_16x16x32_bf16 v[36:39], a[252:255], a[140:143], v[36:39]\n"
    "v_cvt_pk_bf16_f32 v122, v124, v125\n"
    "v_cvt_pk_bf16_f32 v123, v126, v127\n"
    "v_mfma_f32_16x16x32_bf16 v[44:47], a[252:255], a[156:159], v[44:47]\n"
    "v_mfma_f32_16x16x32_bf16 v[52:55], a[252:255], a[172:175], v[52:55]\n"
    "v_mfma_f32_16x16x32_bf16 v[60:63], a[252:255], a[188:191], v[60:63]\n"
    "s_waitcnt vmcnt(8) lgkmcnt(0)\n"
    "s_barrier\n"
    "v_mfma_f32_16x16x32_bf16 a[0:3], v[128:131], v[64:67], a[0:3]\n"
    "v_mfma_f32_16x16x32_bf16 a[32:35], v[128:131], v[72:75], a[32:35]\n"
    "s_add_u32 m0, s72, s99\n"
    "v_mfma_f32_16x16x32_bf16 a[64:67], v[128:131], v[80:83], a[64:67]\n"
    "buffer_load_dwordx4 v224, s[36:39], s70 offen offset:0 nt lds\n"
    "v_mfma_f32_16x16x32_bf16 a[96:99], v[128:131], v[88:91], a[96:99]\n"
    "v_mfma_f32_16x16x32_bf16 a[4:7], v[132:135], v[64:67], a[4:7]\n"
    "buffer_load_dwordx4 v225, s[36:39], s70 offen offset:1024 nt lds\n"
    "v_max3_f32 v240, v0, v1, v2\n"
    "v_max3_f32 v241, v8, v9, v10\n"
    "v_mfma_f32_16x16x32_bf16 a[36:39], v[132:135], v[72:75], a[36:39]\n"
    "v_max3_f32 v242, v16, v17, v18\n"
    "v_max3_f32 v243, v24, v25, v26\n"
    "v_max3_f32 v240, v240, v3, v4\n"
    "v_max3_f32 v241, v241, v11, v12\n"
    "v_mfma_f32_16x16x32_bf16 a[68:71], v[132:135], v[80:83], a[68:71]\n"
    "buffer_load_dwordx4 v226, s[36:39], s70 offen offset:2048 nt lds\n"
    "v_max3_f32 v242, v242, v19, v20\n"
    "v_max3_f32 v243, v243, v27, v28\n"
    "v_mfma_f32_16x16x32_bf16 a[100:103], v[132:135], v[88:91], a[100:103]\n"
    "v_max3_f32 v240, v240, v5, v6\n"
    "v_max3_f32 v241, v241, v13, v14\n"
    "v_max3_f32 v242, v242, v21, v22\n"
    "v_max3_f32 v243, v243, v29, v30\n"
    "v_mfma_f32_16x16x32_bf16 a[8:11], v[136:139], v[64:67], a[8:11]\n"
    "buffer_load_dwordx4 v227, s[36:39], s70 offen offset:3072 nt lds\n"
    "v_max3_f32 v240, v240, v7, v32\n"
    "v_max3_f32 v241, v241, v15, v40\n"
    "v_mfma_f32_16x16x32_bf16 a[40:43], v[136:139], v[72:75], a[40:43]\n"
    "s_add_u32 m0, s73, s83\n"
    "s_add_u32 s70, s70, 16384\n"
    "v_max3_f32 v242, v242, v23, v48\n"
    "v_max3_f32 v243, v243, v31, v56\n"
    "v_mfma_f32_16x16x32_bf16 a[72:75], v[136:139], v[80:83], a[72:75]\n"
    "buffer_load_dwordx4 v224, s[40:43], s71 offen offset:0 nt lds\n"
    "v_max3_f32 v240, v240, v33, v34\n"
    "v_max3_f32 v241, v241, v41, v42\n"
    "v_mfma_f32_16x16x32_bf16 a[104:107], v[136:139], v[88:91], a[104:107]\n"
    "v_add_u32 v250, s99, v230\n"
    "v_add_u32 v251, s99, v231\n"
    "v_max3_f32 v242, v242, v49, v50\n"
    "v_max3_f32 v243, v243, v57, v58\n"
    "v_mfma_f32_16x16x32_bf16 a[12:15], v[140:143], v[64:67], a[12:15]\n"
    "buffer_load_dwordx4 v225, s[40:43], s71 offen offset:1024 nt lds\n"
    "v_max3_f32 v240, v240, v35, v36\n"
    "v_max3_f32 v241, v241, v43, v44\n"
    "v_mfma_f32_16x16x32_bf16 a[44:47], v[140:143], v[72:75], a[44:47]\n"
    "s_mov_b32 s83, s99\n"
    "s_add_u32 s99, s99, 16384\n"
    "v_max3_f32 v242, v242, v51, v52\n"
    "v_max3_f32 v243, v243, v59, v60\n"
    "v_mfma_f32_16x16x32_bf16 a[76:79], v[140:143], v[80:83], a[76:79]\n"
    "buffer_load_dwordx4 v226, s[40:43], s71 offen offset:2048 nt lds\n"
    "v_max3_f32 v240, v240, v37, v38\n"
    "v_max3_f32 v241, v241, v45, v46\n"
    "v_mfma_f32_16x16x32_bf16 a[108:111], v[140:143], v[88:91], a[108:111]\n"
    "s_cmp_eq_u32 s99, 49152\n"
    "s_cselect_b32 s99, 0, s99\n"
    "v_max3_f32 v242, v242, v53, v54\n"
    "v_max3_f32 v243, v243, v61, v62\n"
    "v_mfma_f32_16x16x32_bf16 a[16:19], v[144:147], v[64:67], a[16:19]\n"
    "buffer_load_dwordx4 v227, s[40:43], s71 offen offset:3072 nt lds\n"
    "v_max3_f32 v240, v240, v39, v39\n"
    "v_max3_f32 v241, v241, v47, v47\n"
    "v_mfma_f32_16x16x32_bf16 a[48:51], v[144:147], v[72:75], a[48:51]\n"
    "s_add_u32 s71, s71, 16384\n"
    "v_add_u32 v253, s99, v228\n"
    "v_max3_f32 v242, v242, v55, v55\n"
    "v_max3_f32 v243, v243, v63, v63\n"
    "v_mfma_f32_16x16x32_bf16 a[80:83], v[144:147], v[80:83], a[80:83]\n"
    "ds_read_b128 a[192:195], v253 offset:0\n"
    "ds_read_b128 a[196:199], v253 offset:4096\n"
    "v_mov_b32 v236, v240\n"
    "v_mov_b32 v237, v241\n"
    "v_mfma_f32_16x16x32_bf16 a[112:115], v[144:147], v[88:91], a[112:115]\n"
    "v_mov_b32 v238, v242\n"
    "v_mov_b32 v239, v243\n"
    "s_nop 0\n"
    "v_permlane32_swap_b32 v240, v236\n"
    "v_mfma_f32_16x16x32_bf16 a[20:23], v[148:151], v[64:67], a[20:23]\n"
    "v_permlane32_swap_b32 v241, v237\n"
    "v_permlane32_swap_b32 v242, v238\n"
    "v_permlane32_swap_b32 v243, v239\n"
    "s_nop 0\n"
    "v_mfma_f32_16x16x32_bf16 a[52:55], v[148:151], v[72:75], a[52:55]\n"
    "v_max_f32 v240, v240, v236\n"
    "v_max_f32 v241, v241, v237\n"
    "v_max_f32 v242, v242, v238\n"
    "v_max_f32 v243, v243, v239\n"
    "v_mfma_f32_16x16x32_bf16 a[84:87], v[148:151], v[80:83], a[84:87]\n"
    "v_mov_b32 v236, v240\n"
    "v_mov_b32 v237, v241\n"
    "v_mov_b32 v238, v242\n"
    "v_mov_b32 v239, v243\n"
    "v_mfma_f32_16x16x32_bf16 a[116:119], v[148:151], v[88:91], a[116:119]\n"
    "ds_read_b128 a[200:203], v253 offset:8192\n"
    "ds_read_b128 a[204:207], v253 offset:12288\n"
    "s_nop 0\n"
    "v_permlane16_swap_b32 v240, v236\n"
    "v_mfma_f32_16x16x32_bf16 a[24:27], v[152:155], v[64:67], a[24:27]\n"
    "v_permlane16_swap_b32 v241, v237\n"
    "v_permlane16_swap_b32 v242, v238\n"
    "v_permlane16_swap_b32 v243, v239\n"
    "s_nop 0\n"
    "v_mfma_f32_16x16x32_bf16 a[56:59], v[152:155], v[72:75], a[56:59]\n"
    "v_max_f32 v240, v240, v236\n"
    "v_max_f32 v241, v241, v237\n"
    "v_max_f32 v242, v242, v238\n"
    "v_max_f32 v243, v243, v239\n"
    "v_mfma_f32_16x16x32_bf16 a[88:91], v[152:155], v[80:83], a[88:91]\n"
    "v_sub_f32 v0, v0, v240\n"
    "v_sub_f32 v1, v1, v240\n"
    "v_sub_f32 v2, v2, v240\n"
    "v_sub_f32 v3, v3, v240\n"
    "v_mfma_f32_16x16x32_bf16 a[120:123], v[152:155], v[88:91], a[120:123]\n"
    "v_sub_f32 v4, v4, v240\n"
    "v_sub_f32 v5, v5, v240\n"
    "v_sub_f32 v6, v6, v240\n"
    "v_sub_f32 v7, v7, v240\n"
    "v_mfma_f32_16x16x32_bf16 a[28:31], v[156:159], v[64:67], a[28:31]\n"
    "ds_read_b128 a[208:211], v253 offset:512\n"
    "ds_read_b128 a[212:215], v253 offset:4608\n"
    "v_sub_f32 v8, v8, v241\n"
    "v_sub_f32 v9, v9, v241\n"
    "v_mfma_f32_16x16x32_bf16 a[60:63], v[156:159], v[72:75], a[60:63]\n"
    "v_sub_f32 v10, v10, v241\n"
    "v_sub_f32 v11, v11, v241\n"
    "v_sub_f32 v12, v12, v241\n"
    "v_sub_f32 v13, v13, v241\n"
    "v_mfma_f32_16x16x32_bf16 a[92:95], v[156:159], v[80:83], a[92:95]\n"
    "v_sub_f32 v14, v14, v241\n"
    "v_sub_f32 v15, v15, v241\n"
    "v_sub_f32 v16, v16, v242\n"
    "v_sub_f32 v17, v17, v242\n"
    "v_mfma_f32_16x16x32_bf16 a[124:127], v[156:159], v[88:91], a[124:127]\n"
    "v_sub_f32 v18, v18, v242\n"
    "v_sub_f32 v19, v19, v242\n"
    "v_sub_f32 v20, v20, v242\n"
    "v_sub_f32 v21, v21, v242\n"
    "v_mfma_f32_16x16x32_bf16 v[208:211], v[232:235], v[64:67], v[208:211]\n"
    "v_sub_f32 v22, v22, v242\n"
    "v_sub_f32 v23, v23, v242\n"
    "v_sub_f32 v24, v24, v243\n"
    "v_sub_f32 v25, v25, v243\n"
    "v_mfma_f32_16x16x32_bf16 v[212:215], v[232:235], v[72:75], v[212:215]\n"
    "ds_read_b128 a[216:219], v253 offset:8704\n"
    "ds_read_b128 a[220:223], v253 offset:12800\n"
    "v_sub_f32 v26, v26, v243\n"
    "v_sub_f32 v27, v27, v243\n"
    "v_mfma_f32_16x16x32_bf16 v[216:219], v[232:235], v[80:83], v[216:219]\n"
    "v_sub_f32 v28, v28, v243\n"
    "v_sub_f32 v29, v29, v243\n"
    "v_sub_f32 v30, v30, v243\n"
    "v_sub_f32 v31, v31, v243\n"
    "v_mfma_f32_16x16x32_bf16 v[220:223], v[232:235], v[88:91], v[220:223]\n"
    "v_sub_f32 v32, v32, v240\n"
    "v_sub_f32 v33, v33, v240\n"
    "v_sub_f32 v34, v34, v240\n"
    "v_sub_f32 v35, v35, v240\n"
    "v_mfma_f32_16x16x32_bf16 a[0:3], v[160:163], v[96:99], a[0:3]\n"
    "v_sub_f32 v36, v36, v240\n"
    "v_sub_f32 v37, v37, v240\n"
    "v_sub_f32 v38, v38, v240\n"
    "v_sub_f32 v39, v39, v240\n"
    "v_mfma_f32_16x16x32_bf16 a[32:35], v[160:163], v[104:107], a[32:35]\n"
    "v_sub_f32 v40, v40, v241\n"
    "v_sub_f32 v41, v41, v241\n"
    "v_sub_f32 v42, v42, v241\n"
    "v_sub_f32 v43, v43, v241\n"
    "v_mfma_f32_16x16x32_bf16 a[64:67], v[160:163], v[112:115], a[64:67]\n"
    "ds_read_b128 a[224:227], v253 offset:1024\n"
    "ds_read_b128 a[228:231], v253 offset:5120\n"
    "v_sub_f32 v44, v44, v241\n"
    "v_sub_f32 v45, v45, v241\n"
    "v_mfma_f32_16x16x32_bf16 a[96:99], v[160:163], v[120:123], a[96:99]\n"
    "v_sub_f32 v46, v46, v241\n"
    "v_sub_f32 v47, v47, v241\n"
    "v_sub_f32 v48, v48, v242\n"
    "v_sub_f32 v49, v49, v242\n"
    "v_sub_f32 v50, v50, v242\n"
    "v_sub_f32 v51, v51, v242\n"
    "v_sub_f32 v52, v52, v242\n"
    "v_sub_f32 v53, v53, v242\n"
    "v_sub_f32 v54, v54, v242\n"
    "v_sub_f32 v55, v55, v242\n"
    "v_sub_f32 v56, v56, v243\n"
    "v_sub_f32 v57, v57, v243\n"
    "v_sub_f32 v58, v58, v243\n"
    "v_sub_f32 v59, v59, v243\n"
    "v_sub_f32 v60, v60, v243\n"
    "v_sub_f32 v61, v61, v243\n"
    "v_sub_f32 v62, v62, v243\n"
    "v_sub_f32 v63, v63, v243\n"
    "v_sub_f32 v192, 0, v240\n"
    "v_sub_f32 v193, 0, v240\n"
    "v_sub_f32 v194, 0, v240\n"
    "v_sub_f32 v195, 0, v240\n"
    "v_sub_f32 v196, 0, v241\n"
    "v_sub_f32 v197, 0, v241\n"
    "v_sub_f32 v198, 0, v241\n"
    "v_sub_f32 v199, 0, v241\n"
    "v_sub_f32 v200, 0, v242\n"
    "v_sub_f32 v201, 0, v242\n"
    "v_sub_f32 v202, 0, v242\n"
    "v_sub_f32 v203, 0, v242\n"
    "v_sub_f32 v204, 0, v243\n"
    "v_sub_f32 v205, 0, v243\n"
    "v_sub_f32 v206, 0, v243\n"
    "v_sub_f32 v207, 0, v243\n"
    "v_mfma_f32_16x16x32_bf16 a[4:7], v[164:167], v[96:99], a[4:7]\n"
    "v_exp_f32 v0, v0\n"
    "v_exp_f32 v1, v1\n"
    "v_mfma_f32_16x16x32_bf16 a[36:39], v[164:167], v[104:107], a[36:39]\n"
    "v_exp_f32 v2, v2\n"
    "v_exp_f32 v3, v3\n"
    "v_mfma_f32_16x16x32_bf16 a[68:71], v[164:167], v[112:115], a[68:71]\n"
    "v_exp_f32 v4, v4\n"
    "v_mfma_f32_16x16x32_bf16 a[100:103], v[164:167], v[120:123], a[100:103]\n"
    "v_cvt_pk_bf16_f32 v0, v0, v1\n"
    "v_cvt_pk_bf16_f32 v1, v2, v3\n"
    "v_exp_f32 v5, v5\n"
    "v_mfma_f32_16x16x32_bf16 a[8:11], v[168:171], v[96:99], a[8:11]\n"
    "ds_read_b128 a[232:235], v253 offset:9216\n"
    "ds_read_b128 a[236:239], v253 offset:13312\n"
    "v_exp_f32 v6, v6\n"
    "v_mfma_f32_16x16x32_bf16 a[40:43], v[168:171], v[104:107], a[40:43]\n"
    "v_exp_f32 v7, v7\n"
    "v_mfma_f32_16x16x32_bf16 a[72:75], v[168:171], v[112:115], a[72:75]\n"
    "v_exp_f32 v8, v8\n"
    "v_cvt_pk_bf16_f32 v2, v4, v5\n"
    "v_cvt_pk_bf16_f32 v3, v6, v7\n"
    "v_mfma_f32_16x16x32_bf16 a[104:107], v[168:171], v[120:123], a[104:107]\n"
    "v_exp_f32 v9, v9\n"
    "v_exp_f32 v10, v10\n"
    "v_mfma_f32_16x16x32_bf16 a[12:15], v[172:175], v[96:99], a[12:15]\n"
    "v_exp_f32 v11, v11\n"
    "v_mfma_f32_16x16x32_bf16 a[44:47], v[172:175], v[104:107], a[44:47]\n"
    "ds_read_b128 a[240:243], v253 offset:1536\n"
    "ds_read_b128 a[244:247], v253 offset:5632\n"
    "v_exp_f32 v12, v12\n"
    "v_mfma_f32_16x16x32_bf16 a[76:79], v[172:175], v[112:115], a[76:79]\n"
    "v_cvt_pk_bf16_f32 v8, v8, v9\n"
    "v_cvt_pk_bf16_f32 v9, v10, v11\n"
    "v_exp_f32 v13, v13\n"
    "v_mfma_f32_16x16x32_bf16 a[108:111], v[172:175], v[120:123], a[108:111]\n"
    "v_exp_f32 v14, v14\n"
    "v_mfma_f32_16x16x32_bf16 a[16:19], v[176:179], v[96:99], a[16:19]\n"
    "v_exp_f32 v15, v15\n"
    "v_exp_f32 v16, v16\n"
    "v_mfma_f32_16x16x32_bf16 a[48:51], v[176:179], v[104:107], a[48:51]\n"
    "v_cvt_pk_bf16_f32 v10, v12, v13\n"
    "v_cvt_pk_bf16_f32 v11, v14, v15\n"
    "v_exp_f32 v17, v17\n"
    "v_mfma_f32_16x16x32_bf16 a[80:83], v[176:179], v[112:115], a[80:83]\n"
    "ds_read_b128 a[248:251], v253 offset:9728\n"
    "ds_read_b128 a[252:255], v253 offset:13824\n"
    "v_mfma_f32_16x16x32_bf16 a[112:115], v[176:179], v[120:123], a[112:115]\n"
    "v_exp_f32 v18, v18\n"
    "v_exp_f32 v19, v19\n"
    "v_mfma_f32_16x16x32_bf16 a[20:23], v[180:183], v[96:99], a[20:23]\n"
    "v_exp_f32 v20, v20\n"
    "v_cvt_pk_bf16_f32 v16, v16, v17\n"
    "v_cvt_pk_bf16_f32 v17, v18, v19\n"
    "v_mfma_f32_16x16x32_bf16 a[52:55], v[180:183], v[104:107], a[52:55]\n"
    "v_exp_f32 v21, v21\n"
    "v_mfma_f32_16x16x32_bf16 a[84:87], v[180:183], v[112:115], a[84:87]\n"
    "v_exp_f32 v22, v22\n"
    "v_exp_f32 v23, v23\n"
    "v_mfma_f32_16x16x32_bf16 a[116:119], v[180:183], v[120:123], a[116:119]\n"
    "v_exp_f32 v24, v24\n"
    "v_cvt_pk_bf16_f32 v18, v20, v21\n"
    "v_cvt_pk_bf16_f32 v19, v22, v23\n"
    "v_mfma_f32_16x16x32_bf16 a[24:27], v[184:187], v[96:99], a[24:27]\n"
    "v_exp_f32 v25, v25\n"
    "v_mfma_f32_16x16x32_bf16 a[56:59], v[184:187], v[104:107], a[56:59]\n"
    "v_exp_f32 v26, v26\n"
    "v_exp_f32 v27, v27\n"
    "v_mfma_f32_16x16x32_bf16 a[88:91], v[184:187], v[112:115], a[88:91]\n"
    "v_exp_f32 v28, v28\n"
    "v_mfma_f32_16x16x32_bf16 a[120:123], v[184:187], v[120:123], a[120:123]\n"
    "v_exp_f32 v29, v29\n"
    "v_cvt_pk_bf16_f32 v24, v24, v25\n"
    "v_cvt_pk_bf16_f32 v25, v26, v27\n"
    "v_mfma_f32_16x16x32_bf16 a[28:31], v[188:191], v[96:99], a[28:31]\n"
    "v_exp_f32 v30, v30\n"
    "v_exp_f32 v31, v31\n"
    "v_mfma_f32_16x16x32_bf16 a[60:63], v[188:191], v[104:107], a[60:63]\n"
    "v_exp_f32 v32, v32\n"
    "v_mfma_f32_16x16x32_bf16 a[92:95], v[188:191], v[112:115], a[92:95]\n"
    "v_exp_f32 v33, v33\n"
    "v_exp_f32 v34, v34\n"
    "v_mfma_f32_16x16x32_bf16 a[124:127], v[188:191], v[120:123], a[124:127]\n"
    "v_cvt_pk_bf16_f32 v26, v28, v29\n"
    "v_cvt_pk_bf16_f32 v27, v30, v31\n"
    "v_exp_f32 v35, v35\n"
    "v_mfma_f32_16x16x32_bf16 v[208:211], v[232:235], v[96:99], v[208:211]\n"
    "v_exp_f32 v36, v36\n"
    "v_mfma_f32_16x16x32_bf16 v[212:215], v[232:235], v[104:107], v[212:215]\n"
    "v_exp_f32 v37, v37\n"
    "v_exp_f32 v38, v38\n"
    "v_mfma_f32_16x16x32_bf16 v[216:219], v[232:235], v[112:115], v[216:219]\n"
    "v_exp_f32 v39, v39\n"
    "v_mfma_f32_16x16x32_bf16 v[220:223], v[232:235], v[120:123], v[220:223]\n"
    "s_waitcnt lgkmcnt(0)\n"
    "s_mov_b32 s54, 2097152\n"
    "s_nop 7\n"
    "v_cmp_nlt_f32 vcc, v208, v252\n"
    "v_rcp_f32 v236, v208\n"
    "s_or_b64 s[96:97], s[96:97], vcc\n"
    "v_cmp_nlt_f32 vcc, v212, v252\n"
    "v_rcp_f32 v237, v212\n"
    "s_or_b64 s[96:97], s[96:97], vcc\n"
    "v_cmp_nlt_f32 vcc, v216, v252\n"
    "v_rcp_f32 v238, v216\n"
    "s_or_b64 s[96:97], s[96:97], vcc\n"
    "v_cmp_nlt_f32 vcc, v220, v252\n"
    "v_rcp_f32 v239, v220\n"
    "s_or_b64 s[96:97], s[96:97], vcc\n"
    "v_accvgpr_read_b32 v128, a0\n"
    "v_accvgpr_read_b32 v129, a1\n"
    "v_accvgpr_read_b32 v130, a2\n"
    "v_accvgpr_read_b32 v131, a3\n"
    "v_accvgpr_read_b32 v132, a4\n"
    "v_accvgpr_read_b32 v133, a5\n"
    "v_accvgpr_read_b32 v134, a6\n"
    "v_accvgpr_read_b32 v135, a7\n"
    "v_pk_mul_f32 v[128:129], v[128:129], v[236:237] op_sel:[0,0] op_sel_hi:[1,0]\n"
    "v_pk_mul_f32 v[130:131], v[130:131], v[236:237] op_sel:[0,0] op_sel_hi:[1,0]\n"
    "v_pk_mul_f32 v[132:133], v[132:133], v[236:237] op_sel:[0,0] op_sel_hi:[1,0]\n"
    "v_pk_mul_f32 v[134:135], v[134:135], v[236:237] op_sel:[0,0] op_sel_hi:[1,0]\n"
    "v_cvt_pk_bf16_f32 v128, v128, v129\n"
    "v_cvt_pk_bf16_f32 v129, v130, v131\n"
    "v_cvt_pk_bf16_f32 v130, v132, v133\n"
    "v_cvt_pk_bf16_f32 v131, v134, v135\n"
    "ds_write_b128 v255, v[128:131] offset:0\n"
    "v_accvgpr_read_b32 v136, a8\n"
    "v_accvgpr_read_b32 v137, a9\n"
    "v_accvgpr_read_b32 v138, a10\n"
    "v_accvgpr_read_b32 v139, a11\n"
    "v_accvgpr_read_b32 v140, a12\n"
    "v_accvgpr_read_b32 v141, a13\n"
    "v_accvgpr_read_b32 v142, a14\n"
    "v_accvgpr_read_b32 v143, a15\n"
    "v_pk_mul_f32 v[136:137], v[136:137], v[236:237] op_sel:[0,0] op_sel_hi:[1,0]\n"
    "v_pk_mul_f32 v[138:139], v[138:139], v[236:237] op_sel:[0,0] op_sel_hi:[1,0]\n"
    "v_pk_mul_f32 v[140:141], v[140:141], v[236:237] op_sel:[0,0] op_sel_hi:[1,0]\n"
    "v_pk_mul_f32 v[142:143], v[142:143], v[236:237] op_sel:[0,0] op_sel_hi:[1,0]\n"
    "v_cvt_pk_bf16_f32 v136, v136, v137\n"
    "v_cvt_pk_bf16_f32 v137, v138, v139\n"
    "v_cvt_pk_bf16_f32 v138, v140, v141\n"
    "v_cvt_pk_bf16_f32 v139, v142, v143\n"
    "ds_write_b128 v255, v[136:139] offset:1024\n"
    "v_accvgpr_read_b32 v144, a16\n"
    "v_accvgpr_read_b32 v145, a17\n"
    "v_accvgpr_read_b32 v146, a18\n"
    "v_accvgpr_read_b32 v147, a19\n"
    "v_accvgpr_read_b32 v148, a20\n"
    "v_accvgpr_read_b32 v149, a21\n"
    "v_accvgpr_read_b32 v150, a22\n"
    "v_accvgpr_read_b32 v151, a23\n"
    "v_pk_mul_f32 v[144:145], v[144:145], v[236:237] op_sel:[0,0] op_sel_hi:[1,0]\n"
    "v_pk_mul_f32 v[146:147], v[146:147], v[236:237] op_sel:[0,0] op_sel_hi:[1,0]\n"
    "v_pk_mul_f32 v[148:149], v[148:149], v[236:237] op_sel:[0,0] op_sel_hi:[1,0]\n"
    "v_pk_mul_f32 v[150:151], v[150:151], v[236:237] op_sel:[0,0] op_sel_hi:[1,0]\n"
    "v_cvt_pk_bf16_f32 v144, v144, v145\n"
    "v_cvt_pk_bf16_f32 v145, v146, v147\n"
    "v_cvt_pk_bf16_f32 v146, v148, v149\n"
    "v_cvt_pk_bf16_f32 v147, v150, v151\n"
    "ds_write_b128 v255, v[144:147] offset:2048\n"
    "v_accvgpr_read_b32 v152, a24\n"
    "v_accvgpr_read_b32 v153, a25\n"
    "v_accvgpr_read_b32 v154, a26\n"
    "v_accvgpr_read_b32 v155, a27\n"
    "v_accvgpr_read_b32 v156, a28\n"
    "v_accvgpr_read_b32 v157, a29\n"
    "v_accvgpr_read_b32 v158, a30\n"
    "v_accvgpr_read_b32 v159, a31\n"
    "v_pk_mul_f32 v[152:153], v[152:153], v[236:237] op_sel:[0,0] op_sel_hi:[1,0]\n"
    "v_pk_mul_f32 v[154:155], v[154:155], v[236:237] op_sel:[0,0] op_sel_hi:[1,0]\n"
    "v_pk_mul_f32 v[156:157], v[156:157], v[236:237] op_sel:[0,0] op_sel_hi:[1,0]\n"
    "v_pk_mul_f32 v[158:159], v[158:159], v[236:237] op_sel:[0,0] op_sel_hi:[1,0]\n"
    "v_cvt_pk_bf16_f32 v152, v152, v153\n"
    "v_cvt_pk_bf16_f32 v153, v154, v155\n"
    "v_cvt_pk_bf16_f32 v154, v156, v157\n"
    "v_cvt_pk_bf16_f32 v155, v158, v159\n"
    "ds_write_b128 v255, v[152:155] offset:3072\n"
    "v_accvgpr_read_b32 v160, a32\n"
    "v_accvgpr_read_b32 v161, a33\n"
    "v_accvgpr_read_b32 v162, a34\n"
    "v_accvgpr_read_b32 v163, a35\n"
    "v_accvgpr_read_b32 v164, a36\n"
    "v_accvgpr_read_b32 v165, a37\n"
    "v_accvgpr_read_b32 v166, a38\n"
    "v_accvgpr_read_b32 v167, a39\n"
    "v_pk_mul_f32 v[160:161], v[160:161], v[236:237] op_sel:[0,1] op_sel_hi:[1,1]\n"
    "v_pk_mul_f32 v[162:163], v[162:163], v[236:237] op_sel:[0,1] op_sel_hi:[1,1]\n"
    "v_pk_mul_f32 v[164:165], v[164:165], v[236:237] op_sel:[0,1] op_sel_hi:[1,1]\n"
    "v_pk_mul_f32 v[166:167], v[166:167], v[236:237] op_sel:[0,1] op_sel_hi:[1,1]\n"
    "v_cvt_pk_bf16_f32 v160, v160, v161\n"
    "v_cvt_pk_bf16_f32 v161, v162, v163\n"
    "v_cvt_pk_bf16_f32 v162, v164, v165\n"
    "v_cvt_pk_bf16_f32 v163, v166, v167\n"
    "ds_write_b128 v255, v[160:163] offset:4096\n"
    "v_accvgpr_read_b32 v168, a40\n"
    "v_accvgpr_read_b32 v169, a41\n"
    "v_accvgpr_read_b32 v170, a42\n"
    "v_accvgpr_read_b32 v171, a43\n"
    "v_accvgpr_read_b32 v172, a44\n"
    "v_accvgpr_read_b32 v173, a45\n"
    "v_accvgpr_read_b32 v174, a46\n"
    "v_accvgpr_read_b32 v175, a47\n"
    "v_pk_mul_f32 v[168:169], v[168:169], v[236:237] op_sel:[0,1] op_sel_hi:[1,1]\n"
    "v_pk_mul_f32 v[170:171], v[170:171], v[236:237] op_sel:[0,1] op_sel_hi:[1,1]\n"
    "v_pk_mul_f32 v[172:173], v[172:173], v[236:237] op_sel:[0,1] op_sel_hi:[1,1]\n"
    "v_pk_mul_f32 v[174:175], v[174:175], v[236:237] op_sel:[0,1] op_sel_hi:[1,1]\n"
    "v_cvt_pk_bf16_f32 v168, v168, v169\n"
    "v_cvt_pk_bf16_f32 v169, v170, v171\n"
    "v_cvt_pk_bf16_f32 v170, v172, v173\n"
    "v_cvt_pk_bf16_f32 v171, v174, v175\n"
    "ds_write_b128 v255, v[168:171] offset:5120\n"
    "v_accvgpr_read_b32 v176, a48\n"
    "v_accvgpr_read_b32 v177, a49\n"
    "v_accvgpr_read_b32 v178, a50\n"
    "v_accvgpr_read_b32 v179, a51\n"
    "v_accvgpr_read_b32 v180, a52\n"
    "v_accvgpr_read_b32 v181, a53\n"
    "v_accvgpr_read_b32 v182, a54\n"
    "v_accvgpr_read_b32 v183, a55\n"
    "v_pk_mul_f32 v[176:177], v[176:177], v[236:237] op_sel:[0,1] op_sel_hi:[1,1]\n"
    "v_pk_mul_f32 v[178:179], v[178:179], v[236:237] op_sel:[0,1] op_sel_hi:[1,1]\n"
    "v_pk_mul_f32 v[180:181], v[180:181], v[236:237] op_sel:[0,1] op_sel_hi:[1,1]\n"
    "v_pk_mul_f32 v[182:183], v[182:183], v[236:237] op_sel:[0,1] op_sel_hi:[1,1]\n"
    "v_cvt_pk_bf16_f32 v176, v176, v177\n"
    "v_cvt_pk_bf16_f32 v177, v178, v179\n"
    "v_cvt_pk_bf16_f32 v178, v180, v181\n"
    "v_cvt_pk_bf16_f32 v179, v182, v183\n"
    "ds_write_b128 v255, v[176:179] offset:6144\n"
    "v_accvgpr_read_b32 v184, a56\n"
    "v_accvgpr_read_b32 v185, a57\n"
    "v_accvgpr_read_b32 v186, a58\n"
    "v_accvgpr_read_b32 v187, a59\n"
    "v_accvgpr_read_b32 v188, a60\n"
    "v_accvgpr_read_b32 v189, a61\n"
    "v_accvgpr_read_b32 v190, a62\n"
    "v_accvgpr_read_b32 v191, a63\n"
    "v_pk_mul_f32 v[184:185], v[184:185], v[236:237] op_sel:[0,1] op_sel_hi:[1,1]\n"
    "v_pk_mul_f32 v[186:187], v[186:187], v[236:237] op_sel:[0,1] op_sel_hi:[1,1]\n"
    "v_pk_mul_f32 v[188:189], v[188:189], v[236:237] op_sel:[0,1] op_sel_hi:[1,1]\n"
    "v_pk_mul_f32 v[190:191], v[190:191], v[236:237] op_sel:[0,1] op_sel_hi:[1,1]\n"
    "v_cvt_pk_bf16_f32 v184, v184, v185\n"
    "v_cvt_pk_bf16_f32 v185, v186, v187\n"
    "v_cvt_pk_bf16_f32 v186, v188, v189\n"
    "v_cvt_pk_bf16_f32 v187, v190, v191\n"
    "ds_write_b128 v255, v[184:187] offset:7168\n"
    "v_accvgpr_read_b32 v128, a64\n"
    "v_accvgpr_read_b32 v129, a65\n"
    "v_accvgpr_read_b32 v130, a66\n"
    "v_accvgpr_read_b32 v131, a67\n"
    "v_accvgpr_read_b32 v132, a68\n"
    "v_accvgpr_read_b32 v133, a69\n"
    "v_accvgpr_read_b32 v134, a70\n"
    "v_accvgpr_read_b32 v135, a71\n"
    "v_pk_mul_f32 v[128:129], v[128:129], v[238:239] op_sel:[0,0] op_sel_hi:[1,0]\n"
    "v_pk_mul_f32 v[130:131], v[130:131], v[238:239] op_sel:[0,0] op_sel_hi:[1,0]\n"
    "v_pk_mul_f32 v[132:133], v[132:133], v[238:239] op_sel:[0,0] op_sel_hi:[1,0]\n"
    "v_pk_mul_f32 v[134:135], v[134:135], v[238:239] op_sel:[0,0] op_sel_hi:[1,0]\n"
    "v_cvt_pk_bf16_f32 v128, v128, v129\n"
    "v_cvt_pk_bf16_f32 v129, v130, v131\n"
    "v_cvt_pk_bf16_f32 v130, v132, v133\n"
    "v_cvt_pk_bf16_f32 v131, v134, v135\n"
    "ds_write_b128 v255, v[128:131] offset:8192\n"
    "v_accvgpr_read_b32 v136, a72\n"
    "v_accvgpr_read_b32 v137, a73\n"
    "v_accvgpr_read_b32 v138, a74\n"
    "v_accvgpr_read_b32 v139, a75\n"
    "v_accvgpr_read_b32 v140, a76\n"
    "v_accvgpr_read_b32 v141, a77\n"
    "v_accvgpr_read_b32 v142, a78\n"
    "v_accvgpr_read_b32 v143, a79\n"
    "v_pk_mul_f32 v[136:137], v[136:137], v[238:239] op_sel:[0,0] op_sel_hi:[1,0]\n"
    "v_pk_mul_f32 v[138:139], v[138:139], v[238:239] op_sel:[0,0] op_sel_hi:[1,0]\n"
    "v_pk_mul_f32 v[140:141], v[140:141], v[238:239] op_sel:[0,0] op_sel_hi:[1,0]\n"
    "v_pk_mul_f32 v[142:143], v[142:143], v[238:239] op_sel:[0,0] op_sel_hi:[1,0]\n"
    "v_cvt_pk_bf16_f32 v136, v136, v137\n"
    "v_cvt_pk_bf16_f32 v137, v138, v139\n"
    "v_cvt_pk_bf16_f32 v138, v140, v141\n"
    "v_cvt_pk_bf16_f32 v139, v142, v143\n"
    "ds_write_b128 v255, v[136:139] offset:9216\n"
    "v_accvgpr_read_b32 v144, a80\n"
    "v_accvgpr_read_b32 v145, a81\n"
    "v_accvgpr_read_b32 v146, a82\n"
    "v_accvgpr_read_b32 v147, a83\n"
    "v_accvgpr_read_b32 v148, a84\n"
    "v_accvgpr_read_b32 v149, a85\n"
    "v_accvgpr_read_b32 v150, a86\n"
    "v_accvgpr_read_b32 v151, a87\n"
    "v_pk_mul_f32 v[144:145], v[144:145], v[238:239] op_sel:[0,0] op_sel_hi:[1,0]\n"
    "v_pk_mul_f32 v[146:147], v[146:147], v[238:239] op_sel:[0,0] op_sel_hi:[1,0]\n"
    "v_pk_mul_f32 v[148:149], v[148:149], v[238:239] op_sel:[0,0] op_sel_hi:[1,0]\n"
    "v_pk_mul_f32 v[150:151], v[150:151], v[238:239] op_sel:[0,0] op_sel_hi:[1,0]\n"
    "v_cvt_pk_bf16_f32 v144, v144, v145\n"
    "v_cvt_pk_bf16_f32 v145, v146, v147\n"
    "v_cvt_pk_bf16_f32 v146, v148, v149\n"
    "v_cvt_pk_bf16_f32 v147, v150, v151\n"
    "ds_write_b128 v255, v[144:147] offset:10240\n"
    "v_accvgpr_read_b32 v152, a88\n"
    "v_accvgpr_read_b32 v153, a89\n"
    "v_accvgpr_read_b32 v154, a90\n"
    "v_accvgpr_read_b32 v155, a91\n"
    "v_accvgpr_read_b32 v156, a92\n"
    "v_accvgpr_read_b32 v157, a93\n"
    "v_accvgpr_read_b32 v158, a94\n"
    "v_accvgpr_read_b32 v159, a95\n"
    "v_pk_mul_f32 v[152:153], v[152:153], v[238:239] op_sel:[0,0] op_sel_hi:[1,0]\n"
    "v_pk_mul_f32 v[154:155], v[154:155], v[238:239] op_sel:[0,0] op_sel_hi:[1,0]\n"
    "v_pk_mul_f32 v[156:157], v[156:157], v[238:239] op_sel:[0,0] op_sel_hi:[1,0]\n"
    "v_pk_mul_f32 v[158:159], v[158:159], v[238:239] op_sel:[0,0] op_sel_hi:[1,0]\n"
    "v_cvt_pk_bf16_f32 v152, v152, v153\n"
    "v_cvt_pk_bf16_f32 v153, v154, v155\n"
    "v_cvt_pk_bf16_f32 v154, v156, v157\n"
    "v_cvt_pk_bf16_f32 v155, v158, v159\n"
    "ds_write_b128 v255, v[152:155] offset:11264\n"
    "v_accvgpr_read_b32 v160, a96\n"
    "v_accvgpr_read_b32 v161, a97\n"
    "v_accvgpr_read_b32 v162, a98\n"
    "v_accvgpr_read_b32 v163, a99\n"
    "v_accvgpr_read_b32 v164, a100\n"
    "v_accvgpr_read_b32 v165, a101\n"
    "v_accvgpr_read_b32 v166, a102\n"
    "v_accvgpr_read_b32 v167, a103\n"
    "v_pk_mul_f32 v[160:161], v[160:161], v[238:239] op_sel:[0,1] op_sel_hi:[1,1]\n"
    "v_pk_mul_f32 v[162:163], v[162:163], v[238:239] op_sel:[0,1] op_sel_hi:[1,1]\n"
    "v_pk_mul_f32 v[164:165], v[164:165], v[238:239] op_sel:[0,1] op_sel_hi:[1,1]\n"
    "v_pk_mul_f32 v[166:167], v[166:167], v[238:239] op_sel:[0,1] op_sel_hi:[1,1]\n"
    "v_cvt_pk_bf16_f32 v160, v160, v161\n"
    "v_cvt_pk_bf16_f32 v161, v162, v163\n"
    "v_cvt_pk_bf16_f32 v162, v164, v165\n"
    "v_cvt_pk_bf16_f32 v163, v166, v167\n"
    "ds_write_b128 v255, v[160:163] offset:12288\n"
    "v_accvgpr_read_b32 v168, a104\n"
    "v_accvgpr_read_b32 v169, a105\n"
    "v_accvgpr_read_b32 v170, a106\n"
    "v_accvgpr_read_b32 v171, a107\n"
    "v_accvgpr_read_b32 v172, a108\n"
    "v_accvgpr_read_b32 v173, a109\n"
    "v_accvgpr_read_b32 v174, a110\n"
    "v_accvgpr_read_b32 v175, a111\n"
    "v_pk_mul_f32 v[168:169], v[168:169], v[238:239] op_sel:[0,1] op_sel_hi:[1,1]\n"
    "v_pk_mul_f32 v[170:171], v[170:171], v[238:239] op_sel:[0,1] op_sel_hi:[1,1]\n"
    "v_pk_mul_f32 v[172:173], v[172:173], v[238:239] op_sel:[0,1] op_sel_hi:[1,1]\n"
    "v_pk_mul_f32 v[174:175], v[174:175], v[238:239] op_sel:[0,1] op_sel_hi:[1,1]\n"
    "v_cvt_pk_bf16_f32 v168, v168, v169\n"
    "v_cvt_pk_bf16_f32 v169, v170, v171\n"
    "v_cvt_pk_bf16_f32 v170, v172, v173\n"
    "v_cvt_pk_bf16_f32 v171, v174, v175\n"
    "ds_write_b128 v255, v[168:171] offset:13312\n"
    "v_accvgpr_read_b32 v176, a112\n"
    "v_accvgpr_read_b32 v177, a113\n"
    "v_accvgpr_read_b32 v178, a114\n"
    "v_accvgpr_read_b32 v179, a115\n"
    "v_accvgpr_read_b32 v180, a116\n"
    "v_accvgpr_read_b32 v181, a117\n"
    "v_accvgpr_read_b32 v182, a118\n"
    "v_accvgpr_read_b32 v183, a119\n"
    "v_pk_mul_f32 v[176:177], v[176:177], v[238:239] op_sel:[0,1] op_sel_hi:[1,1]\n"
    "v_pk_mul_f32 v[178:179], v[178:179], v[238:239] op_sel:[0,1] op_sel_hi:[1,1]\n"
    "v_pk_mul_f32 v[180:181], v[180:181], v[238:239] op_sel:[0,1] op_sel_hi:[1,1]\n"
    "v_pk_mul_f32 v[182:183], v[182:183], v[238:239] op_sel:[0,1] op_sel_hi:[1,1]\n"
    "v_cvt_pk_bf16_f32 v176, v176, v177\n"
    "v_cvt_pk_bf16_f32 v177, v178, v179\n"
    "v_cvt_pk_bf16_f32 v178, v180, v181\n"
    "v_cvt_pk_bf16_f32 v179, v182, v183\n"
    "ds_write_b128 v255, v[176:179] offset:14336\n"
    "v_accvgpr_read_b32 v184, a120\n"
    "v_accvgpr_read_b32 v185, a121\n"
    "v_accvgpr_read_b32 v186, a122\n"
    "v_accvgpr_read_b32 v187, a123\n"
    "v_accvgpr_read_b32 v188, a124\n"
    "v_accvgpr_read_b32 v189, a125\n"
    "v_accvgpr_read_b32 v190, a126\n"
    "v_accvgpr_read_b32 v191, a127\n"
    "v_pk_mul_f32 v[184:185], v[184:185], v[238:239] op_sel:[0,1] op_sel_hi:[1,1]\n"
    "v_pk_mul_f32 v[186:187], v[186:187], v[238:239] op_sel:[0,1] op_sel_hi:[1,1]\n"
    "v_pk_mul_f32 v[188:189], v[188:189], v[238:239] op_sel:[0,1] op_sel_hi:[1,1]\n"
    "v_pk_mul_f32 v[190:191], v[190:191], v[238:239] op_sel:[0,1] op_sel_hi:[1,1]\n"
    "v_cvt_pk_bf16_f32 v184, v184, v185\n"
    "v_cvt_pk_bf16_f32 v185, v186, v187\n"
    "v_cvt_pk_bf16_f32 v186, v188, v189\n"
    "v_cvt_pk_bf16_f32 v187, v190, v191\n"
    "ds_write_b128 v255, v[184:187] offset:15360\n"
    "s_nop 1\n"
    "s_movk_i32 s74, 7\n"
    "P1F%=:\n"
    "v_mfma_f32_16x16x32_bf16 v[64:67], a[192:195], a[128:131], v[192:195]\n"
    "v_exp_f32 v40, v40\n"
    "v_mfma_f32_16x16x32_bf16 v[72:75], a[192:195], a[144:147], v[196:199]\n"
    "ds_read_b64_tr_b16 v[128:129], v250 offset:0\n"
    "ds_read_b64_tr_b16 v[130:131], v250 offset:4096\n"
    "v_mfma_f32_16x16x32_bf16 v[80:83], a[192:195], a[160:163], v[200:203]\n"
    "v_cvt_pk_bf16_f32 v32, v32, v33\n"
    "v_cvt_pk_bf16_f32 v33, v34, v35\n"
    "v_mfma_f32_16x16x32_bf16 v[88:91], a[192:195], a[176:179], v[204:207]\n"
    "v_exp_f32 v41, v41\n"
    "v_mfma_f32_16x16x32_bf16 v[68:71], a[196:199], a[128:131], v[192:195]\n"
    "ds_read_b64_tr_b16 v[132:133], v251 offset:0\n"
    "ds_read_b64_tr_b16 v[134:135], v251 offset:4096\n"
    "v_mfma_f32_16x16x32_bf16 v[76:79], a[196:199], a[144:147], v[196:199]\n"
    "v_mfma_f32_16x16x32_bf16 v[84:87], a[196:199], a[160:163], v[200:203]\n"
    "v_exp_f32 v42, v42\n"
    "v_mfma_f32_16x16x32_bf16 v[92:95], a[196:199], a[176:179], v[204:207]\n"
    "ds_read_b64_tr_b16 v[136:137], v250 offset:512\n"
    "ds_read_b64_tr_b16 v[138:139], v250 offset:4608\n"
    "v_mfma_f32_16x16x32_bf16 v[96:99], a[200:203], a[128:131], v[192:195]\n"
    "v_cvt_pk_bf16_f32 v34, v36, v37\n"
    "v_cvt_pk_bf16_f32 v35, v38, v39\n"
    "v_mfma_f32_16x16x32_bf16 v[104:107], a[200:203], a[144:147], v[196:199]\n"
    "v_mfma_f32_16x16x32_bf16 v[112:115], a[200:203], a[160:163], v[200:203]\n"
    "ds_read_b64_tr_b16 v[140:141], v251 offset:512\n"
    "ds_read_b64_tr_b16 v[142:143], v251 offset:4608\n"
    "v_mfma_f32_16x16x32_bf16 v[120:123], a[200:203], a[176:179], v[204:207]\n"
    "v_exp_f32 v43, v43\n"
    "v_mfma_f32_16x16x32_bf16 v[100:103], a[204:207], a[128:131], v[192:195]\n"
    "v_exp_f32 v44, v44\n"
    "v_mfma_f32_16x16x32_bf16 v[108:111], a[204:207], a[144:147], v[196:199]\n"
    "ds_read_b64_tr_b16 v[144:145], v250 offset:1024\n"
    "ds_read_b64_tr_b16 v[146:147], v250 offset:5120\n"
    "v_mfma_f32_16x16x32_bf16 v[116:119], a[204:207], a[160:163], v[200:203]\n"
    "v_exp_f32 v45, v45\n"
    "v_mfma_f32_16x16x32_bf16 v[124:127], a[204:207], a[176:179], v[204:207]\n"
    "v_mfma_f32_16x16x32_bf16 v[64:67], a[208:211], a[132:135], v[64:67]\n"
    "ds_read_b64_tr_b16 v[148:149], v251 offset:1024\n"
    "ds_read_b64_tr_b16 v[150:151], v251 offset:5120\n"
    "v_mfma_f32_16x16x32_bf16 v[72:75], a[208:211], a[148:151], v[72:75]\n"
    "v_cvt_pk_bf16_f32 v40, v40, v41\n"
    "v_cvt_pk_bf16_f32 v41, v42, v43\n"
    "v_mfma_f32_16x16x32_bf16 v[80:83], a[208:211], a[164:167], v[80:83]\n"
    "v_exp_f32 v46, v46\n"
    "v_mfma_f32_16x16x32_bf16 v[88:91], a[208:211], a[180:183], v[88:91]\n"
    "ds_read_b64_tr_b16 v[152:153], v250 offset:1536\n"
    "ds_read_b64_tr_b16 v[154:155], v250 offset:5632\n"
    "v_mfma_f32_16x16x32_bf16 v[68:71], a[212:215], a[132:135], v[68:71]\n"
    "v_mfma_f32_16x16x32_bf16 v[76:79], a[212:215], a[148:151], v[76:79]\n"
    "v_exp_f32 v47, v47\n"
    "v_mfma_f32_16x16x32_bf16 v[84:87], a[212:215], a[164:167], v[84:87]\n"
    "ds_read_b64_tr_b16 v[156:157], v251 offset:1536\n"
    "ds_read_b64_tr_b16 v[158:159], v251 offset:5632\n"
    "v_mfma_f32_16x16x32_bf16 v[92:95], a[212:215], a[180:183], v[92:95]\n"
    "v_exp_f32 v48, v48\n"
    "v_mfma_f32_16x16x32_bf16 v[96:99], a[216:219], a[132:135], v[96:99]\n"
    "v_cvt_pk_bf16_f32 v42, v44, v45\n"
    "v_cvt_pk_bf16_f32 v43, v46, v47\n"
    "v_mfma_f32_16x16x32_bf16 v[104:107], a[216:219], a[148:151], v[104:107]\n"
    "v_mfma_f32_16x16x32_bf16 v[112:115], a[216:219], a[164:167], v[112:115]\n"
    "ds_read_b64_tr_b16 v[160:161], v250 offset:8192\n"
    "ds_read_b64_tr_b16 v[162:163], v250 offset:12288\n"
    "v_mfma_f32_16x16x32_bf16 v[120:123], a[216:219], a[180:183], v[120:123]\n"
    "v_exp_f32 v49, v49\n"
    "v_mfma_f32_16x16x32_bf16 v[100:103], a[220:223], a[132:135], v[100:103]\n"
    "v_exp_f32 v50, v50\n"
    "v_mfma_f32_16x16x32_bf16 v[108:111], a[220:223], a[148:151], v[108:111]\n"
    "ds_read_b64_tr_b16 v[164:165], v251 offset:8192\n"
    "ds_read_b64_tr_b16 v[166:167], v251 offset:12288\n"
    "v_mfma_f32_16x16x32_bf16 v[116:119], a[220:223], a[164:167], v[116:119]\n"
    "v_mfma_f32_16x16x32_bf16 v[124:127], a[220:223], a[180:183], v[124:127]\n"
    "v_exp_f32 v51, v51\n"
    "v_mfma_f32_16x16x32_bf16 v[64:67], a[224:227], a[136:139], v[64:67]\n"
    "ds_read_b64_tr_b16 v[168:169], v250 offset:8704\n"
    "ds_read_b64_tr_b16 v[170:171], v250 offset:12800\n"
    "v_mfma_f32_16x16x32_bf16 v[72:75], a[224:227], a[152:155], v[72:75]\n"
    "v_exp_f32 v52, v52\n"
    "v_mfma_f32_16x16x32_bf16 v[80:83], a[224:227], a[168:171], v[80:83]\n"
    "v_mfma_f32_16x16x32_bf16 v[88:91], a[224:227], a[184:187], v[88:91]\n"
    "ds_read_b64_tr_b16 v[172:173], v251 offset:8704\n"
    "ds_read_b64_tr_b16 v[174:175], v251 offset:12800\n"
    "v_mfma_f32_16x16x32_bf16 v[68:71], a[228:231], a[136:139], v[68:71]\n"
    "v_cvt_pk_bf16_f32 v48, v48, v49\n"
    "v_cvt_pk_bf16_f32 v49, v50, v51\n"
    "v_mfma_f32_16x16x32_bf16 v[76:79], a[228:231], a[152:155], v[76:79]\n"
    "v_exp_f32 v53, v53\n"
    "v_mfma_f32_16x16x32_bf16 v[84:87], a[228:231], a[168:171], v[84:87]\n"
    "ds_read_b64_tr_b16 v[176:177], v250 offset:9216\n"
    "ds_read_b64_tr_b16 v[178:179], v250 offset:13312\n"
    "v_mfma_f32_16x16x32_bf16 v[92:95], a[228:231], a[184:187], v[92:95]\n"
    "v_exp_f32 v54, v54\n"
    "v_mfma_f32_16x16x32_bf16 v[96:99], a[232:235], a[136:139], v[96:99]\n"
    "v_mfma_f32_16x16x32_bf16 v[104:107], a[232:235], a[152:155], v[104:107]\n"
    "ds_read_b64_tr_b16 v[180:181], v251 offset:9216\n"
    "ds_read_b64_tr_b16 v[182:183], v251 offset:13312\n"
    "v_mfma_f32_16x16x32_bf16 v[112:115], a[232:235], a[168:171], v[112:115]\n"
    "v_exp_f32 v55, v55\n"
    "v_mfma_f32_16x16x32_bf16 v[120:123], a[232:235], a[184:187], v[120:123]\n"
    "v_exp_f32 v56, v56\n"
    "v_mfma_f32_16x16x32_bf16 v[100:103], a[236:239], a[136:139], v[100:103]\n"
    "ds_read_b64_tr_b16 v[184:185], v250 offset:9728\n"
    "ds_read_b64_tr_b16 v[186:187], v250 offset:13824\n"
    "v_mfma_f32_16x16x32_bf16 v[108:111], a[236:239], a[152:155], v[108:111]\n"
    "v_mfma_f32_16x16x32_bf16 v[116:119], a[236:239], a[168:171], v[116:119]\n"
    "v_cvt_pk_bf16_f32 v50, v52, v53\n"
    "v_cvt_pk_bf16_f32 v51, v54, v55\n"
    "v_mfma_f32_16x16x32_bf16 v[124:127], a[236:239], a[184:187], v[124:127]\n"
    "ds_read_b64_tr_b16 v[188:189], v251 offset:9728\n"
    "ds_read_b64_tr_b16 v[190:191], v251 offset:13824\n"
    "v_mfma_f32_16x16x32_bf16 v[64:67], a[240:243], a[140:143], v[64:67]\n"
    "v_exp_f32 v57, v57\n"
    "v_mfma_f32_16x16x32_bf16 v[72:75], a[240:243], a[156:159], v[72:75]\n"
    "v_exp_f32 v58, v58\n"
    "v_mfma_f32_16x16x32_bf16 v[80:83], a[240:243], a[172:175], v[80:83]\n"
    "v_mfma_f32_16x16x32_bf16 v[88:91], a[240:243], a[188:191], v[88:91]\n"
    "v_exp_f32 v59, v59\n"
    "v_mfma_f32_16x16x32_bf16 v[68:71], a[244:247], a[140:143], v[68:71]\n"
    "v_exp_f32 v60, v60\n"
    "v_mfma_f32_16x16x32_bf16 v[76:79], a[244:247], a[156:159], v[76:79]\n"
    "v_mfma_f32_16x16x32_bf16 v[84:87], a[244:247], a[172:175], v[84:87]\n"
    "v_cvt_pk_bf16_f32 v56, v56, v57\n"
    "v_cvt_pk_bf16_f32 v57, v58, v59\n"
    "v_mfma_f32_16x16x32_bf16 v[92:95], a[244:247], a[188:191], v[92:95]\n"
    "v_exp_f32 v61, v61\n"
    "v_mfma_f32_16x16x32_bf16 v[96:99], a[248:251], a[140:143], v[96:99]\n"
    "v_mfma_f32_16x16x32_bf16 v[104:107], a[248:251], a[156:159], v[104:107]\n"
    "v_exp_f32 v62, v62\n"
    "v_mfma_f32_16x16x32_bf16 v[112:115], a[248:251], a[172:175], v[112:115]\n"
    "v_exp_f32 v63, v63\n"
    "v_mfma_f32_16x16x32_bf16 v[120:123], a[248:251], a[188:191], v[120:123]\n"
    "s_nop 0\n"
    "v_mfma_f32_16x16x32_bf16 v[100:103], a[252:255], a[140:143], v[100:103]\n"
    "v_cvt_pk_bf16_f32 v58, v60, v61\n"
    "v_cvt_pk_bf16_f32 v59, v62, v63\n"
    "v_mfma_f32_16x16x32_bf16 v[108:111], a[252:255], a[156:159], v[108:111]\n"
    "v_mfma_f32_16x16x32_bf16 v[116:119], a[252:255], a[172:175], v[116:119]\n"
    "v_mfma_f32_16x16x32_bf16 v[124:127], a[252:255], a[188:191], v[124:127]\n"
    "s_waitcnt vmcnt(8) lgkmcnt(0)\n"
    "s_barrier\n"
    "v_mfma_f32_16x16x32_bf16 a[0:3], v[128:131], v[0:3], 0\n"
    "v_mfma_f32_16x16x32_bf16 a[32:35], v[128:131], v[8:11], 0\n"
    "s_add_u32 m0, s72, s99\n"
    "v_mfma_f32_16x16x32_bf16 a[64:67], v[128:131], v[16:19], 0\n"
    "buffer_load_dwordx4 v224, s[36:39], s70 offen offset:0 nt lds\n"
    "v_mfma_f32_16x16x32_bf16 a[96:99], v[128:131], v[24:27], 0\n"
    "v_exp_f32 v64, v64\n"
    "v_mfma_f32_16x16x32_bf16 a[4:7], v[132:135], v[0:3], 0\n"
    "buffer_load_dwordx4 v225, s[36:39], s70 offen offset:1024 nt lds\n"
    "v_mfma_f32_16x16x32_bf16 a[36:39], v[132:135], v[8:11], 0\n"
    "v_exp_f32 v65, v65\n"
    "v_mfma_f32_16x16x32_bf16 a[68:71], v[132:135], v[16:19], 0\n"
    "buffer_load_dwordx4 v226, s[36:39], s70 offen offset:2048 nt lds\n"
    "v_mfma_f32_16x16x32_bf16 a[100:103], v[132:135], v[24:27], 0\n"
    "v_exp_f32 v66, v66\n"
    "v_mfma_f32_16x16x32_bf16 a[8:11], v[136:139], v[0:3], 0\n"
    "buffer_load_dwordx4 v227, s[36:39], s70 offen offset:3072 nt lds\n"
    "v_mfma_f32_16x16x32_bf16 a[40:43], v[136:139], v[8:11], 0\n"
    "s_add_u32 m0, s73, s83\n"
    "s_add_u32 s70, s70, 16384\n"
    "v_mfma_f32_16x16x32_bf16 a[72:75], v[136:139], v[16:19], 0\n"
    "buffer_load_dwordx4 v224, s[40:43], s71 offen offset:0 nt lds\n"
    "v_mfma_f32_16x16x32_bf16 a[104:107], v[136:139], v[24:27], 0\n"
    "v_add_u32 v250, s99, v230\n"
    "v_add_u32 v251, s99, v231\n"
    "v_mfma_f32_16x16x32_bf16 a[12:15], v[140:143], v[0:3], 0\n"
    "buffer_load_dwordx4 v225, s[40:43], s71 offen offset:1024 nt lds\n"
    "v_mfma_f32_16x16x32_bf16 a[44:47], v[140:143], v[8:11], 0\n"
    "s_mov_b32 s83, s99\n"
    "s_add_u32 s99, s99, 16384\n"
    "v_mfma_f32_16x16x32_bf16 a[76:79], v[140:143], v[16:19], 0\n"
    "buffer_load_dwordx4 v226, s[40:43], s71 offen offset:2048 nt lds\n"
    "v_mfma_f32_16x16x32_bf16 a[108:111], v[140:143], v[24:27], 0\n"
    "s_cmp_eq_u32 s99, 49152\n"
    "s_cselect_b32 s99, 0, s99\n"
    "v_mfma_f32_16x16x32_bf16 a[16:19], v[144:147], v[0:3], 0\n"
    "buffer_load_dwordx4 v227, s[40:43], s71 offen offset:3072 nt lds\n"
    "v_mfma_f32_16x16x32_bf16 a[48:51], v[144:147], v[8:11], 0\n"
    "s_add_u32 s71, s71, 16384\n"
    "v_add_u32 v253, s99, v228\n"
    "v_mfma_f32_16x16x32_bf16 a[80:83], v[144:147], v[16:19], 0\n"
    "ds_read_b128 a[192:195], v253 offset:0\n"
    "ds_read_b128 a[196:199], v253 offset:4096\n"
    "v_mfma_f32_16x16x32_bf16 a[112:115], v[144:147], v[24:27], 0\n"
    "v_exp_f32 v67, v67\n"
    "v_mfma_f32_16x16x32_bf16 a[20:23], v[148:151], v[0:3], 0\n"
    "v_exp_f32 v68, v68\n"
    "v_mfma_f32_16x16x32_bf16 a[52:55], v[148:151], v[8:11], 0\n"
    "v_cvt_pk_bf16_f32 v64, v64, v65\n"
    "v_cvt_pk_bf16_f32 v65, v66, v67\n"
    "v_mfma_f32_16x16x32_bf16 a[84:87], v[148:151], v[16:19], 0\n"
    "v_exp_f32 v69, v69\n"
    "v_mfma_f32_16x16x32_bf16 a[116:119], v[148:151], v[24:27], 0\n"
    "ds_read_b128 a[200:203], v253 offset:8192\n"
    "ds_read_b128 a[204:207], v253 offset:12288\n"
    "v_mfma_f32_16x16x32_bf16 a[24:27], v[152:155], v[0:3], 0\n"
    "v_exp_f32 v70, v70\n"
    "v_mfma_f32_16x16x32_bf16 a[56:59], v[152:155], v[8:11], 0\n"
    "v_exp_f32 v71, v71\n"
    "v_mfma_f32_16x16x32_bf16 a[88:91], v[152:155], v[16:19], 0\n"
    "v_exp_f32 v72, v72\n"
    "v_mfma_f32_16x16x32_bf16 a[120:123], v[152:155], v[24:27], 0\n"
    "v_cvt_pk_bf16_f32 v66, v68, v69\n"
    "v_cvt_pk_bf16_f32 v67, v70, v71\n"
    "v_mfma_f32_16x16x32_bf16 a[28:31], v[156:159], v[0:3], 0\n"
    "ds_read_b128 a[208:211], v253 offset:512\n"
    "ds_read_b128 a[212:215], v253 offset:4608\n"
    "v_mfma_f32_16x16x32_bf16 a[60:63], v[156:159], v[8:11], 0\n"
    "v_exp_f32 v73, v73\n"
    "v_mfma_f32_16x16x32_bf16 a[92:95], v[156:159], v[16:19], 0\n"
    "v_exp_f32 v74, v74\n"
    "v_mfma_f32_16x16x32_bf16 a[124:127], v[156:159], v[24:27], 0\n"
    "v_exp_f32 v75, v75\n"
    "v_mfma_f32_16x16x32_bf16 v[208:211], v[232:235], v[0:3], 0\n"
    "v_exp_f32 v76, v76\n"
    "v_mfma_f32_16x16x32_bf16 v[212:215], v[232:235], v[8:11], 0\n"
    "ds_read_b128 a[216:219], v253 offset:8704\n"
    "ds_read_b128 a[220:223], v253 offset:12800\n"
    "v_mfma_f32_16x16x32_bf16 v[216:219], v[232:235], v[16:19], 0\n"
    "v_cvt_pk_bf16_f32 v72, v72, v73\n"
    "v_cvt_pk_bf16_f32 v73, v74, v75\n"
    "v_mfma_f32_16x16x32_bf16 v[220:223], v[232:235], v[24:27], 0\n"
    "v_exp_f32 v77, v77\n"
    "v_mfma_f32_16x16x32_bf16 a[0:3], v[160:163], v[32:35], a[0:3]\n"
    "v_exp_f32 v78, v78\n"
    "v_mfma_f32_16x16x32_bf16 a[32:35], v[160:163], v[40:43], a[32:35]\n"
    "v_exp_f32 v79, v79\n"
    "v_mfma_f32_16x16x32_bf16 a[64:67], v[160:163], v[48:51], a[64:67]\n"
    "ds_read_b128 a[224:227], v253 offset:1024\n"
    "ds_read_b128 a[228:231], v253 offset:5120\n"
    "v_mfma_f32_16x16x32_bf16 a[96:99], v[160:163], v[56:59], a[96:99]\n"
    "v_exp_f32 v80, v80\n"
    "v_mfma_f32_16x16x32_bf16 a[4:7], v[164:167], v[32:35], a[4:7]\n"
    "v_cvt_pk_bf16_f32 v74, v76, v77\n"
    "v_cvt_pk_bf16_f32 v75, v78, v79\n"
    "v_mfma_f32_16x16x32_bf16 a[36:39], v[164:167], v[40:43], a[36:39]\n"
    "v_exp_f32 v81, v81\n"
    "v_mfma_f32_16x16x32_bf16 a[68:71], v[164:167], v[48:51], a[68:71]\n"
    "v_exp_f32 v82, v82\n"
    "v_mfma_f32_16x16x32_bf16 a[100:103], v[164:167], v[56:59], a[100:103]\n"
    "v_exp_f32 v83, v83\n"
    "v_mfma_f32_16x16x32_bf16 a[8:11], v[168:171], v[32:35], a[8:11]\n"
    "ds_read_b128 a[232:235], v253 offset:9216\n"
    "ds_read_b128 a[236:239], v253 offset:13312\n"
    "v_mfma_f32_16x16x32_bf16 a[40:43], v[168:171], v[40:43], a[40:43]\n"
    "v_exp_f32 v84, v84\n"
    "v_mfma_f32_16x16x32_bf16 a[72:75], v[168:171], v[48:51], a[72:75]\n"
    "v_cvt_pk_bf16_f32 v80, v80, v81\n"
    "v_cvt_pk_bf16_f32 v81, v82, v83\n"
    "v_mfma_f32_16x16x32_bf16 a[104:107], v[168:171], v[56:59], a[104:107]\n"
    "v_exp_f32 v85, v85\n"
    "v_mfma_f32_16x16x32_bf16 a[12:15], v[172:175], v[32:35], a[12:15]\n"
    "v_exp_f32 v86, v86\n"
    "v_mfma_f32_16x16x32_bf16 a[44:47], v[172:175], v[40:43], a[44:47]\n"
    "ds_read_b128 a[240:243], v253 offset:1536\n"
    "ds_read_b128 a[244:247], v253 offset:5632\n"
    "v_mfma_f32_16x16x32_bf16 a[76:79], v[172:175], v[48:51], a[76:79]\n"
    "v_exp_f32 v87, v87\n"
    "v_mfma_f32_16x16x32_bf16 a[108:111], v[172:175], v[56:59], a[108:111]\n"
    "v_exp_f32 v88, v88\n"
    "v_mfma_f32_16x16x32_bf16 a[16:19], v[176:179], v[32:35], a[16:19]\n"
    "v_cvt_pk_bf16_f32 v82, v84, v85\n"
    "v_cvt_pk_bf16_f32 v83, v86, v87\n"
    "v_mfma_f32_16x16x32_bf16 a[48:51], v[176:179], v[40:43], a[48:51]\n"
    "v_exp_f32 v89, v89\n"
    "v_mfma_f32_16x16x32_bf16 a[80:83], v[176:179], v[48:51], a[80:83]\n"
    "ds_read_b128 a[248:251], v253 offset:9728\n"
    "ds_read_b128 a[252:255], v253 offset:13824\n"
    "v_mfma_f32_16x16x32_bf16 a[112:115], v[176:179], v[56:59], a[112:115]\n"
    "v_exp_f32 v90, v90\n"
    "v_mfma_f32_16x16x32_bf16 a[20:23], v[180:183], v[32:35], a[20:23]\n"
    "v_exp_f32 v91, v91\n"
    "v_mfma_f32_16x16x32_bf16 a[52:55], v[180:183], v[40:43], a[52:55]\n"
    "v_exp_f32 v92, v92\n"
    "v_mfma_f32_16x16x32_bf16 a[84:87], v[180:183], v[48:51], a[84:87]\n"
    "v_exp_f32 v93, v93\n"
    "v_mfma_f32_16x16x32_bf16 a[116:119], v[180:183], v[56:59], a[116:119]\n"
    "v_cvt_pk_bf16_f32 v88, v88, v89\n"
    "v_cvt_pk_bf16_f32 v89, v90, v91\n"
    "v_mfma_f32_16x16x32_bf16 a[24:27], v[184:187], v[32:35], a[24:27]\n"
    "v_exp_f32 v94, v94\n"
    "v_mfma_f32_16x16x32_bf16 a[56:59], v[184:187], v[40:43], a[56:59]\n"
    "v_exp_f32 v95, v95\n"
    "v_mfma_f32_16x16x32_bf16 a[88:91], v[184:187], v[48:51], a[88:91]\n"
    "v_exp_f32 v96, v96\n"
    "v_mfma_f32_16x16x32_bf16 a[120:123], v[184:187], v[56:59], a[120:123]\n"
    "v_exp_f32 v97, v97\n"
    "v_mfma_f32_16x16x32_bf16 a[28:31], v[188:191], v[32:35], a[28:31]\n"
    "v_exp_f32 v98, v98\n"
    "v_mfma_f32_16x16x32_bf16 a[60:63], v[188:191], v[40:43], a[60:63]\n"
    "v_cvt_pk_bf16_f32 v90, v92, v93\n"
    "v_cvt_pk_bf16_f32 v91, v94, v95\n"
    "v_mfma_f32_16x16x32_bf16 a[92:95], v[188:191], v[48:51], a[92:95]\n"
    "v_exp_f32 v99, v99\n"
    "v_mfma_f32_16x16x32_bf16 a[124:127], v[188:191], v[56:59], a[124:127]\n"
    "v_exp_f32 v100, v100\n"
    "v_mfma_f32_16x16x32_bf16 v[208:211], v[232:235], v[32:35], v[208:211]\n"
    "v_exp_f32 v101, v101\n"
    "v_mfma_f32_16x16x32_bf16 v[212:215], v[232:235], v[40:43], v[212:215]\n"
    "v_exp_f32 v102, v102\n"
    "v_mfma_f32_16x16x32_bf16 v[216:219], v[232:235], v[48:51], v[216:219]\n"
    "v_exp_f32 v103, v103\n"
    "v_mfma_f32_16x16x32_bf16 v[220:223], v[232:235], v[56:59], v[220:223]\n"
    "s_waitcnt lgkmcnt(0)\n"
    "LPF%=:\n"
    "v_mfma_f32_16x16x32_bf16 v[0:3], a[192:195], a[128:131], v[192:195]\n"
    "v_exp_f32 v104, v104\n"
    "v_mfma_f32_16x16x32_bf16 v[8:11], a[192:195], a[144:147], v[196:199]\n"
    "ds_read_b64_tr_b16 v[128:129], v250 offset:0\n"
    "ds_read_b64_tr_b16 v[130:131], v250 offset:4096\n"
    "v_mfma_f32_16x16x32_bf16 v[16:19], a[192:195], a[160:163], v[200:203]\n"
    "v_cvt_pk_bf16_f32 v96, v96, v97\n"
    "v_cvt_pk_bf16_f32 v97, v98, v99\n"
    "v_mfma_f32_16x16x32_bf16 v[24:27], a[192:195], a[176:179], v[204:207]\n"
    "v_exp_f32 v105, v105\n"
    "v_mfma_f32_16x16x32_bf16 v[4:7], a[196:199], a[128:131], v[192:195]\n"
    "ds_read_b64_tr_b16 v[132:133], v251 offset:0\n"
    "ds_read_b64_tr_b16 v[134:135], v251 offset:4096\n"
    "v_mfma_f32_16x16x32_bf16 v[12:15], a[196:199], a[144:147], v[196:199]\n"
    "v_exp_f32 v106, v106\n"
    "v_mfma_f32_16x16x32_bf16 v[20:23], a[196:199], a[160:163], v[200:203]\n"
    "v_mfma_f32_16x16x32_bf16 v[28:31], a[196:199], a[176:179], v[204:207]\n"
    "ds_read_b64_tr_b16 v[136:137], v250 offset:512\n"
    "ds_read_b64_tr_b16 v[138:139], v250 offset:4608\n"
    "v_mfma_f32_16x16x32_bf16 v[32:35], a[200:203], a[128:131], v[192:195]\n"
    "v_cvt_pk_bf16_f32 v98, v100, v101\n"
    "v_cvt_pk_bf16_f32 v99, v102, v103\n"
    "v_mfma_f32_16x16x32_bf16 v[40:43], a[200:203], a[144:147], v[196:199]\n"
    "v_exp_f32 v107, v107\n"
    "v_mfma_f32_16x16x32_bf16 v[48:51], a[200:203], a[160:163], v[200:203]\n"
    "ds_read_b64_tr_b16 v[140:141], v251 offset:512\n"
    "ds_read_b64_tr_b16 v[142:143], v251 offset:4608\n"
    "v_mfma_f32_16x16x32_bf16 v[56:59], a[200:203], a[176:179], v[204:207]\n"
    "v_exp_f32 v108, v108\n"
    "v_mfma_f32_16x16x32_bf16 v[36:39], a[204:207], a[128:131], v[192:195]\n"
    "v_mfma_f32_16x16x32_bf16 v[44:47], a[204:207], a[144:147], v[196:199]\n"
    "ds_read_b64_tr_b16 v[144:145], v250 offset:1024\n"
    "ds_read_b64_tr_b16 v[146:147], v250 offset:5120\n"
    "v_mfma_f32_16x16x32_bf16 v[52:55], a[204:207], a[160:163], v[200:203]\n"
    "v_exp_f32 v109, v109\n"
    "v_mfma_f32_16x16x32_bf16 v[60:63], a[204:207], a[176:179], v[204:207]\n"
    "v_cvt_pk_bf16_f32 v104, v104, v105\n"
    "v_cvt_pk_bf16_f32 v105, v106, v107\n"
    "v_mfma_f32_16x16x32_bf16 v[0:3], a[208:211], a[132:135], v[0:3]\n"
    "ds_read_b64_tr_b16 v[148:149], v251 offset:1024\n"
    "ds_read_b64_tr_b16 v[150:151], v251 offset:5120\n"
    "v_mfma_f32_16x16x32_bf16 v[8:11], a[208:211], a[148:151], v[8:11]\n"
    "ds_read_b128 a[192:195], v255 offset:0\n"
    "ds_read_b128 a[196:199], v255 offset:1024\n"
    "v_mfma_f32_16x16x32_bf16 v[16:19], a[208:211], a[164:167], v[16:19]\n"
    "v_exp_f32 v110, v110\n"
    "v_mfma_f32_16x16x32_bf16 v[24:27], a[208:211], a[180:183], v[24:27]\n"
    "ds_read_b64_tr_b16 v[152:153], v250 offset:1536\n"
    "ds_read_b64_tr_b16 v[154:155], v250 offset:5632\n"
    "v_mfma_f32_16x16x32_bf16 v[4:7], a[212:215], a[132:135], v[4:7]\n"
    "v_mfma_f32_16x16x32_bf16 v[12:15], a[212:215], a[148:151], v[12:15]\n"
    "v_exp_f32 v111, v111\n"
    "v_mfma_f32_16x16x32_bf16 v[20:23], a[212:215], a[164:167], v[20:23]\n"
    "ds_read_b64_tr_b16 v[156:157], v251 offset:1536\n"
    "ds_read_b64_tr_b16 v[158:159], v251 offset:5632\n"
    "v_mfma_f32_16x16x32_bf16 v[28:31], a[212:215], a[180:183], v[28:31]\n"
    "v_exp_f32 v112, v112\n"
    "v_mfma_f32_16x16x32_bf16 v[32:35], a[216:219], a[132:135], v[32:35]\n"
    "v_cvt_pk_bf16_f32 v106, v108, v109\n"
    "v_cvt_pk_bf16_f32 v107, v110, v111\n"
    "v_mfma_f32_16x16x32_bf16 v[40:43], a[216:219], a[148:151], v[40:43]\n"
    "v_mfma_f32_16x16x32_bf16 v[48:51], a[216:219], a[164:167], v[48:51]\n"
    "ds_read_b64_tr_b16 v[160:161], v250 offset:8192\n"
    "ds_read_b64_tr_b16 v[162:163], v250 offset:12288\n"
    "v_mfma_f32_16x16x32_bf16 v[56:59], a[216:219], a[180:183], v[56:59]\n"
    "v_exp_f32 v113, v113\n"
    "v_mfma_f32_16x16x32_bf16 v[36:39], a[220:223], a[132:135], v[36:39]\n"
    "v_exp_f32 v114, v114\n"
    "v_mfma_f32_16x16x32_bf16 v[44:47], a[220:223], a[148:151], v[44:47]\n"
    "ds_read_b64_tr_b16 v[164:165], v251 offset:8192\n"
    "ds_read_b64_tr_b16 v[166:167], v251 offset:12288\n"
    "v_mfma_f32_16x16x32_bf16 v[52:55], a[220:223], a[164:167], v[52:55]\n"
    "v_exp_f32 v115, v115\n"
    "v_mfma_f32_16x16x32_bf16 v[60:63], a[220:223], a[180:183], v[60:63]\n"
    "v_mfma_f32_16x16x32_bf16 v[0:3], a[224:227], a[136:139], v[0:3]\n"
    "ds_read_b64_tr_b16 v[168:169], v250 offset:8704\n"
    "ds_read_b64_tr_b16 v[170:171], v250 offset:12800\n"
    "v_mfma_f32_16x16x32_bf16 v[8:11], a[224:227], a[152:155], v[8:11]\n"
    "v_exp_f32 v116, v116\n"
    "v_mfma_f32_16x16x32_bf16 v[16:19], a[224:227], a[168:171], v[16:19]\n"
    "v_cvt_pk_bf16_f32 v112, v112, v113\n"
    "v_cvt_pk_bf16_f32 v113, v114, v115\n"
    "v_mfma_f32_16x16x32_bf16 v[24:27], a[224:227], a[184:187], v[24:27]\n"
    "ds_read_b64_tr_b16 v[172:173], v251 offset:8704\n"
    "ds_read_b64_tr_b16 v[174:175], v251 offset:12800\n"
    "v_mfma_f32_16x16x32_bf16 v[4:7], a[228:231], a[136:139], v[4:7]\n"
    "s_waitcnt lgkmcnt(12)\n"
    "buffer_store_dwordx4 a[192:195], v229, s[52:55], s82 offen offset:0 sc0 sc1\n"
    "v_mfma_f32_16x16x32_bf16 v[12:15], a[228:231], a[152:155], v[12:15]\n"
    "v_exp_f32 v117, v117\n"
    "v_mfma_f32_16x16x32_bf16 v[20:23], a[228:231], a[168:171], v[20:23]\n"
    "ds_read_b64_tr_b16 v[176:177], v250 offset:9216\n"
    "ds_read_b64_tr_b16 v[178:179], v250 offset:13312\n"
    "v_mfma_f32_16x16x32_bf16 v[28:31], a[228:231], a[184:187], v[28:31]\n"
    "v_mfma_f32_16x16x32_bf16 v[32:35], a[232:235], a[136:139], v[32:35]\n"
    "v_exp_f32 v118, v118\n"
    "v_mfma_f32_16x16x32_bf16 v[40:43], a[232:235], a[152:155], v[40:43]\n"
    "ds_read_b64_tr_b16 v[180:181], v251 offset:9216\n"
    "ds_read_b64_tr_b16 v[182:183], v251 offset:13312\n"
    "v_mfma_f32_16x16x32_bf16 v[48:51], a[232:235], a[168:171], v[48:51]\n"
    "v_exp_f32 v119, v119\n"
    "v_mfma_f32_16x16x32_bf16 v[56:59], a[232:235], a[184:187], v[56:59]\n"
    "v_exp_f32 v120, v120\n"
    "v_mfma_f32_16x16x32_bf16 v[36:39], a[236:239], a[136:139], v[36:39]\n"
    "ds_read_b64_tr_b16 v[184:185], v250 offset:9728\n"
    "ds_read_b64_tr_b16 v[186:187], v250 offset:13824\n"
    "v_mfma_f32_16x16x32_bf16 v[44:47], a[236:239], a[152:155], v[44:47]\n"
    "v_mfma_f32_16x16x32_bf16 v[52:55], a[236:239], a[168:171], v[52:55]\n"
    "v_cvt_pk_bf16_f32 v114, v116, v117\n"
    "v_cvt_pk_bf16_f32 v115, v118, v119\n"
    "v_mfma_f32_16x16x32_bf16 v[60:63], a[236:239], a[184:187], v[60:63]\n"
    "ds_read_b64_tr_b16 v[188:189], v251 offset:9728\n"
    "ds_read_b64_tr_b16 v[190:191], v251 offset:13824\n"
    "v_mfma_f32_16x16x32_bf16 v[0:3], a[240:243], a[140:143], v[0:3]\n"
    "buffer_store_dwordx4 a[196:199], v229, s[52:55], s82 offen offset:1024 sc0 sc1\n"
    "v_mfma_f32_16x16x32_bf16 v[8:11], a[240:243], a[156:159], v[8:11]\n"
    "v_exp_f32 v121, v121\n"
    "v_mfma_f32_16x16x32_bf16 v[16:19], a[240:243], a[172:175], v[16:19]\n"
    "v_exp_f32 v122, v122\n"
    "v_mfma_f32_16x16x32_bf16 v[24:27], a[240:243], a[188:191], v[24:27]\n"
    "v_mfma_f32_16x16x32_bf16 v[4:7], a[244:247], a[140:143], v[4:7]\n"
    "v_exp_f32 v123, v123\n"
    "v_mfma_f32_16x16x32_bf16 v[12:15], a[244:247], a[156:159], v[12:15]\n"
    "v_exp_f32 v124, v124\n"
    "v_mfma_f32_16x16x32_bf16 v[20:23], a[244:247], a[172:175], v[20:23]\n"
    "v_cvt_pk_bf16_f32 v120, v120, v121\n"
    "v_cvt_pk_bf16_f32 v121, v122, v123\n"
    "v_mfma_f32_16x16x32_bf16 v[28:31], a[244:247], a[188:191], v[28:31]\n"
    "v_mfma_f32_16x16x32_bf16 v[32:35], a[248:251], a[140:143], v[32:35]\n"
    "v_exp_f32 v125, v125\n"
    "v_mfma_f32_16x16x32_bf16 v[40:43], a[248:251], a[156:159], v[40:43]\n"
    "v_exp_f32 v126, v126\n"
    "v_mfma_f32_16x16x32_bf16 v[48:51], a[248:251], a[172:175], v[48:51]\n"
    "v_exp_f32 v127, v127\n"
    "v_mfma_f32_16x16x32_bf16 v[56:59], a[248:251], a[188:191], v[56:59]\n"
    "v_mfma_f32_16x16x32_bf16 v[36:39], a[252:255], a[140:143], v[36:39]\n"
    "s_nop 0\n"
    "v_mfma_f32_16x16x32_bf16 v[44:47], a[252:255], a[156:159], v[44:47]\n"
    "v_cvt_pk_bf16_f32 v122, v124, v125\n"
    "v_cvt_pk_bf16_f32 v123, v126, v127\n"
    "v_mfma_f32_16x16x32_bf16 v[52:55], a[252:255], a[172:175], v[52:55]\n"
    "v_mfma_f32_16x16x32_bf16 v[60:63], a[252:255], a[188:191], v[60:63]\n"
    "s_waitcnt vmcnt(10) lgkmcnt(0)\n"
    "s_barrier\n"
    "v_mfma_f32_16x16x32_bf16 a[0:3], v[128:131], v[64:67], a[0:3]\n"
    "v_mfma_f32_16x16x32_bf16 a[32:35], v[128:131], v[72:75], a[32:35]\n"
    "s_add_u32 m0, s72, s99\n"
    "v_mfma_f32_16x16x32_bf16 a[64:67], v[128:131], v[80:83], a[64:67]\n"
    "buffer_load_dwordx4 v224, s[36:39], s70 offen offset:0 nt lds\n"
    "v_mfma_f32_16x16x32_bf16 a[96:99], v[128:131], v[88:91], a[96:99]\n"
    "v_exp_f32 v0, v0\n"
    "v_mfma_f32_16x16x32_bf16 a[4:7], v[132:135], v[64:67], a[4:7]\n"
    "buffer_load_dwordx4 v225, s[36:39], s70 offen offset:1024 nt lds\n"
    "v_mfma_f32_16x16x32_bf16 a[36:39], v[132:135], v[72:75], a[36:39]\n"
    "v_exp_f32 v1, v1\n"
    "v_mfma_f32_16x16x32_bf16 a[68:71], v[132:135], v[80:83], a[68:71]\n"
    "buffer_load_dwordx4 v226, s[36:39], s70 offen offset:2048 nt lds\n"
    "v_mfma_f32_16x16x32_bf16 a[100:103], v[132:135], v[88:91], a[100:103]\n"
    "v_exp_f32 v2, v2\n"
    "v_mfma_f32_16x16x32_bf16 a[8:11], v[136:139], v[64:67], a[8:11]\n"
    "buffer_load_dwordx4 v227, s[36:39], s70 offen offset:3072 nt lds\n"
    "v_mfma_f32_16x16x32_bf16 a[40:43], v[136:139], v[72:75], a[40:43]\n"
    "s_add_u32 m0, s73, s83\n"
    "s_add_u32 s70, s70, 16384\n"
    "v_mfma_f32_16x16x32_bf16 a[72:75], v[136:139], v[80:83], a[72:75]\n"
    "buffer_load_dwordx4 v224, s[40:43], s71 offen offset:0 nt lds\n"
    "v_mfma_f32_16x16x32_bf16 a[104:107], v[136:139], v[88:91], a[104:107]\n"
    "v_add_u32 v250, s99, v230\n"
    "v_add_u32 v251, s99, v231\n"
    "v_mfma_f32_16x16x32_bf16 a[12:15], v[140:143], v[64:67], a[12:15]\n"
    "buffer_load_dwordx4 v225, s[40:43], s71 offen offset:1024 nt lds\n"
    "v_mfma_f32_16x16x32_bf16 a[44:47], v[140:143], v[72:75], a[44:47]\n"
    "s_mov_b32 s83, s99\n"
    "s_add_u32 s99, s99, 16384\n"
    "v_mfma_f32_16x16x32_bf16 a[76:79], v[140:143], v[80:83], a[76:79]\n"
    "buffer_load_dwordx4 v226, s[40:43], s71 offen offset:2048 nt lds\n"
    "v_mfma_f32_16x16x32_bf16 a[108:111], v[140:143], v[88:91], a[108:111]\n"
    "s_cmp_eq_u32 s99, 49152\n"
    "s_cselect_b32 s99, 0, s99\n"
    "v_mfma_f32_16x16x32_bf16 a[16:19], v[144:147], v[64:67], a[16:19]\n"
    "buffer_load_dwordx4 v227, s[40:43], s71 offen offset:3072 nt lds\n"
    "v_mfma_f32_16x16x32_bf16 a[48:51], v[144:147], v[72:75], a[48:51]\n"
    "s_add_u32 s71, s71, 16384\n"
    "v_add_u32 v253, s99, v228\n"
    "v_mfma_f32_16x16x32_bf16 a[80:83], v[144:147], v[80:83], a[80:83]\n"
    "ds_read_b128 a[192:195], v253 offset:0\n"
    "ds_read_b128 a[196:199], v253 offset:4096\n"
    "v_mfma_f32_16x16x32_bf16 a[112:115], v[144:147], v[88:91], a[112:115]\n"
    "v_exp_f32 v3, v3\n"
    "v_mfma_f32_16x16x32_bf16 a[20:23], v[148:151], v[64:67], a[20:23]\n"
    "v_exp_f32 v4, v4\n"
    "v_mfma_f32_16x16x32_bf16 a[52:55], v[148:151], v[72:75], a[52:55]\n"
    "v_cvt_pk_bf16_f32 v0, v0, v1\n"
    "v_cvt_pk_bf16_f32 v1, v2, v3\n"
    "v_mfma_f32_16x16x32_bf16 a[84:87], v[148:151], v[80:83], a[84:87]\n"
    "v_exp_f32 v5, v5\n"
    "v_mfma_f32_16x16x32_bf16 a[116:119], v[148:151], v[88:91], a[116:119]\n"
    "ds_read_b128 a[200:203], v253 offset:8192\n"
    "ds_read_b128 a[204:207], v253 offset:12288\n"
    "v_mfma_f32_16x16x32_bf16 a[24:27], v[152:155], v[64:67], a[24:27]\n"
    "v_exp_f32 v6, v6\n"
    "v_mfma_f32_16x16x32_bf16 a[56:59], v[152:155], v[72:75], a[56:59]\n"
    "v_exp_f32 v7, v7\n"
    "v_mfma_f32_16x16x32_bf16 a[88:91], v[152:155], v[80:83], a[88:91]\n"
    "v_exp_f32 v8, v8\n"
    "v_mfma_f32_16x16x32_bf16 a[120:123], v[152:155], v[88:91], a[120:123]\n"
    "v_cvt_pk_bf16_f32 v2, v4, v5\n"
    "v_cvt_pk_bf16_f32 v3, v6, v7\n"
    "v_mfma_f32_16x16x32_bf16 a[28:31], v[156:159], v[64:67], a[28:31]\n"
    "ds_read_b128 a[208:211], v253 offset:512\n"
    "ds_read_b128 a[212:215], v253 offset:4608\n"
    "v_mfma_f32_16x16x32_bf16 a[60:63], v[156:159], v[72:75], a[60:63]\n"
    "v_exp_f32 v9, v9\n"
    "v_mfma_f32_16x16x32_bf16 a[92:95], v[156:159], v[80:83], a[92:95]\n"
    "v_exp_f32 v10, v10\n"
    "v_mfma_f32_16x16x32_bf16 a[124:127], v[156:159], v[88:91], a[124:127]\n"
    "v_exp_f32 v11, v11\n"
    "v_mfma_f32_16x16x32_bf16 v[208:211], v[232:235], v[64:67], v[208:211]\n"
    "v_exp_f32 v12, v12\n"
    "v_mfma_f32_16x16x32_bf16 v[212:215], v[232:235], v[72:75], v[212:215]\n"
    "ds_read_b128 a[216:219], v253 offset:8704\n"
    "ds_read_b128 a[220:223], v253 offset:12800\n"
    "v_mfma_f32_16x16x32_bf16 v[216:219], v[232:235], v[80:83], v[216:219]\n"
    "v_cvt_pk_bf16_f32 v8, v8, v9\n"
    "v_cvt_pk_bf16_f32 v9, v10, v11\n"
    "v_mfma_f32_16x16x32_bf16 v[220:223], v[232:235], v[88:91], v[220:223]\n"
    "v_exp_f32 v13, v13\n"
    "v_mfma_f32_16x16x32_bf16 a[0:3], v[160:163], v[96:99], a[0:3]\n"
    "v_exp_f32 v14, v14\n"
    "v_mfma_f32_16x16x32_bf16 a[32:35], v[160:163], v[104:107], a[32:35]\n"
    "v_exp_f32 v15, v15\n"
    "v_mfma_f32_16x16x32_bf16 a[64:67], v[160:163], v[112:115], a[64:67]\n"
    "ds_read_b128 a[224:227], v253 offset:1024\n"
    "ds_read_b128 a[228:231], v253 offset:5120\n"
    "v_mfma_f32_16x16x32_bf16 a[96:99], v[160:163], v[120:123], a[96:99]\n"
    "v_exp_f32 v16, v16\n"
    "v_mfma_f32_16x16x32_bf16 a[4:7], v[164:167], v[96:99], a[4:7]\n"
    "v_cvt_pk_bf16_f32 v10, v12, v13\n"
    "v_cvt_pk_bf16_f32 v11, v14, v15\n"
    "v_mfma_f32_16x16x32_bf16 a[36:39], v[164:167], v[104:107], a[36:39]\n"
    "v_exp_f32 v17, v17\n"
    "v_mfma_f32_16x16x32_bf16 a[68:71], v[164:167], v[112:115], a[68:71]\n"
    "v_exp_f32 v18, v18\n"
    "v_mfma_f32_16x16x32_bf16 a[100:103], v[164:167], v[120:123], a[100:103]\n"
    "v_exp_f32 v19, v19\n"
    "v_mfma_f32_16x16x32_bf16 a[8:11], v[168:171], v[96:99], a[8:11]\n"
    "ds_read_b128 a[232:235], v253 offset:9216\n"
    "ds_read_b128 a[236:239], v253 offset:13312\n"
    "v_mfma_f32_16x16x32_bf16 a[40:43], v[168:171], v[104:107], a[40:43]\n"
    "v_exp_f32 v20, v20\n"
    "v_mfma_f32_16x16x32_bf16 a[72:75], v[168:171], v[112:115], a[72:75]\n"
    "v_cvt_pk_bf16_f32 v16, v16, v17\n"
    "v_cvt_pk_bf16_f32 v17, v18, v19\n"
    "v_mfma_f32_16x16x32_bf16 a[104:107], v[168:171], v[120:123], a[104:107]\n"
    "v_exp_f32 v21, v21\n"
    "v_mfma_f32_16x16x32_bf16 a[12:15], v[172:175], v[96:99], a[12:15]\n"
    "v_exp_f32 v22, v22\n"
    "v_mfma_f32_16x16x32_bf16 a[44:47], v[172:175], v[104:107], a[44:47]\n"
    "ds_read_b128 a[240:243], v253 offset:1536\n"
    "ds_read_b128 a[244:247], v253 offset:5632\n"
    "v_mfma_f32_16x16x32_bf16 a[76:79], v[172:175], v[112:115], a[76:79]\n"
    "v_exp_f32 v23, v23\n"
    "v_mfma_f32_16x16x32_bf16 a[108:111], v[172:175], v[120:123], a[108:111]\n"
    "v_exp_f32 v24, v24\n"
    "v_mfma_f32_16x16x32_bf16 a[16:19], v[176:179], v[96:99], a[16:19]\n"
    "v_cvt_pk_bf16_f32 v18, v20, v21\n"
    "v_cvt_pk_bf16_f32 v19, v22, v23\n"
    "v_mfma_f32_16x16x32_bf16 a[48:51], v[176:179], v[104:107], a[48:51]\n"
    "v_exp_f32 v25, v25\n"
    "v_mfma_f32_16x16x32_bf16 a[80:83], v[176:179], v[112:115], a[80:83]\n"
    "ds_read_b128 a[248:251], v253 offset:9728\n"
    "ds_read_b128 a[252:255], v253 offset:13824\n"
    "v_mfma_f32_16x16x32_bf16 a[112:115], v[176:179], v[120:123], a[112:115]\n"
    "v_exp_f32 v26, v26\n"
    "v_mfma_f32_16x16x32_bf16 a[20:23], v[180:183], v[96:99], a[20:23]\n"
    "v_exp_f32 v27, v27\n"
    "v_mfma_f32_16x16x32_bf16 a[52:55], v[180:183], v[104:107], a[52:55]\n"
    "v_exp_f32 v28, v28\n"
    "v_mfma_f32_16x16x32_bf16 a[84:87], v[180:183], v[112:115], a[84:87]\n"
    "v_exp_f32 v29, v29\n"
    "v_mfma_f32_16x16x32_bf16 a[116:119], v[180:183], v[120:123], a[116:119]\n"
    "v_cvt_pk_bf16_f32 v24, v24, v25\n"
    "v_cvt_pk_bf16_f32 v25, v26, v27\n"
    "v_mfma_f32_16x16x32_bf16 a[24:27], v[184:187], v[96:99], a[24:27]\n"
    "v_exp_f32 v30, v30\n"
    "v_mfma_f32_16x16x32_bf16 a[56:59], v[184:187], v[104:107], a[56:59]\n"
    "v_exp_f32 v31, v31\n"
    "v_mfma_f32_16x16x32_bf16 a[88:91], v[184:187], v[112:115], a[88:91]\n"
    "v_exp_f32 v32, v32\n"
    "v_mfma_f32_16x16x32_bf16 a[120:123], v[184:187], v[120:123], a[120:123]\n"
    "v_exp_f32 v33, v33\n"
    "v_mfma_f32_16x16x32_bf16 a[28:31], v[188:191], v[96:99], a[28:31]\n"
    "v_exp_f32 v34, v34\n"
    "v_mfma_f32_16x16x32_bf16 a[60:63], v[188:191], v[104:107], a[60:63]\n"
    "v_cvt_pk_bf16_f32 v26, v28, v29\n"
    "v_cvt_pk_bf16_f32 v27, v30, v31\n"
    "v_mfma_f32_16x16x32_bf16 a[92:95], v[188:191], v[112:115], a[92:95]\n"
    "v_exp_f32 v35, v35\n"
    "v_mfma_f32_16x16x32_bf16 a[124:127], v[188:191], v[120:123], a[124:127]\n"
    "v_exp_f32 v36, v36\n"
    "v_mfma_f32_16x16x32_bf16 v[208:211], v[232:235], v[96:99], v[208:211]\n"
    "v_exp_f32 v37, v37\n"
    "v_mfma_f32_16x16x32_bf16 v[212:215], v[232:235], v[104:107], v[212:215]\n"
    "v_exp_f32 v38, v38\n"
    "v_mfma_f32_16x16x32_bf16 v[216:219], v[232:235], v[112:115], v[216:219]\n"
    "v_exp_f32 v39, v39\n"
    "v_mfma_f32_16x16x32_bf16 v[220:223], v[232:235], v[120:123], v[220:223]\n"
    "s_waitcnt lgkmcnt(0)\n"
    "v_mfma_f32_16x16x32_bf16 v[64:67], a[192:195], a[128:131], v[192:195]\n"
    "v_exp_f32 v40, v40\n"
    "v_mfma_f32_16x16x32_bf16 v[72:75], a[192:195], a[144:147], v[196:199]\n"
    "ds_read_b64_tr_b16 v[128:129], v250 offset:0\n"
    "ds_read_b64_tr_b16 v[130:131], v250 offset:4096\n"
    "v_mfma_f32_16x16x32_bf16 v[80:83], a[192:195], a[160:163], v[200:203]\n"
    "v_cvt_pk_bf16_f32 v32, v32, v33\n"
    "v_cvt_pk_bf16_f32 v33, v34, v35\n"
    "v_mfma_f32_16x16x32_bf16 v[88:91], a[192:195], a[176:179], v[204:207]\n"
    "v_exp_f32 v41, v41\n"
    "v_mfma_f32_16x16x32_bf16 v[68:71], a[196:199], a[128:131], v[192:195]\n"
    "ds_read_b64_tr_b16 v[132:133], v251 offset:0\n"
    "ds_read_b64_tr_b16 v[134:135], v251 offset:4096\n"
    "v_mfma_f32_16x16x32_bf16 v[76:79], a[196:199], a[144:147], v[196:199]\n"
    "v_exp_f32 v42, v42\n"
    "v_mfma_f32_16x16x32_bf16 v[84:87], a[196:199], a[160:163], v[200:203]\n"
    "v_mfma_f32_16x16x32_bf16 v[92:95], a[196:199], a[176:179], v[204:207]\n"
    "ds_read_b64_tr_b16 v[136:137], v250 offset:512\n"
    "ds_read_b64_tr_b16 v[138:139], v250 offset:4608\n"
    "v_mfma_f32_16x16x32_bf16 v[96:99], a[200:203], a[128:131], v[192:195]\n"
    "v_cvt_pk_bf16_f32 v34, v36, v37\n"
    "v_cvt_pk_bf16_f32 v35, v38, v39\n"
    "v_mfma_f32_16x16x32_bf16 v[104:107], a[200:203], a[144:147], v[196:199]\n"
    "v_exp_f32 v43, v43\n"
    "v_mfma_f32_16x16x32_bf16 v[112:115], a[200:203], a[160:163], v[200:203]\n"
    "ds_read_b64_tr_b16 v[140:141], v251 offset:512\n"
    "ds_read_b64_tr_b16 v[142:143], v251 offset:4608\n"
    "v_mfma_f32_16x16x32_bf16 v[120:123], a[200:203], a[176:179], v[204:207]\n"
    "v_exp_f32 v44, v44\n"
    "v_mfma_f32_16x16x32_bf16 v[100:103], a[204:207], a[128:131], v[192:195]\n"
    "v_mfma_f32_16x16x32_bf16 v[108:111], a[204:207], a[144:147], v[196:199]\n"
    "ds_read_b64_tr_b16 v[144:145], v250 offset:1024\n"
    "ds_read_b64_tr_b16 v[146:147], v250 offset:5120\n"
    "v_mfma_f32_16x16x32_bf16 v[116:119], a[204:207], a[160:163], v[200:203]\n"
    "v_exp_f32 v45, v45\n"
    "v_mfma_f32_16x16x32_bf16 v[124:127], a[204:207], a[176:179], v[204:207]\n"
    "v_cvt_pk_bf16_f32 v40, v40, v41\n"
    "v_cvt_pk_bf16_f32 v41, v42, v43\n"
    "v_mfma_f32_16x16x32_bf16 v[64:67], a[208:211], a[132:135], v[64:67]\n"
    "ds_read_b64_tr_b16 v[148:149], v251 offset:1024\n"
    "ds_read_b64_tr_b16 v[150:151], v251 offset:5120\n"
    "v_mfma_f32_16x16x32_bf16 v[72:75], a[208:211], a[148:151], v[72:75]\n"
    "ds_read_b128 a[192:195], v255 offset:2048\n"
    "ds_read_b128 a[196:199], v255 offset:3072\n"
    "v_mfma_f32_16x16x32_bf16 v[80:83], a[208:211], a[164:167], v[80:83]\n"
    "v_exp_f32 v46, v46\n"
    "v_mfma_f32_16x16x32_bf16 v[88:91], a[208:211], a[180:183], v[88:91]\n"
    "ds_read_b64_tr_b16 v[152:153], v250 offset:1536\n"
    "ds_read_b64_tr_b16 v[154:155], v250 offset:5632\n"
    "v_mfma_f32_16x16x32_bf16 v[68:71], a[212:215], a[132:135], v[68:71]\n"
    "v_mfma_f32_16x16x32_bf16 v[76:79], a[212:215], a[148:151], v[76:79]\n"
    "v_exp_f32 v47, v47\n"
    "v_mfma_f32_16x16x32_bf16 v[84:87], a[212:215], a[164:167], v[84:87]\n"
    "ds_read_b64_tr_b16 v[156:157], v251 offset:1536\n"
    "ds_read_b64_tr_b16 v[158:159], v251 offset:5632\n"
    "v_mfma_f32_16x16x32_bf16 v[92:95], a[212:215], a[180:183], v[92:95]\n"
    "v_exp_f32 v48, v48\n"
    "v_mfma_f32_16x16x32_bf16 v[96:99], a[216:219], a[132:135], v[96:99]\n"
    "v_cvt_pk_bf16_f32 v42, v44, v45\n"
    "v_cvt_pk_bf16_f32 v43, v46, v47\n"
    "v_mfma_f32_16x16x32_bf16 v[104:107], a[216:219], a[148:151], v[104:107]\n"
    "v_mfma_f32_16x16x32_bf16 v[112:115], a[216:219], a[164:167], v[112:115]\n"
    "ds_read_b64_tr_b16 v[160:161], v250 offset:8192\n"
    "ds_read_b64_tr_b16 v[162:163], v250 offset:12288\n"
    "v_mfma_f32_16x16x32_bf16 v[120:123], a[216:219], a[180:183], v[120:123]\n"
    "v_exp_f32 v49, v49\n"
    "v_mfma_f32_16x16x32_bf16 v[100:103], a[220:223], a[132:135], v[100:103]\n"
    "v_exp_f32 v50, v50\n"
    "v_mfma_f32_16x16x32_bf16 v[108:111], a[220:223], a[148:151], v[108:111]\n"
    "ds_read_b64_tr_b16 v[164:165], v251 offset:8192\n"
    "ds_read_b64_tr_b16 v[166:167], v251 offset:12288\n"
    "v_mfma_f32_16x16x32_bf16 v[116:119], a[220:223], a[164:167], v[116:119]\n"
    "v_exp_f32 v51, v51\n"
    "v_mfma_f32_16x16x32_bf16 v[124:127], a[220:223], a[180:183], v[124:127]\n"
    "v_mfma_f32_16x16x32_bf16 v[64:67], a[224:227], a[136:139], v[64:67]\n"
    "ds_read_b64_tr_b16 v[168:169], v250 offset:8704\n"
    "ds_read_b64_tr_b16 v[170:171], v250 offset:12800\n"
    "v_mfma_f32_16x16x32_bf16 v[72:75], a[224:227], a[152:155], v[72:75]\n"
    "v_exp_f32 v52, v52\n"
    "v_mfma_f32_16x16x32_bf16 v[80:83], a[224:227], a[168:171], v[80:83]\n"
    "v_cvt_pk_bf16_f32 v48, v48, v49\n"
    "v_cvt_pk_bf16_f32 v49, v50, v51\n"
    "v_mfma_f32_16x16x32_bf16 v[88:91], a[224:227], a[184:187], v[88:91]\n"
    "ds_read_b64_tr_b16 v[172:173], v251 offset:8704\n"
    "ds_read_b64_tr_b16 v[174:175], v251 offset:12800\n"
    "v_mfma_f32_16x16x32_bf16 v[68:71], a[228:231], a[136:139], v[68:71]\n"
    "s_waitcnt lgkmcnt(12)\n"
    "buffer_store_dwordx4 a[192:195], v229, s[52:55], s82 offen offset:2048 sc0 sc1\n"
    "v_mfma_f32_16x16x32_bf16 v[76:79], a[228:231], a[152:155], v[76:79]\n"
    "v_exp_f32 v53, v53\n"
    "v_mfma_f32_16x16x32_bf16 v[84:87], a[228:231], a[168:171], v[84:87]\n"
    "ds_read_b64_tr_b16 v[176:177], v250 offset:9216\n"
    "ds_read_b64_tr_b16 v[178:179], v250 offset:13312\n"
    "v_mfma_f32_16x16x32_bf16 v[92:95], a[228:231], a[184:187], v[92:95]\n"
    "v_mfma_f32_16x16x32_bf16 v[96:99], a[232:235], a[136:139], v[96:99]\n"
    "v_exp_f32 v54, v54\n"
    "v_mfma_f32_16x16x32_bf16 v[104:107], a[232:235], a[152:155], v[104:107]\n"
    "ds_read_b64_tr_b16 v[180:181], v251 offset:9216\n"
    "ds_read_b64_tr_b16 v[182:183], v251 offset:13312\n"
    "v_mfma_f32_16x16x32_bf16 v[112:115], a[232:235], a[168:171], v[112:115]\n"
    "v_exp_f32 v55, v55\n"
    "v_mfma_f32_16x16x32_bf16 v[120:123], a[232:235], a[184:187], v[120:123]\n"
    "v_exp_f32 v56, v56\n"
    "v_mfma_f32_16x16x32_bf16 v[100:103], a[236:239], a[136:139], v[100:103]\n"
    "ds_read_b64_tr_b16 v[184:185], v250 offset:9728\n"
    "ds_read_b64_tr_b16 v[186:187], v250 offset:13824\n"
    "v_mfma_f32_16x16x32_bf16 v[108:111], a[236:239], a[152:155], v[108:111]\n"
    "v_mfma_f32_16x16x32_bf16 v[116:119], a[236:239], a[168:171], v[116:119]\n"
    "v_cvt_pk_bf16_f32 v50, v52, v53\n"
    "v_cvt_pk_bf16_f32 v51, v54, v55\n"
    "v_mfma_f32_16x16x32_bf16 v[124:127], a[236:239], a[184:187], v[124:127]\n"
    "ds_read_b64_tr_b16 v[188:189], v251 offset:9728\n"
    "ds_read_b64_tr_b16 v[190:191], v251 offset:13824\n"
    "v_mfma_f32_16x16x32_bf16 v[64:67], a[240:243], a[140:143], v[64:67]\n"
    "buffer_store_dwordx4 a[196:199], v229, s[52:55], s82 offen offset:3072 sc0 sc1\n"
    "v_mfma_f32_16x16x32_bf16 v[72:75], a[240:243], a[156:159], v[72:75]\n"
    "v_exp_f32 v57, v57\n"
    "v_mfma_f32_16x16x32_bf16 v[80:83], a[240:243], a[172:175], v[80:83]\n"
    "v_exp_f32 v58, v58\n"
    "v_mfma_f32_16x16x32_bf16 v[88:91], a[240:243], a[188:191], v[88:91]\n"
    "v_mfma_f32_16x16x32_bf16 v[68:71], a[244:247], a[140:143], v[68:71]\n"
    "v_exp_f32 v59, v59\n"
    "v_mfma_f32_16x16x32_bf16 v[76:79], a[244:247], a[156:159], v[76:79]\n"
    "v_exp_f32 v60, v60\n"
    "v_mfma_f32_16x16x32_bf16 v[84:87], a[244:247], a[172:175], v[84:87]\n"
    "v_cvt_pk_bf16_f32 v56, v56, v57\n"
    "v_cvt_pk_bf16_f32 v57, v58, v59\n"
    "v_mfma_f32_16x16x32_bf16 v[92:95], a[244:247], a[188:191], v[92:95]\n"
    "v_mfma_f32_16x16x32_bf16 v[96:99], a[248:251], a[140:143], v[96:99]\n"
    "v_exp_f32 v61, v61\n"
    "v_mfma_f32_16x16x32_bf16 v[104:107], a[248:251], a[156:159], v[104:107]\n"
    "v_exp_f32 v62, v62\n"
    "v_mfma_f32_16x16x32_bf16 v[112:115], a[248:251], a[172:175], v[112:115]\n"
    "v_exp_f32 v63, v63\n"
    "v_mfma_f32_16x16x32_bf16 v[120:123], a[248:251], a[188:191], v[120:123]\n"
    "v_mfma_f32_16x16x32_bf16 v[100:103], a[252:255], a[140:143], v[100:103]\n"
    "s_nop 0\n"
    "v_mfma_f32_16x16x32_bf16 v[108:111], a[252:255], a[156:159], v[108:111]\n"
    "v_cvt_pk_bf16_f32 v58, v60, v61\n"
    "v_cvt_pk_bf16_f32 v59, v62, v63\n"
    "v_mfma_f32_16x16x32_bf16 v[116:119], a[252:255], a[172:175], v[116:119]\n"
    "v_mfma_f32_16x16x32_bf16 v[124:127], a[252:255], a[188:191], v[124:127]\n"
    "s_waitcnt vmcnt(10) lgkmcnt(0)\n"
    "s_barrier\n"
    "v_mfma_f32_16x16x32_bf16 a[0:3], v[128:131], v[0:3], a[0:3]\n"
    "s_cmp_eq_u32 s74, 0x101\n"
    "s_cbranch_scc1 QL0_%=\n"
    "QB0_%=:\n"
    "v_mfma_f32_16x16x32_bf16 a[32:35], v[128:131], v[8:11], a[32:35]\n"
    "s_add_u32 m0, s72, s99\n"
    "v_mfma_f32_16x16x32_bf16 a[64:67], v[128:131], v[16:19], a[64:67]\n"
    "buffer_load_dwordx4 v224, s[36:39], s70 offen offset:0 nt lds\n"
    "v_mfma_f32_16x16x32_bf16 a[96:99], v[128:131], v[24:27], a[96:99]\n"
    "v_exp_f32 v64, v64\n"
    "v_mfma_f32_16x16x32_bf16 a[4:7], v[132:135], v[0:3], a[4:7]\n"
    "buffer_load_dwordx4 v225, s[36:39], s70 offen offset:1024 nt lds\n"
    "v_mfma_f32_16x16x32_bf16 a[36:39], v[132:135], v[8:11], a[36:39]\n"
    "v_exp_f32 v65, v65\n"
    "v_mfma_f32_16x16x32_bf16 a[68:71], v[132:135], v[16:19], a[68:71]\n"
    "buffer_load_dwordx4 v226, s[36:39], s70 offen offset:2048 nt lds\n"
    "v_mfma_f32_16x16x32_bf16 a[100:103], v[132:135], v[24:27], a[100:103]\n"
    "v_exp_f32 v66, v66\n"
    "v_mfma_f32_16x16x32_bf16 a[8:11], v[136:139], v[0:3], a[8:11]\n"
    "buffer_load_dwordx4 v227, s[36:39], s70 offen offset:3072 nt lds\n"
    "v_mfma_f32_16x16x32_bf16 a[40:43], v[136:139], v[8:11], a[40:43]\n"
    "s_add_u32 m0, s73, s83\n"
    "s_add_u32 s70, s70, 16384\n"
    "v_mfma_f32_16x16x32_bf16 a[72:75], v[136:139], v[16:19], a[72:75]\n"
    "buffer_load_dwordx4 v224, s[40:43], s71 offen offset:0 nt lds\n"
    "v_mfma_f32_16x16x32_bf16 a[104:107], v[136:139], v[24:27], a[104:107]\n"
    "v_add_u32 v250, s99, v230\n"
    "v_add_u32 v251, s99, v231\n"
    "v_mfma_f32_16x16x32_bf16 a[12:15], v[140:143], v[0:3], a[12:15]\n"
    "buffer_load_dwordx4 v225, s[40:43], s71 offen offset:1024 nt lds\n"
    "v_mfma_f32_16x16x32_bf16 a[44:47], v[140:143], v[8:11], a[44:47]\n"
    "s_mov_b32 s83, s99\n"
    "s_add_u32 s99, s99, 16384\n"
    "v_mfma_f32_16x16x32_bf16 a[76:79], v[140:143], v[16:19], a[76:79]\n"
    "buffer_load_dwordx4 v226, s[40:43], s71 offen offset:2048 nt lds\n"
    "v_mfma_f32_16x16x32_bf16 a[108:111], v[140:143], v[24:27], a[108:111]\n"
    "s_cmp_eq_u32 s99, 49152\n"
    "s_cselect_b32 s99, 0, s99\n"
    "v_mfma_f32_16x16x32_bf16 a[16:19], v[144:147], v[0:3], a[16:19]\n"
    "buffer_load_dwordx4 v227, s[40:43], s71 offen offset:3072 nt lds\n"
    "v_mfma_f32_16x16x32_bf16 a[48:51], v[144:147], v[8:11], a[48:51]\n"
    "s_add_u32 s71, s71, 16384\n"
    "v_add_u32 v253, s99, v228\n"
    "v_mfma_f32_16x16x32_bf16 a[80:83], v[144:147], v[16:19], a[80:83]\n"
    "ds_read_b128 a[192:195], v253 offset:0\n"
    "ds_read_b128 a[196:199], v253 offset:4096\n"
    "v_mfma_f32_16x16x32_bf16 a[112:115], v[144:147], v[24:27], a[112:115]\n"
    "v_exp_f32 v67, v67\n"
    "v_mfma_f32_16x16x32_bf16 a[20:23], v[148:151], v[0:3], a[20:23]\n"
    "v_exp_f32 v68, v68\n"
    "v_mfma_f32_16x16x32_bf16 a[52:55], v[148:151], v[8:11], a[52:55]\n"
    "v_cvt_pk_bf16_f32 v64, v64, v65\n"
    "v_cvt_pk_bf16_f32 v65, v66, v67\n"
    "v_mfma_f32_16x16x32_bf16 a[84:87], v[148:151], v[16:19], a[84:87]\n"
    "v_exp_f32 v69, v69\n"
    "v_mfma_f32_16x16x32_bf16 a[116:119], v[148:151], v[24:27], a[116:119]\n"
    "ds_read_b128 a[200:203], v253 offset:8192\n"
    "ds_read_b128 a[204:207], v253 offset:12288\n"
    "v_mfma_f32_16x16x32_bf16 a[24:27], v[152:155], v[0:3], a[24:27]\n"
    "v_exp_f32 v70, v70\n"
    "v_mfma_f32_16x16x32_bf16 a[56:59], v[152:155], v[8:11], a[56:59]\n"
    "v_exp_f32 v71, v71\n"
    "v_mfma_f32_16x16x32_bf16 a[88:91], v[152:155], v[16:19], a[88:91]\n"
    "v_exp_f32 v72, v72\n"
    "v_mfma_f32_16x16x32_bf16 a[120:123], v[152:155], v[24:27], a[120:123]\n"
    "v_cvt_pk_bf16_f32 v66, v68, v69\n"
    "v_cvt_pk_bf16_f32 v67, v70, v71\n"
    "v_mfma_f32_16x16x32_bf16 a[28:31], v[156:159], v[0:3], a[28:31]\n"
    "ds_read_b128 a[208:211], v253 offset:512\n"
    "ds_read_b128 a[212:215], v253 offset:4608\n"
    "v_mfma_f32_16x16x32_bf16 a[60:63], v[156:159], v[8:11], a[60:63]\n"
    "v_exp_f32 v73, v73\n"
    "v_mfma_f32_16x16x32_bf16 a[92:95], v[156:159], v[16:19], a[92:95]\n"
    "v_exp_f32 v74, v74\n"
    "v_mfma_f32_16x16x32_bf16 a[124:127], v[156:159], v[24:27], a[124:127]\n"
    "v_exp_f32 v75, v75\n"
    "v_mfma_f32_16x16x32_bf16 v[208:211], v[232:235], v[0:3], v[208:211]\n"
    "v_exp_f32 v76, v76\n"
    "v_mfma_f32_16x16x32_bf16 v[212:215], v[232:235], v[8:11], v[212:215]\n"
    "ds_read_b128 a[216:219], v253 offset:8704\n"
    "ds_read_b128 a[220:223], v253 offset:12800\n"
    "v_mfma_f32_16x16x32_bf16 v[216:219], v[232:235], v[16:19], v[216:219]\n"
    "v_cvt_pk_bf16_f32 v72, v72, v73\n"
    "v_cvt_pk_bf16_f32 v73, v74, v75\n"
    "v_mfma_f32_16x16x32_bf16 v[220:223], v[232:235], v[24:27], v[220:223]\n"
    "v_exp_f32 v77, v77\n"
    "v_mfma_f32_16x16x32_bf16 a[0:3], v[160:163], v[32:35], a[0:3]\n"
    "v_exp_f32 v78, v78\n"
    "v_mfma_f32_16x16x32_bf16 a[32:35], v[160:163], v[40:43], a[32:35]\n"
    "v_exp_f32 v79, v79\n"
    "v_mfma_f32_16x16x32_bf16 a[64:67], v[160:163], v[48:51], a[64:67]\n"
    "ds_read_b128 a[224:227], v253 offset:1024\n"
    "ds_read_b128 a[228:231], v253 offset:5120\n"
    "v_mfma_f32_16x16x32_bf16 a[96:99], v[160:163], v[56:59], a[96:99]\n"
    "v_exp_f32 v80, v80\n"
    "v_mfma_f32_16x16x32_bf16 a[4:7], v[164:167], v[32:35], a[4:7]\n"
    "v_cvt_pk_bf16_f32 v74, v76, v77\n"
    "v_cvt_pk_bf16_f32 v75, v78, v79\n"
    "v_mfma_f32_16x16x32_bf16 a[36:39], v[164:167], v[40:43], a[36:39]\n"
    "v_exp_f32 v81, v81\n"
    "v_mfma_f32_16x16x32_bf16 a[68:71], v[164:167], v[48:51], a[68:71]\n"
    "v_exp_f32 v82, v82\n"
    "v_mfma_f32_16x16x32_bf16 a[100:103], v[164:167], v[56:59], a[100:103]\n"
    "v_exp_f32 v83, v83\n"
    "v_mfma_f32_16x16x32_bf16 a[8:11], v[168:171], v[32:35], a[8:11]\n"
    "ds_read_b128 a[232:235], v253 offset:9216\n"
    "ds_read_b128 a[236:239], v253 offset:13312\n"
    "v_mfma_f32_16x16x32_bf16 a[40:43], v[168:171], v[40:43], a[40:43]\n"
    "v_exp_f32 v84, v84\n"
    "v_mfma_f32_16x16x32_bf16 a[72:75], v[168:171], v[48:51], a[72:75]\n"
    "v_cvt_pk_bf16_f32 v80, v80, v81\n"
    "v_cvt_pk_bf16_f32 v81, v82, v83\n"
    "v_mfma_f32_16x16x32_bf16 a[104:107], v[168:171], v[56:59], a[104:107]\n"
    "v_exp_f32 v85, v85\n"
    "v_mfma_f32_16x16x32_bf16 a[12:15], v[172:175], v[32:35], a[12:15]\n"
    "v_exp_f32 v86, v86\n"
    "v_mfma_f32_16x16x32_bf16 a[44:47], v[172:175], v[40:43], a[44:47]\n"
    "ds_read_b128 a[240:243], v253 offset:1536\n"
    "ds_read_b128 a[244:247], v253 offset:5632\n"
    "v_mfma_f32_16x16x32_bf16 a[76:79], v[172:175], v[48:51], a[76:79]\n"
    "v_exp_f32 v87, v87\n"
    "v_mfma_f32_16x16x32_bf16 a[108:111], v[172:175], v[56:59], a[108:111]\n"
    "v_exp_f32 v88, v88\n"
    "v_mfma_f32_16x16x32_bf16 a[16:19], v[176:179], v[32:35], a[16:19]\n"
    "v_cvt_pk_bf16_f32 v82, v84, v85\n"
    "v_cvt_pk_bf16_f32 v83, v86, v87\n"
    "v_mfma_f32_16x16x32_bf16 a[48:51], v[176:179], v[40:43], a[48:51]\n"
    "v_exp_f32 v89, v89\n"
    "v_mfma_f32_16x16x32_bf16 a[80:83], v[176:179], v[48:51], a[80:83]\n"
    "ds_read_b128 a[248:251], v253 offset:9728\n"
    "ds_read_b128 a[252:255], v253 offset:13824\n"
    "v_mfma_f32_16x16x32_bf16 a[112:115], v[176:179], v[56:59], a[112:115]\n"
    "v_exp_f32 v90, v90\n"
    "v_mfma_f32_16x16x32_bf16 a[20:23], v[180:183], v[32:35], a[20:23]\n"
    "v_exp_f32 v91, v91\n"
    "v_mfma_f32_16x16x32_bf16 a[52:55], v[180:183], v[40:43], a[52:55]\n"
    "v_exp_f32 v92, v92\n"
    "v_mfma_f32_16x16x32_bf16 a[84:87], v[180:183], v[48:51], a[84:87]\n"
    "v_exp_f32 v93, v93\n"
    "v_mfma_f32_16x16x32_bf16 a[116:119], v[180:183], v[56:59], a[116:119]\n"
    "v_cvt_pk_bf16_f32 v88, v88, v89\n"
    "v_cvt_pk_bf16_f32 v89, v90, v91\n"
    "v_mfma_f32_16x16x32_bf16 a[24:27], v[184:187], v[32:35], a[24:27]\n"
    "v_exp_f32 v94, v94\n"
    "v_mfma_f32_16x16x32_bf16 a[56:59], v[184:187], v[40:43], a[56:59]\n"
    "v_exp_f32 v95, v95\n"
    "v_mfma_f32_16x16x32_bf16 a[88:91], v[184:187], v[48:51], a[88:91]\n"
    "s_and_b32 s76, s74, 0x1fc\n"
    "s_cmp_eq_u32 s76, 4\n"
    "s_cselect_b32 s76, 4096, 0\n"
    "v_mfma_f32_16x16x32_bf16 a[120:123], v[184:187], v[56:59], a[120:123]\n"
    "v_exp_f32 v96, v96\n"
    "v_mfma_f32_16x16x32_bf16 a[28:31], v[188:191], v[32:35], a[28:31]\n"
    "v_add_u32 v255, s76, v255\n"
    "s_lshl_b32 s76, s76, 5\n"
    "v_mfma_f32_16x16x32_bf16 a[60:63], v[188:191], v[40:43], a[60:63]\n"
    "v_exp_f32 v97, v97\n"
    "v_mfma_f32_16x16x32_bf16 a[92:95], v[188:191], v[48:51], a[92:95]\n"
    "s_add_u32 s82, s82, s76\n"
    "s_cmp_eq_u32 s74, 4\n"
    "s_cselect_b32 s54, 0, s54\n"
    "v_mfma_f32_16x16x32_bf16 a[124:127], v[188:191], v[56:59], a[124:127]\n"
    "v_exp_f32 v98, v98\n"
    "v_mfma_f32_16x16x32_bf16 v[208:211], v[232:235], v[32:35], v[208:211]\n"
    "v_cvt_pk_bf16_f32 v90, v92, v93\n"
    "v_cvt_pk_bf16_f32 v91, v94, v95\n"
    "v_mfma_f32_16x16x32_bf16 v[212:215], v[232:235], v[40:43], v[212:215]\n"
    "v_exp_f32 v99, v99\n"
    "v_mfma_f32_16x16x32_bf16 v[216:219], v[232:235], v[48:51], v[216:219]\n"
    "v_exp_f32 v100, v100\n"
    "v_exp_f32 v101, v101\n"
    "v_exp_f32 v102, v102\n"
    "v_exp_f32 v103, v103\n"
    "v_mfma_f32_16x16x32_bf16 v[220:223], v[232:235], v[56:59], v[220:223]\n"
    "s_waitcnt lgkmcnt(0)\n"
    "s_sub_u32 s74, s74, 1\n"
    "s_cmp_eq_u32 s74, 2\n"
    "s_cbranch_scc1 PBF%=\n"
    "s_and_b32 s76, s74, 0xff\n"
    "s_cbranch_scc1 LPF%=\n"
    "s_cmp_lg_u32 s74, 0\n"
    "s_cbranch_scc1 PWF%=\n"
    "s_mov_b32 s54, 2097152\n"
    "s_sub_u32 s82, s82, 520192\n"
    "s_mov_b64 s[36:37], %11\n"
    "s_and_b32 s37, s37, 0xffff\n"
    "s_mov_b32 s38, 1309760\n"
    "s_mov_b32 s39, 0x20000\n"
    "s_mov_b64 s[48:49], %4\n"
    "s_add_u32 s48, s48, %5\n"
    "s_addc_u32 s49, s49, 0\n"
    "s_and_b32 s49, s49, 0xffff\n"
    "s_mov_b32 s50, 8192\n"
    "s_mov_b32 s51, 0x20000\n"
    "s_and_b32 s63, %9, 3\n"
    "s_lshl_b32 s60, %7, 19\n"
    "s_lshl_b32 s61, s63, 13\n"
    "s_mov_b64 s[40:41], %4\n"
    "s_add_u32 s40, s40, s60\n"
    "s_addc_u32 s41, s41, 0\n"
    "s_sub_u32 s40, s40, s61\n"
    "s_subb_u32 s41, s41, 0\n"
    "s_and_b32 s41, s41, 0xffff\n"
    "s_mov_b32 s42, 425984\n"
    "s_mov_b32 s43, 0x20000\n"
    "s_lshl_b32 s70, %14, 2\n"
    "s_add_u32 s70, s70, s63\n"
    "s_lshl_b32 s70, s70, 9\n"
    "v_lshrrev_b32 v230, 4, v229\n"
    "v_and_b32 v226, 3, v230\n"
    "v_lshlrev_b32 v227, 11, v226\n"
    "v_lshlrev_b32 v226, 9, v226\n"
    "s_lshl_b32 s60, %14, 11\n"
    "s_and_b32 s61, %9, 3\n"
    "s_lshl_b32 s61, s61, 9\n"
    "s_add_u32 s61, s61, 384\n"
    "v_add_u32 v226, s60, v226\n"
    "v_add_u32 v227, s61, v227\n"
    "v_and_b32 v228, 4, v230\n"
    "v_cmp_eq_u32 vcc, 0, v228\n"
    "s_nop 1\n"
    "v_cndmask_b32 v226, v227, v226, vcc\n"
    "v_and_b32 v227, 8, v230\n"
    "v_lshl_add_u32 v226, v227, 3, v226\n"
    "v_lshrrev_b32 v1, 3, v230\n"
    "v_and_b32 v1, 2, v1\n"
    "v_lshrrev_b32 v2, 5, v230\n"
    "v_mul_u32_u24 v2, 3, v2\n"
    "v_xor_b32 v1, v1, v2\n"
    "v_and_b32 v2, 3, v230\n"
    "v_xor_b32 v1, v1, v2\n"
    "v_lshlrev_b32 v1, 4, v1\n"
    "v_lshrrev_b32 v2, 2, v230\n"
    "v_mul_u32_u24 v3, 1280, v2\n"
    "v_add_u32 v231, v3, v1\n"
    "s_add_u32 s60, %14, 0\n"
    "s_and_b32 s60, s60, 3\n"
    "s_lshl_b32 s60, s60, 17\n"
    "v_add_u32 v224, s60, v229\n"
    "s_add_u32 s60, %14, 1\n"
    "s_and_b32 s60, s60, 3\n"
    "s_lshl_b32 s60, s60, 17\n"
    "v_add_u32 v225, s60, v229\n"
    "s_add_u32 s60, %14, 2\n"
    "s_and_b32 s60, s60, 3\n"
    "s_lshl_b32 s60, s60, 17\n"
    "v_add_u32 v227, s60, v229\n"
    "s_add_u32 s60, %14, 3\n"
    "s_and_b32 s60, s60, 3\n"
    "s_lshl_b32 s60, s60, 17\n"
    "v_add_u32 v253, s60, v229\n"
    "s_mul_i32 s56, %7, 20480\n"
    "s_lshr_b32 s60, %7, 1\n"
    "s_mul_i32 s59, s60, 8192\n"
    "s_lshl_b32 s60, %7, 10\n"
    "s_add_u32 s59, s59, s60\n"
    "s_add_u32 s59, %8, s59\n"
    "s_mov_b32 s57, 0\n"
    "s_mov_b32 s62, 0x100000\n"
    "s_waitcnt vmcnt(0)\n"
    "s_barrier\n"
    "s_getpc_b64 s[44:45]\n"
    "CQ%=:\n"
    "s_add_u32 s44, s44, END%=-CQ%=\n"
    "s_addc_u32 s45, s45, 0\n"
    "s_and_b32 s45, s45, 0xffff\n"
    "s_mov_b32 s46, K2E%=-END%=\n"
    "s_mov_b32 s47, 0x20000\n"
    "s_lshl_b32 s76, %10, 2\n"
    "s_add_u32 s76, s76, s68\n"
    "s_lshl_b32 s76, s76, 10\n"
    "buffer_load_dwordx4 a[252:255], v229, s[44:47], s76 offen\n"
    "v_exp_f32 v104, v104\n"
    "buffer_load_dword v228, v226, s[48:51], 0 offen sc0 sc1\n"
    "ds_read_b64_tr_b16 v[128:129], v250 offset:0\n"
    "ds_read_b64_tr_b16 v[130:131], v250 offset:4096\n"
    "v_cvt_pk_bf16_f32 v96, v96, v97\n"
    "v_cvt_pk_bf16_f32 v97, v98, v99\n"
    "v_exp_f32 v105, v105\n"
    "ds_read_b64_tr_b16 v[132:133], v251 offset:0\n"
    "ds_read_b64_tr_b16 v[134:135], v251 offset:4096\n"
    "s_add_u32 m0, s59, 0\n"
    "s_nop 0\n"
    "buffer_load_dwordx4 v231, s[36:39], s56 offen offset:0 lds\n"
    "s_add_u32 m0, s59, 1984\n"
    "v_exp_f32 v106, v106\n"
    "ds_read_b64_tr_b16 v[136:137], v250 offset:512\n"
    "ds_read_b64_tr_b16 v[138:139], v250 offset:4608\n"
    "v_cvt_pk_bf16_f32 v98, v100, v101\n"
    "v_cvt_pk_bf16_f32 v99, v102, v103\n"
    "buffer_load_dwordx4 v231, s[36:39], s56 offen offset:64 lds\n"
    "s_add_u32 m0, s59, 3968\n"
    "ds_read_b64_tr_b16 v[140:141], v251 offset:512\n"
    "ds_read_b64_tr_b16 v[142:143], v251 offset:4608\n"
    "v_exp_f32 v107, v107\n"
    "v_exp_f32 v108, v108\n"
    "buffer_load_dwordx4 v231, s[36:39], s56 offen offset:128 lds\n"
    "s_add_u32 m0, s59, 5952\n"
    "ds_read_b64_tr_b16 v[144:145], v250 offset:1024\n"
    "ds_read_b64_tr_b16 v[146:147], v250 offset:5120\n"
    "v_exp_f32 v109, v109\n"
    "ds_read_b64_tr_b16 v[148:149], v251 offset:1024\n"
    "ds_read_b64_tr_b16 v[150:151], v251 offset:5120\n"
    "buffer_load_dwordx4 v231, s[36:39], s56 offen offset:192 lds\n"
    "s_add_u32 m0, s59, 7936\n"
    "v_cvt_pk_bf16_f32 v104, v104, v105\n"
    "v_cvt_pk_bf16_f32 v105, v106, v107\n"
    "v_exp_f32 v110, v110\n"
    "ds_read_b64_tr_b16 v[152:153], v250 offset:1536\n"
    "ds_read_b64_tr_b16 v[154:155], v250 offset:5632\n"
    "buffer_load_dwordx4 v231, s[36:39], s56 offen offset:256 lds\n"
    "s_add_u32 s56, s56, 81920\n"
    "v_exp_f32 v111, v111\n"
    "ds_read_b64_tr_b16 v[156:157], v251 offset:1536\n"
    "ds_read_b64_tr_b16 v[158:159], v251 offset:5632\n"
    "v_exp_f32 v112, v112\n"
    "v_cvt_pk_bf16_f32 v106, v108, v109\n"
    "v_cvt_pk_bf16_f32 v107, v110, v111\n"
    "s_add_u32 m0, s59, 20480\n"
    "s_nop 0\n"
    "buffer_load_dwordx4 v231, s[36:39], s56 offen offset:0 lds\n"
    "s_add_u32 m0, s59, 22464\n"
    "ds_read_b64_tr_b16 v[160:161], v250 offset:8192\n"
    "ds_read_b64_tr_b16 v[162:163], v250 offset:12288\n"
    "v_exp_f32 v113, v113\n"
    "v_exp_f32 v114, v114\n"
    "buffer_load_dwordx4 v231, s[36:39], s56 offen offset:64 lds\n"
    "s_add_u32 m0, s59, 24448\n"
    "ds_read_b64_tr_b16 v[164:165], v251 offset:8192\n"
    "ds_read_b64_tr_b16 v[166:167], v251 offset:12288\n"
    "v_exp_f32 v115, v115\n"
    "ds_read_b64_tr_b16 v[168:169], v250 offset:8704\n"
    "ds_read_b64_tr_b16 v[170:171], v250 offset:12800\n"
    "buffer_load_dwordx4 v231, s[36:39], s56 offen offset:128 lds\n"
    "s_add_u32 m0, s59, 26432\n"
    "v_exp_f32 v116, v116\n"
    "ds_read_b64_tr_b16 v[172:173], v251 offset:8704\n"
    "ds_read_b64_tr_b16 v[174:175], v251 offset:12800\n"
    "v_cvt_pk_bf16_f32 v112, v112, v113\n"
    "v_cvt_pk_bf16_f32 v113, v114, v115\n"
    "buffer_load_dwordx4 v231, s[36:39], s56 offen offset:192 lds\n"
    "s_add_u32 m0, s59, 28416\n"
    "v_exp_f32 v117, v117\n"
    "ds_read_b64_tr_b16 v[176:177], v250 offset:9216\n"
    "ds_read_b64_tr_b16 v[178:179], v250 offset:13312\n"
    "v_exp_f32 v118, v118\n"
    "buffer_load_dwordx4 v231, s[36:39], s56 offen offset:256 lds\n"
    "s_add_u32 s56, s56, 245760\n"
    "ds_read_b64_tr_b16 v[180:181], v251 offset:9216\n"
    "ds_read_b64_tr_b16 v[182:183], v251 offset:13312\n"
    "v_exp_f32 v119, v119\n"
    "v_exp_f32 v120, v120\n"
    "ds_read_b64_tr_b16 v[184:185], v250 offset:9728\n"
    "ds_read_b64_tr_b16 v[186:187], v250 offset:13824\n"
    "s_add_u32 m0, s59, 40960\n"
    "s_nop 0\n"
    "buffer_load_dwordx4 v231, s[36:39], s56 offen offset:0 lds\n"
    "s_add_u32 m0, s59, 42944\n"
    "v_cvt_pk_bf16_f32 v114, v116, v117\n"
    "v_cvt_pk_bf16_f32 v115, v118, v119\n"
    "ds_read_b64_tr_b16 v[188:189], v251 offset:9728\n"
    "ds_read_b64_tr_b16 v[190:191], v251 offset:13824\n"
    "v_exp_f32 v121, v121\n"
    "buffer_load_dwordx4 v231, s[36:39], s56 offen offset:64 lds\n"
    "s_add_u32 m0, s59, 44928\n"
    "v_exp_f32 v122, v122\n"
    "v_exp_f32 v123, v123\n"
    "v_exp_f32 v124, v124\n"
    "buffer_load_dwordx4 v231, s[36:39], s56 offen offset:128 lds\n"
    "s_add_u32 m0, s59, 46912\n"
    "v_cvt_pk_bf16_f32 v120, v120, v121\n"
    "v_cvt_pk_bf16_f32 v121, v122, v123\n"
    "v_exp_f32 v125, v125\n"
    "buffer_load_dwordx4 v231, s[36:39], s56 offen offset:192 lds\n"
    "s_add_u32 m0, s59, 48896\n"
    "v_exp_f32 v126, v126\n"
    "v_exp_f32 v127, v127\n"
    "s_nop 0\n"
    "v_cvt_pk_bf16_f32 v122, v124, v125\n"
    "v_cvt_pk_bf16_f32 v123, v126, v127\n"
    "buffer_load_dwordx4 v231, s[36:39], s56 offen offset:256 lds\n"
    "s_add_u32 s56, s56, 81920\n"
    "s_waitcnt lgkmcnt(0)\n"
    "s_nop 1\n"
    "v_mfma_f32_16x16x32_bf16 a[0:3], v[128:131], v[64:67], a[0:3]\n"
    "s_waitcnt vmcnt(15)\n"
    "v_and_b32 v228, 1, v228\n"
    "v_cmp_ne_u32 vcc, 0, v228\n"
    "s_xor_b32 s60, vcc_lo, s64\n"
    "s_andn2_b32 s60, 15, s60\n"
    "s_cselect_b32 s42, 0, s42\n"
    "v_mfma_f32_16x16x32_bf16 a[32:35], v[128:131], v[72:75], a[32:35]\n"
    "v_mfma_f32_16x16x32_bf16 a[64:67], v[128:131], v[80:83], a[64:67]\n"
    "v_mfma_f32_16x16x32_bf16 a[96:99], v[128:131], v[88:91], a[96:99]\n"
    "v_mfma_f32_16x16x32_bf16 a[4:7], v[132:135], v[64:67], a[4:7]\n"
    "buffer_load_dwordx4 v[0:3], v224, s[40:43], s57 offen offset:0 sc1\n"
    "v_mfma_f32_16x16x32_bf16 a[36:39], v[132:135], v[72:75], a[36:39]\n"
    "v_mfma_f32_16x16x32_bf16 a[68:71], v[132:135], v[80:83], a[68:71]\n"
    "v_mfma_f32_16x16x32_bf16 a[100:103], v[132:135], v[88:91], a[100:103]\n"
    "v_mfma_f32_16x16x32_bf16 a[8:11], v[136:139], v[64:67], a[8:11]\n"
    "buffer_load_dwordx4 v[4:7], v225, s[40:43], s57 offen offset:0 sc1\n"
    "v_mfma_f32_16x16x32_bf16 a[40:43], v[136:139], v[72:75], a[40:43]\n"
    "v_mfma_f32_16x16x32_bf16 a[72:75], v[136:139], v[80:83], a[72:75]\n"
    "v_mfma_f32_16x16x32_bf16 a[104:107], v[136:139], v[88:91], a[104:107]\n"
    "v_mfma_f32_16x16x32_bf16 a[12:15], v[140:143], v[64:67], a[12:15]\n"
    "buffer_load_dwordx4 v[8:11], v227, s[40:43], s57 offen offset:0 sc1\n"
    "v_mfma_f32_16x16x32_bf16 a[44:47], v[140:143], v[72:75], a[44:47]\n"
    "v_mfma_f32_16x16x32_bf16 a[76:79], v[140:143], v[80:83], a[76:79]\n"
    "v_mfma_f32_16x16x32_bf16 a[108:111], v[140:143], v[88:91], a[108:111]\n"
    "v_mfma_f32_16x16x32_bf16 a[16:19], v[144:147], v[64:67], a[16:19]\n"
    "buffer_load_dwordx4 v[12:15], v253, s[40:43], s57 offen offset:0 sc1\n"
    "v_mfma_f32_16x16x32_bf16 a[48:51], v[144:147], v[72:75], a[48:51]\n"
    "v_mfma_f32_16x16x32_bf16 a[80:83], v[144:147], v[80:83], a[80:83]\n"
    "v_mfma_f32_16x16x32_bf16 a[112:115], v[144:147], v[88:91], a[112:115]\n"
    "v_mfma_f32_16x16x32_bf16 a[20:23], v[148:151], v[64:67], a[20:23]\n"
    "buffer_load_dwordx4 v[16:19], v224, s[40:43], s57 offen offset:1024 sc1\n"
    "v_mfma_f32_16x16x32_bf16 a[52:55], v[148:151], v[72:75], a[52:55]\n"
    "v_mfma_f32_16x16x32_bf16 a[84:87], v[148:151], v[80:83], a[84:87]\n"
    "v_mfma_f32_16x16x32_bf16 a[116:119], v[148:151], v[88:91], a[116:119]\n"
    "v_mfma_f32_16x16x32_bf16 a[24:27], v[152:155], v[64:67], a[24:27]\n"
    "buffer_load_dwordx4 v[20:23], v225, s[40:43], s57 offen offset:1024 sc1\n"
    "v_mfma_f32_16x16x32_bf16 a[56:59], v[152:155], v[72:75], a[56:59]\n"
    "v_mfma_f32_16x16x32_bf16 a[88:91], v[152:155], v[80:83], a[88:91]\n"
    "v_mfma_f32_16x16x32_bf16 a[120:123], v[152:155], v[88:91], a[120:123]\n"
    "v_mfma_f32_16x16x32_bf16 a[28:31], v[156:159], v[64:67], a[28:31]\n"
    "buffer_load_dwordx4 v[24:27], v227, s[40:43], s57 offen offset:1024 sc1\n"
    "v_mfma_f32_16x16x32_bf16 a[60:63], v[156:159], v[72:75], a[60:63]\n"
    "v_mfma_f32_16x16x32_bf16 a[92:95], v[156:159], v[80:83], a[92:95]\n"
    "v_mfma_f32_16x16x32_bf16 a[124:127], v[156:159], v[88:91], a[124:127]\n"
    "v_mfma_f32_16x16x32_bf16 v[208:211], v[232:235], v[64:67], v[208:211]\n"
    "buffer_load_dwordx4 v[28:31], v253, s[40:43], s57 offen offset:1024 sc1\n"
    "v_mfma_f32_16x16x32_bf16 v[212:215], v[232:235], v[72:75], v[212:215]\n"
    "v_mfma_f32_16x16x32_bf16 v[216:219], v[232:235], v[80:83], v[216:219]\n"
    "v_mfma_f32_16x16x32_bf16 v[220:223], v[232:235], v[88:91], v[220:223]\n"
    "v_mfma_f32_16x16x32_bf16 a[0:3], v[160:163], v[96:99], a[0:3]\n"
    "buffer_load_dwordx4 v[32:35], v224, s[40:43], s57 offen offset:2048 sc1\n"
    "v_mfma_f32_16x16x32_bf16 a[32:35], v[160:163], v[104:107], a[32:35]\n"
    "v_mfma_f32_16x16x32_bf16 a[64:67], v[160:163], v[112:115], a[64:67]\n"
    "v_mfma_f32_16x16x32_bf16 a[96:99], v[160:163], v[120:123], a[96:99]\n"
    "v_mfma_f32_16x16x32_bf16 a[4:7], v[164:167], v[96:99], a[4:7]\n"
    "buffer_load_dwordx4 v[36:39], v225, s[40:43], s57 offen offset:2048 sc1\n"
    "v_mfma_f32_16x16x32_bf16 a[36:39], v[164:167], v[104:107], a[36:39]\n"
    "v_mfma_f32_16x16x32_bf16 a[68:71], v[164:167], v[112:115], a[68:71]\n"
    "v_mfma_f32_16x16x32_bf16 a[100:103], v[164:167], v[120:123], a[100:103]\n"
    "v_mfma_f32_16x16x32_bf16 a[8:11], v[168:171], v[96:99], a[8:11]\n"
    "buffer_load_dwordx4 v[40:43], v227, s[40:43], s57 offen offset:2048 sc1\n"
    "v_mfma_f32_16x16x32_bf16 a[40:43], v[168:171], v[104:107], a[40:43]\n"
    "v_mfma_f32_16x16x32_bf16 a[72:75], v[168:171], v[112:115], a[72:75]\n"
    "v_mfma_f32_16x16x32_bf16 a[104:107], v[168:171], v[120:123], a[104:107]\n"
    "v_mfma_f32_16x16x32_bf16 a[12:15], v[172:175], v[96:99], a[12:15]\n"
    "buffer_load_dwordx4 v[44:47], v253, s[40:43], s57 offen offset:2048 sc1\n"
    "v_mfma_f32_16x16x32_bf16 a[44:47], v[172:175], v[104:107], a[44:47]\n"
    "v_mfma_f32_16x16x32_bf16 a[76:79], v[172:175], v[112:115], a[76:79]\n"
    "v_mfma_f32_16x16x32_bf16 a[108:111], v[172:175], v[120:123], a[108:111]\n"
    "v_mfma_f32_16x16x32_bf16 a[16:19], v[176:179], v[96:99], a[16:19]\n"
    "buffer_load_dwordx4 v[48:51], v224, s[40:43], s57 offen offset:3072 sc1\n"
    "v_mfma_f32_16x16x32_bf16 a[48:51], v[176:179], v[104:107], a[48:51]\n"
    "v_mfma_f32_16x16x32_bf16 a[80:83], v[176:179], v[112:115], a[80:83]\n"
    "v_mfma_f32_16x16x32_bf16 a[112:115], v[176:179], v[120:123], a[112:115]\n"
    "v_mfma_f32_16x16x32_bf16 a[20:23], v[180:183], v[96:99], a[20:23]\n"
    "buffer_load_dwordx4 v[52:55], v225, s[40:43], s57 offen offset:3072 sc1\n"
    "v_mfma_f32_16x16x32_bf16 a[52:55], v[180:183], v[104:107], a[52:55]\n"
    "v_mfma_f32_16x16x32_bf16 a[84:87], v[180:183], v[112:115], a[84:87]\n"
    "v_mfma_f32_16x16x32_bf16 a[116:119], v[180:183], v[120:123], a[116:119]\n"
    "v_mfma_f32_16x16x32_bf16 a[24:27], v[184:187], v[96:99], a[24:27]\n"
    "buffer_load_dwordx4 v[56:59], v227, s[40:43], s57 offen offset:3072 sc1\n"
    "v_mfma_f32_16x16x32_bf16 a[56:59], v[184:187], v[104:107], a[56:59]\n"
    "v_mfma_f32_16x16x32_bf16 a[88:91], v[184:187], v[112:115], a[88:91]\n"
    "v_mfma_f32_16x16x32_bf16 a[120:123], v[184:187], v[120:123], a[120:123]\n"
    "v_mfma_f32_16x16x32_bf16 a[28:31], v[188:191], v[96:99], a[28:31]\n"
    "buffer_load_dwordx4 v[60:63], v253, s[40:43], s57 offen offset:3072 sc1\n"
    "v_mfma_f32_16x16x32_bf16 a[60:63], v[188:191], v[104:107], a[60:63]\n"
    "v_mfma_f32_16x16x32_bf16 a[92:95], v[188:191], v[112:115], a[92:95]\n"
    "v_mfma_f32_16x16x32_bf16 a[124:127], v[188:191], v[120:123], a[124:127]\n"
    "v_mfma_f32_16x16x32_bf16 v[208:211], v[232:235], v[96:99], v[208:211]\n"
    "s_add_u32 s57, s57, 8192\n"
    "v_mfma_f32_16x16x32_bf16 v[212:215], v[232:235], v[104:107], v[212:215]\n"
    "v_mfma_f32_16x16x32_bf16 v[216:219], v[232:235], v[112:115], v[216:219]\n"
    "v_mfma_f32_16x16x32_bf16 v[220:223], v[232:235], v[120:123], v[220:223]\n"
    "s_nop 7\n"
    "v_cmp_nlt_f32 vcc, v208, v252\n"
    "v_rcp_f32 v236, v208\n"
    "s_or_b64 s[96:97], s[96:97], vcc\n"
    "v_cmp_nlt_f32 vcc, v212, v252\n"
    "v_rcp_f32 v237, v212\n"
    "s_or_b64 s[96:97], s[96:97], vcc\n"
    "v_cmp_nlt_f32 vcc, v216, v252\n"
    "v_rcp_f32 v238, v216\n"
    "s_or_b64 s[96:97], s[96:97], vcc\n"
    "v_cmp_nlt_f32 vcc, v220, v252\n"
    "v_rcp_f32 v239, v220\n"
    "s_or_b64 s[96:97], s[96:97], vcc\n"
    "s_nop 1\n"
    "s_cmp_lg_u64 s[96:97], 0\n"
    "s_cselect_b32 s76, 1, 0\n"
    "s_lshl_b32 s77, s68, 2\n"
    "s_add_u32 s78, s69, 131072\n"
    "s_add_u32 s77, s77, s78\n"
    "v_mov_b32 v128, s77\n"
    "v_mov_b32 v129, s76\n"
    "v_mov_b32 v130, s78\n"
    "ds_write_b32 v128, v129\n"
    "s_waitcnt lgkmcnt(0)\n"
    "s_barrier\n"
    "ds_read_b128 v[132:135], v130\n"
    "s_waitcnt lgkmcnt(0)\n"
    "v_or_b32 v132, v132, v133\n"
    "v_or3_b32 v132, v132, v134, v135\n"
    "s_nop 0\n"
    "v_readfirstlane_b32 s75, v132\n"
    "s_cmp_eq_u32 s75, 0\n"
    "s_cbranch_scc1 END%=\n"
    "s_waitcnt vmcnt(0)\n"
    "s_barrier\n"
    "s_branch TOP%=\n"
    "QL0_%=:\n"
    "ds_read_b128 a[128:131], v255 offset:0\n"
    "ds_read_b128 a[132:135], v255 offset:1024\n"
    "ds_read_b128 a[136:139], v255 offset:2048\n"
    "ds_read_b128 a[140:143], v255 offset:3072\n"
    "ds_read_b128 a[144:147], v255 offset:4096\n"
    "ds_read_b128 a[148:151], v255 offset:5120\n"
    "ds_read_b128 a[152:155], v255 offset:6144\n"
    "ds_read_b128 a[156:159], v255 offset:7168\n"
    "ds_read_b128 a[160:163], v255 offset:8192\n"
    "ds_read_b128 a[164:167], v255 offset:9216\n"
    "ds_read_b128 a[168:171], v255 offset:10240\n"
    "ds_read_b128 a[172:175], v255 offset:11264\n"
    "ds_read_b128 a[176:179], v255 offset:12288\n"
    "ds_read_b128 a[180:183], v255 offset:13312\n"
    "ds_read_b128 a[184:187], v255 offset:14336\n"
    "ds_read_b128 a[188:191], v255 offset:15360\n"
    "s_branch QB0_%=\n"
    "PBF%=:\n"
    "s_cmp_lg_u64 s[96:97], 0\n"
    "s_cbranch_scc1 LPF%=\n"
    "s_mov_b32 s67, 1\n"
    "s_and_b32 s77, %9, 3\n"
    "s_lshr_b32 s76, s64, s77\n"
    "s_andn2_b32 s76, 1, s76\n"
    "v_mov_b32 v236, s76\n"
    "s_lshl_b32 s76, %14, 2\n"
    "s_add_u32 s76, s76, s77\n"
    "s_lshl_b32 s76, s76, 9\n"
    "buffer_store_dword v236, off, s[56:59], s76 sc0 sc1\n"
    "s_branch LPF%=\n"
    "SAFE%=:\n"
    "s_getpc_b64 s[90:91]\n"
    "PCFIX0%=:\n"
    "s_add_u32 s90, s90, FIX0%=-PCFIX0%=\n"
    "s_addc_u32 s91, s91, 0\n"
    "s_getpc_b64 s[92:93]\n"
    "PCFIX1%=:\n"
    "s_add_u32 s92, s92, FIX1%=-PCFIX1%=\n"
    "s_addc_u32 s93, s93, 0\n"
    "s_getpc_b64 s[94:95]\n"
    "PCRESC%=:\n"
    "s_add_u32 s94, s94, RESC%=-PCRESC%=\n"
    "s_addc_u32 s95, s95, 0\n"
    "v_mfma_f32_16x16x32_bf16 v[0:3], a[192:195], a[128:131], 0\n"
    "v_mfma_f32_16x16x32_bf16 v[8:11], a[192:195], a[144:147], 0\n"
    "v_mfma_f32_16x16x32_bf16 v[16:19], a[192:195], a[160:163], 0\n"
    "v_mfma_f32_16x16x32_bf16 v[24:27], a[192:195], a[176:179], 0\n"
    "v_mfma_f32_16x16x32_bf16 v[4:7], a[196:199], a[128:131], 0\n"
    "v_mfma_f32_16x16x32_bf16 v[12:15], a[196:199], a[144:147], 0\n"
    "v_mfma_f32_16x16x32_bf16 v[20:23], a[196:199], a[160:163], 0\n"
    "v_mfma_f32_16x16x32_bf16 v[28:31], a[196:199], a[176:179], 0\n"
    "v_mfma_f32_16x16x32_bf16 v[32:35], a[200:203], a[128:131], 0\n"
    "v_mfma_f32_16x16x32_bf16 v[40:43], a[200:203], a[144:147], 0\n"
    "v_mfma_f32_16x16x32_bf16 v[48:51], a[200:203], a[160:163], 0\n"
    "v_mfma_f32_16x16x32_bf16 v[56:59], a[200:203], a[176:179], 0\n"
    "v_mfma_f32_16x16x32_bf16 v[36:39], a[204:207], a[128:131], 0\n"
    "v_mfma_f32_16x16x32_bf16 v[44:47], a[204:207], a[144:147], 0\n"
    "v_mfma_f32_16x16x32_bf16 v[52:55], a[204:207], a[160:163], 0\n"
    "v_mfma_f32_16x16x32_bf16 v[60:63], a[204:207], a[176:179], 0\n"
    "v_mfma_f32_16x16x32_bf16 v[0:3], a[208:211], a[132:135], v[0:3]\n"
    "v_mfma_f32_16x16x32_bf16 v[8:11], a[208:211], a[148:151], v[8:11]\n"
    "v_mfma_f32_16x16x32_bf16 v[16:19], a[208:211], a[164:167], v[16:19]\n"
    "v_mfma_f32_16x16x32_bf16 v[24:27], a[208:211], a[180:183], v[24:27]\n"
    "v_mfma_f32_16x16x32_bf16 v[4:7], a[212:215], a[132:135], v[4:7]\n"
    "v_mfma_f32_16x16x32_bf16 v[12:15], a[212:215], a[148:151], v[12:15]\n"
    "v_mfma_f32_16x16x32_bf16 v[20:23], a[212:215], a[164:167], v[20:23]\n"
    "v_mfma_f32_16x16x32_bf16 v[28:31], a[212:215], a[180:183], v[28:31]\n"
    "v_mfma_f32_16x16x32_bf16 v[32:35], a[216:219], a[132:135], v[32:35]\n"
    "v_mfma_f32_16x16x32_bf16 v[40:43], a[216:219], a[148:151], v[40:43]\n"
    "v_mfma_f32_16x16x32_bf16 v[48:51], a[216:219], a[164:167], v[48:51]\n"
    "v_mfma_f32_16x16x32_bf16 v[56:59], a[216:219], a[180:183], v[56:59]\n"
    "v_mfma_f32_16x16x32_bf16 v[36:39], a[220:223], a[132:135], v[36:39]\n"
    "v_mfma_f32_16x16x32_bf16 v[44:47], a[220:223], a[148:151], v[44:47]\n"
    "v_mfma_f32_16x16x32_bf16 v[52:55], a[220:223], a[164:167], v[52:55]\n"
    "v_mfma_f32_16x16x32_bf16 v[60:63], a[220:223], a[180:183], v[60:63]\n"
    "v_mfma_f32_16x16x32_bf16 v[0:3], a[224:227], a[136:139], v[0:3]\n"
    "v_mfma_f32_16x16x32_bf16 v[8:11], a[224:227], a[152:155], v[8:11]\n"
    "v_mfma_f32_16x16x32_bf16 v[16:19], a[224:227], a[168:171], v[16:19]\n"
    "v_mfma_f32_16x16x32_bf16 v[24:27], a[224:227], a[184:187], v[24:27]\n"
    "v_mfma_f32_16x16x32_bf16 v[4:7], a[228:231], a[136:139], v[4:7]\n"
    "v_mfma_f32_16x16x32_bf16 v[12:15], a[228:231], a[152:155], v[12:15]\n"
    "v_mfma_f32_16x16x32_bf16 v[20:23], a[228:231], a[168:171], v[20:23]\n"
    "v_mfma_f32_16x16x32_bf16 v[28:31], a[228:231], a[184:187], v[28:31]\n"
    "v_mfma_f32_16x16x32_bf16 v[32:35], a[232:235], a[136:139], v[32:35]\n"
    "v_mfma_f32_16x16x32_bf16 v[40:43], a[232:235], a[152:155], v[40:43]\n"
    "v_mfma_f32_16x16x32_bf16 v[48:51], a[232:235], a[168:171], v[48:51]\n"
    "v_mfma_f32_16x16x32_bf16 v[56:59], a[232:235], a[184:187], v[56:59]\n"
    "v_mfma_f32_16x16x32_bf16 v[36:39], a[236:239], a[136:139], v[36:39]\n"
    "v_mfma_f32_16x16x32_bf16 v[44:47], a[236:239], a[152:155], v[44:47]\n"
    "v_mfma_f32_16x16x32_bf16 v[52:55], a[236:239], a[168:171], v[52:55]\n"
    "v_mfma_f32_16x16x32_bf16 v[60:63], a[236:239], a[184:187], v[60:63]\n"
    "v_mfma_f32_16x16x32_bf16 v[0:3], a[240:243], a[140:143], v[0:3]\n"
    "v_mfma_f32_16x16x32_bf16 v[8:11], a[240:243], a[156:159], v[8:11]\n"
    "v_mfma_f32_16x16x32_bf16 v[16:19], a[240:243], a[172:175], v[16:19]\n"
    "v_mfma_f32_16x16x32_bf16 v[24:27], a[240:243], a[188:191], v[24:27]\n"
    "v_mfma_f32_16x16x32_bf16 v[4:7], a[244:247], a[140:143], v[4:7]\n"
    "v_mfma_f32_16x16x32_bf16 v[12:15], a[244:247], a[156:159], v[12:15]\n"
    "v_mfma_f32_16x16x32_bf16 v[20:23], a[244:247], a[172:175], v[20:23]\n"
    "v_mfma_f32_16x16x32_bf16 v[28:31], a[244:247], a[188:191], v[28:31]\n"
    "v_mfma_f32_16x16x32_bf16 v[32:35], a[248:251], a[140:143], v[32:35]\n"
    "v_mfma_f32_16x16x32_bf16 v[40:43], a[248:251], a[156:159], v[40:43]\n"
    "v_mfma_f32_16x16x32_bf16 v[48:51], a[248:251], a[172:175], v[48:51]\n"
    "v_mfma_f32_16x16x32_bf16 v[56:59], a[248:251], a[188:191], v[56:59]\n"
    "v_mfma_f32_16x16x32_bf16 v[36:39], a[252:255], a[140:143], v[36:39]\n"
    "v_mfma_f32_16x16x32_bf16 v[44:47], a[252:255], a[156:159], v[44:47]\n"
    "v_mfma_f32_16x16x32_bf16 v[52:55], a[252:255], a[172:175], v[52:55]\n"
    "v_mfma_f32_16x16x32_bf16 v[60:63], a[252:255], a[188:191], v[60:63]\n"
    "s_waitcnt vmcnt(8) lgkmcnt(0)\n"
    "s_barrier\n"
    "s_nop 7\n"
    "s_add_u32 m0, s72, s99\n"
    "s_nop 0\n"
    "buffer_load_dwordx4 v224, s[36:39], s70 offen offset:0 nt lds\n"
    "buffer_load_dwordx4 v225, s[36:39], s70 offen offset:1024 nt lds\n"
    "v_max3_f32 v240, v0, v1, v2\n"
    "v_max3_f32 v241, v8, v9, v10\n"
    "v_max3_f32 v242, v16, v17, v18\n"
    "v_max3_f32 v243, v24, v25, v26\n"
    "v_max3_f32 v240, v240, v3, v4\n"
    "v_max3_f32 v241, v241, v11, v12\n"
    "buffer_load_dwordx4 v226, s[36:39], s70 offen offset:2048 nt lds\n"
    "v_max3_f32 v242, v242, v19, v20\n"
    "v_max3_f32 v243, v243, v27, v28\n"
    "v_max3_f32 v240, v240, v5, v6\n"
    "v_max3_f32 v241, v241, v13, v14\n"
    "v_max3_f32 v242, v242, v21, v22\n"
    "v_max3_f32 v243, v243, v29, v30\n"
    "buffer_load_dwordx4 v227, s[36:39], s70 offen offset:3072 nt lds\n"
    "v_max3_f32 v240, v240, v7, v32\n"
    "v_max3_f32 v241, v241, v15, v40\n"
    "s_add_u32 m0, s73, s83\n"
    "s_add_u32 s70, s70, 16384\n"
    "v_max3_f32 v242, v242, v23, v48\n"
    "v_max3_f32 v243, v243, v31, v56\n"
    "buffer_load_dwordx4 v224, s[40:43], s71 offen offset:0 nt lds\n"
    "v_max3_f32 v240, v240, v33, v34\n"
    "v_max3_f32 v241, v241, v41, v42\n"
    "v_add_u32 v250, s99, v230\n"
    "v_add_u32 v251, s99, v231\n"
    "v_max3_f32 v242, v242, v49, v50\n"
    "v_max3_f32 v243, v243, v57, v58\n"
    "buffer_load_dwordx4 v225, s[40:43], s71 offen offset:1024 nt lds\n"
    "v_max3_f32 v240, v240, v35, v36\n"
    "v_max3_f32 v241, v241, v43, v44\n"
    "s_mov_b32 s83, s99\n"
    "s_add_u32 s99, s99, 16384\n"
    "v_max3_f32 v242, v242, v51, v52\n"
    "v_max3_f32 v243, v243, v59, v60\n"
    "buffer_load_dwordx4 v226, s[40:43], s71 offen offset:2048 nt lds\n"
    "v_max3_f32 v240, v240, v37, v38\n"
    "v_max3_f32 v241, v241, v45, v46\n"
    "s_cmp_eq_u32 s99, 49152\n"
    "s_cselect_b32 s99, 0, s99\n"
    "v_max3_f32 v242, v242, v53, v54\n"
    "v_max3_f32 v243, v243, v61, v62\n"
    "buffer_load_dwordx4 v227, s[40:43], s71 offen offset:3072 nt lds\n"
    "v_max3_f32 v240, v240, v39, v39\n"
    "v_max3_f32 v241, v241, v47, v47\n"
    "s_add_u32 s71, s71, 16384\n"
    "v_add_u32 v253, s99, v228\n"
    "v_max3_f32 v242, v242, v55, v55\n"
    "v_max3_f32 v243, v243, v63, v63\n"
    "ds_read_b128 a[192:195], v253 offset:0\n"
    "ds_read_b128 a[196:199], v253 offset:4096\n"
    "v_mov_b32 v236, v240\n"
    "v_mov_b32 v237, v241\n"
    "v_mov_b32 v238, v242\n"
    "v_mov_b32 v239, v243\n"
    "s_nop 0\n"
    "v_permlane32_swap_b32 v240, v236\n"
    "v_permlane32_swap_b32 v241, v237\n"
    "v_permlane32_swap_b32 v242, v238\n"
    "v_permlane32_swap_b32 v243, v239\n"
    "s_nop 0\n"
    "v_max_f32 v240, v240, v236\n"
    "v_max_f32 v241, v241, v237\n"
    "v_max_f32 v242, v242, v238\n"
    "v_max_f32 v243, v243, v239\n"
    "v_mov_b32 v236, v240\n"
    "v_mov_b32 v237, v241\n"
    "v_mov_b32 v238, v242\n"
    "v_mov_b32 v239, v243\n"
    "ds_read_b128 a[200:203], v253 offset:8192\n"
    "ds_read_b128 a[204:207], v253 offset:12288\n"
    "s_nop 0\n"
    "v_permlane16_swap_b32 v240, v236\n"
    "v_permlane16_swap_b32 v241, v237\n"
    "v_permlane16_swap_b32 v242, v238\n"
    "v_permlane16_swap_b32 v243, v239\n"
    "s_nop 0\n"
    "v_max_f32 v240, v240, v236\n"
    "v_max_f32 v241, v241, v237\n"
    "v_max_f32 v242, v242, v238\n"
    "v_max_f32 v243, v243, v239\n"
    "v_sub_f32 v0, v0, v240\n"
    "v_sub_f32 v1, v1, v240\n"
    "v_sub_f32 v2, v2, v240\n"
    "v_sub_f32 v3, v3, v240\n"
    "v_sub_f32 v4, v4, v240\n"
    "v_sub_f32 v5, v5, v240\n"
    "v_sub_f32 v6, v6, v240\n"
    "v_sub_f32 v7, v7, v240\n"
    "ds_read_b128 a[208:211], v253 offset:512\n"
    "ds_read_b128 a[212:215], v253 offset:4608\n"
    "v_sub_f32 v8, v8, v241\n"
    "v_sub_f32 v9, v9, v241\n"
    "v_sub_f32 v10, v10, v241\n"
    "v_sub_f32 v11, v11, v241\n"
    "v_sub_f32 v12, v12, v241\n"
    "v_sub_f32 v13, v13, v241\n"
    "v_sub_f32 v14, v14, v241\n"
    "v_sub_f32 v15, v15, v241\n"
    "v_sub_f32 v16, v16, v242\n"
    "v_sub_f32 v17, v17, v242\n"
    "v_sub_f32 v18, v18, v242\n"
    "v_sub_f32 v19, v19, v242\n"
    "v_sub_f32 v20, v20, v242\n"
    "v_sub_f32 v21, v21, v242\n"
    "v_sub_f32 v22, v22, v242\n"
    "v_sub_f32 v23, v23, v242\n"
    "v_sub_f32 v24, v24, v243\n"
    "v_sub_f32 v25, v25, v243\n"
    "ds_read_b128 a[216:219], v253 offset:8704\n"
    "ds_read_b128 a[220:223], v253 offset:12800\n"
    "v_sub_f32 v26, v26, v243\n"
    "v_sub_f32 v27, v27, v243\n"
    "v_sub_f32 v28, v28, v243\n"
    "v_sub_f32 v29, v29, v243\n"
    "v_sub_f32 v30, v30, v243\n"
    "v_sub_f32 v31, v31, v243\n"
    "v_sub_f32 v32, v32, v240\n"
    "v_sub_f32 v33, v33, v240\n"
    "v_sub_f32 v34, v34, v240\n"
    "v_sub_f32 v35, v35, v240\n"
    "v_sub_f32 v36, v36, v240\n"
    "v_sub_f32 v37, v37, v240\n"
    "v_sub_f32 v38, v38, v240\n"
    "v_sub_f32 v39, v39, v240\n"
    "v_sub_f32 v40, v40, v241\n"
    "v_sub_f32 v41, v41, v241\n"
    "v_sub_f32 v42, v42, v241\n"
    "v_sub_f32 v43, v43, v241\n"
    "ds_read_b128 a[224:227], v253 offset:1024\n"
    "ds_read_b128 a[228:231], v253 offset:5120\n"
    "v_sub_f32 v44, v44, v241\n"
    "v_sub_f32 v45, v45, v241\n"
    "v_sub_f32 v46, v46, v241\n"
    "v_sub_f32 v47, v47, v241\n"
    "v_sub_f32 v48, v48, v242\n"
    "v_sub_f32 v49, v49, v242\n"
    "v_sub_f32 v50, v50, v242\n"
    "v_sub_f32 v51, v51, v242\n"
    "v_sub_f32 v52, v52, v242\n"
    "v_sub_f32 v53, v53, v242\n"
    "v_sub_f32 v54, v54, v242\n"
    "v_sub_f32 v55, v55, v242\n"
    "v_sub_f32 v56, v56, v243\n"
    "v_sub_f32 v57, v57, v243\n"
    "v_sub_f32 v58, v58, v243\n"
    "v_sub_f32 v59, v59, v243\n"
    "v_sub_f32 v60, v60, v243\n"
    "v_sub_f32 v61, v61, v243\n"
    "v_sub_f32 v62, v62, v243\n"
    "v_sub_f32 v63, v63, v243\n"
    "v_sub_f32 v192, 0, v240\n"
    "v_sub_f32 v193, 0, v240\n"
    "v_sub_f32 v194, 0, v240\n"
    "v_sub_f32 v195, 0, v240\n"
    "v_sub_f32 v196, 0, v241\n"
    "v_sub_f32 v197, 0, v241\n"
    "v_sub_f32 v198, 0, v241\n"
    "v_sub_f32 v199, 0, v241\n"
    "v_sub_f32 v200, 0, v242\n"
    "v_sub_f32 v201, 0, v242\n"
    "v_sub_f32 v202, 0, v242\n"
    "v_sub_f32 v203, 0, v242\n"
    "v_sub_f32 v204, 0, v243\n"
    "v_sub_f32 v205, 0, v243\n"
    "v_sub_f32 v206, 0, v243\n"
    "v_sub_f32 v207, 0, v243\n"
    "v_exp_f32 v0, v0\n"
    "v_exp_f32 v1, v1\n"
    "v_exp_f32 v2, v2\n"
    "v_exp_f32 v3, v3\n"
    "v_exp_f32 v4, v4\n"
    "v_cvt_pk_bf16_f32 v0, v0, v1\n"
    "v_cvt_pk_bf16_f32 v1, v2, v3\n"
    "v_exp_f32 v5, v5\n"
    "ds_read_b128 a[232:235], v253 offset:9216\n"
    "ds_read_b128 a[236:239], v253 offset:13312\n"
    "v_exp_f32 v6, v6\n"
    "v_exp_f32 v7, v7\n"
    "v_exp_f32 v8, v8\n"
    "v_cvt_pk_bf16_f32 v2, v4, v5\n"
    "v_cvt_pk_bf16_f32 v3, v6, v7\n"
    "v_exp_f32 v9, v9\n"
    "v_exp_f32 v10, v10\n"
    "v_exp_f32 v11, v11\n"
    "ds_read_b128 a[240:243], v253 offset:1536\n"
    "ds_read_b128 a[244:247], v253 offset:5632\n"
    "v_exp_f32 v12, v12\n"
    "v_cvt_pk_bf16_f32 v8, v8, v9\n"
    "v_cvt_pk_bf16_f32 v9, v10, v11\n"
    "v_exp_f32 v13, v13\n"
    "v_exp_f32 v14, v14\n"
    "v_exp_f32 v15, v15\n"
    "v_exp_f32 v16, v16\n"
    "v_cvt_pk_bf16_f32 v10, v12, v13\n"
    "v_cvt_pk_bf16_f32 v11, v14, v15\n"
    "v_exp_f32 v17, v17\n"
    "ds_read_b128 a[248:251], v253 offset:9728\n"
    "ds_read_b128 a[252:255], v253 offset:13824\n"
    "v_exp_f32 v18, v18\n"
    "v_exp_f32 v19, v19\n"
    "v_exp_f32 v20, v20\n"
    "v_cvt_pk_bf16_f32 v16, v16, v17\n"
    "v_cvt_pk_bf16_f32 v17, v18, v19\n"
    "v_exp_f32 v21, v21\n"
    "v_exp_f32 v22, v22\n"
    "v_exp_f32 v23, v23\n"
    "v_exp_f32 v24, v24\n"
    "v_cvt_pk_bf16_f32 v18, v20, v21\n"
    "v_cvt_pk_bf16_f32 v19, v22, v23\n"
    "v_exp_f32 v25, v25\n"
    "v_exp_f32 v26, v26\n"
    "v_exp_f32 v27, v27\n"
    "v_exp_f32 v28, v28\n"
    "v_exp_f32 v29, v29\n"
    "v_cvt_pk_bf16_f32 v24, v24, v25\n"
    "v_cvt_pk_bf16_f32 v25, v26, v27\n"
    "v_exp_f32 v30, v30\n"
    "v_exp_f32 v31, v31\n"
    "v_exp_f32 v32, v32\n"
    "v_exp_f32 v33, v33\n"
    "v_exp_f32 v34, v34\n"
    "v_cvt_pk_bf16_f32 v26, v28, v29\n"
    "v_cvt_pk_bf16_f32 v27, v30, v31\n"
    "v_exp_f32 v35, v35\n"
    "v_exp_f32 v36, v36\n"
    "v_exp_f32 v37, v37\n"
    "v_exp_f32 v38, v38\n"
    "v_exp_f32 v39, v39\n"
    "s_waitcnt lgkmcnt(0)\n"
    "s_mov_b32 s54, 0\n"
    "s_movk_i32 s74, 0x107\n"
    "s_branch P1S%=\n"
    "PWS%=:\n"
    "v_mfma_f32_16x16x32_bf16 v[0:3], a[192:195], a[128:131], 0\n"
    "v_exp_f32 v104, v104\n"
    "v_mfma_f32_16x16x32_bf16 v[8:11], a[192:195], a[144:147], 0\n"
    "ds_read_b64_tr_b16 v[128:129], v250 offset:0\n"
    "ds_read_b64_tr_b16 v[130:131], v250 offset:4096\n"
    "v_mfma_f32_16x16x32_bf16 v[16:19], a[192:195], a[160:163], 0\n"
    "v_cvt_pk_bf16_f32 v96, v96, v97\n"
    "v_cvt_pk_bf16_f32 v97, v98, v99\n"
    "v_mfma_f32_16x16x32_bf16 v[24:27], a[192:195], a[176:179], 0\n"
    "v_exp_f32 v105, v105\n"
    "v_mfma_f32_16x16x32_bf16 v[4:7], a[196:199], a[128:131], 0\n"
    "ds_read_b64_tr_b16 v[132:133], v251 offset:0\n"
    "ds_read_b64_tr_b16 v[134:135], v251 offset:4096\n"
    "v_mfma_f32_16x16x32_bf16 v[12:15], a[196:199], a[144:147], 0\n"
    "v_mfma_f32_16x16x32_bf16 v[20:23], a[196:199], a[160:163], 0\n"
    "v_exp_f32 v106, v106\n"
    "v_mfma_f32_16x16x32_bf16 v[28:31], a[196:199], a[176:179], 0\n"
    "ds_read_b64_tr_b16 v[136:137], v250 offset:512\n"
    "ds_read_b64_tr_b16 v[138:139], v250 offset:4608\n"
    "v_mfma_f32_16x16x32_bf16 v[32:35], a[200:203], a[128:131], 0\n"
    "v_cvt_pk_bf16_f32 v98, v100, v101\n"
    "v_cvt_pk_bf16_f32 v99, v102, v103\n"
    "v_mfma_f32_16x16x32_bf16 v[40:43], a[200:203], a[144:147], 0\n"
    "v_mfma_f32_16x16x32_bf16 v[48:51], a[200:203], a[160:163], 0\n"
    "ds_read_b64_tr_b16 v[140:141], v251 offset:512\n"
    "ds_read_b64_tr_b16 v[142:143], v251 offset:4608\n"
    "v_mfma_f32_16x16x32_bf16 v[56:59], a[200:203], a[176:179], 0\n"
    "v_exp_f32 v107, v107\n"
    "v_mfma_f32_16x16x32_bf16 v[36:39], a[204:207], a[128:131], 0\n"
    "v_exp_f32 v108, v108\n"
    "v_mfma_f32_16x16x32_bf16 v[44:47], a[204:207], a[144:147], 0\n"
    "ds_read_b64_tr_b16 v[144:145], v250 offset:1024\n"
    "ds_read_b64_tr_b16 v[146:147], v250 offset:5120\n"
    "v_mfma_f32_16x16x32_bf16 v[52:55], a[204:207], a[160:163], 0\n"
    "v_exp_f32 v109, v109\n"
    "v_mfma_f32_16x16x32_bf16 v[60:63], a[204:207], a[176:179], 0\n"
    "v_mfma_f32_16x16x32_bf16 v[0:3], a[208:211], a[132:135], v[0:3]\n"
    "ds_read_b64_tr_b16 v[148:149], v251 offset:1024\n"
    "ds_read_b64_tr_b16 v[150:151], v251 offset:5120\n"
    "v_mfma_f32_16x16x32_bf16 v[8:11], a[208:211], a[148:151], v[8:11]\n"
    "v_cvt_pk_bf16_f32 v104, v104, v105\n"
    "v_cvt_pk_bf16_f32 v105, v106, v107\n"
    "v_mfma_f32_16x16x32_bf16 v[16:19], a[208:211], a[164:167], v[16:19]\n"
    "v_exp_f32 v110, v110\n"
    "v_mfma_f32_16x16x32_bf16 v[24:27], a[208:211], a[180:183], v[24:27]\n"
    "ds_read_b64_tr_b16 v[152:153], v250 offset:1536\n"
    "ds_read_b64_tr_b16 v[154:155], v250 offset:5632\n"
    "v_mfma_f32_16x16x32_bf16 v[4:7], a[212:215], a[132:135], v[4:7]\n"
    "v_mfma_f32_16x16x32_bf16 v[12:15], a[212:215], a[148:151], v[12:15]\n"
    "v_exp_f32 v111, v111\n"
    "v_mfma_f32_16x16x32_bf16 v[20:23], a[212:215], a[164:167], v[20:23]\n"
    "ds_read_b64_tr_b16 v[156:157], v251 offset:1536\n"
    "ds_read_b64_tr_b16 v[158:159], v251 offset:5632\n"
    "v_mfma_f32_16x16x32_bf16 v[28:31], a[212:215], a[180:183], v[28:31]\n"
    "v_exp_f32 v112, v112\n"
    "v_mfma_f32_16x16x32_bf16 v[32:35], a[216:219], a[132:135], v[32:35]\n"
    "v_cvt_pk_bf16_f32 v106, v108, v109\n"
    "v_cvt_pk_bf16_f32 v107, v110, v111\n"
    "v_mfma_f32_16x16x32_bf16 v[40:43], a[216:219], a[148:151], v[40:43]\n"
    "v_mfma_f32_16x16x32_bf16 v[48:51], a[216:219], a[164:167], v[48:51]\n"
    "ds_read_b64_tr_b16 v[160:161], v250 offset:8192\n"
    "ds_read_b64_tr_b16 v[162:163], v250 offset:12288\n"
    "v_mfma_f32_16x16x32_bf16 v[56:59], a[216:219], a[180:183], v[56:59]\n"
    "v_exp_f32 v113, v113\n"
    "v_mfma_f32_16x16x32_bf16 v[36:39], a[220:223], a[132:135], v[36:39]\n"
    "v_exp_f32 v114, v114\n"
    "v_mfma_f32_16x16x32_bf16 v[44:47], a[220:223], a[148:151], v[44:47]\n"
    "ds_read_b64_tr_b16 v[164:165], v251 offset:8192\n"
    "ds_read_b64_tr_b16 v[166:167], v251 offset:12288\n"
    "v_mfma_f32_16x16x32_bf16 v[52:55], a[220:223], a[164:167], v[52:55]\n"
    "v_mfma_f32_16x16x32_bf16 v[60:63], a[220:223], a[180:183], v[60:63]\n"
    "v_exp_f32 v115, v115\n"
    "v_mfma_f32_16x16x32_bf16 v[0:3], a[224:227], a[136:139], v[0:3]\n"
    "ds_read_b64_tr_b16 v[168:169], v250 offset:8704\n"
    "ds_read_b64_tr_b16 v[170:171], v250 offset:12800\n"
    "v_mfma_f32_16x16x32_bf16 v[8:11], a[224:227], a[152:155], v[8:11]\n"
    "v_exp_f32 v116, v116\n"
    "v_mfma_f32_16x16x32_bf16 v[16:19], a[224:227], a[168:171], v[16:19]\n"
    "v_mfma_f32_16x16x32_bf16 v[24:27], a[224:227], a[184:187], v[24:27]\n"
    "ds_read_b64_tr_b16 v[172:173], v251 offset:8704\n"
    "ds_read_b64_tr_b16 v[174:175], v251 offset:12800\n"
    "v_mfma_f32_16x16x32_bf16 v[4:7], a[228:231], a[136:139], v[4:7]\n"
    "v_cvt_pk_bf16_f32 v112, v112, v113\n"
    "v_cvt_pk_bf16_f32 v113, v114, v115\n"
    "v_mfma_f32_16x16x32_bf16 v[12:15], a[228:231], a[152:155], v[12:15]\n"
    "v_exp_f32 v117, v117\n"
    "v_mfma_f32_16x16x32_bf16 v[20:23], a[228:231], a[168:171], v[20:23]\n"
    "ds_read_b64_tr_b16 v[176:177], v250 offset:9216\n"
    "ds_read_b64_tr_b16 v[178:179], v250 offset:13312\n"
    "v_mfma_f32_16x16x32_bf16 v[28:31], a[228:231], a[184:187], v[28:31]\n"
    "v_exp_f32 v118, v118\n"
    "v_mfma_f32_16x16x32_bf16 v[32:35], a[232:235], a[136:139], v[32:35]\n"
    "v_mfma_f32_16x16x32_bf16 v[40:43], a[232:235], a[152:155], v[40:43]\n"
    "ds_read_b64_tr_b16 v[180:181], v251 offset:9216\n"
    "ds_read_b64_tr_b16 v[182:183], v251 offset:13312\n"
    "v_mfma_f32_16x16x32_bf16 v[48:51], a[232:235], a[168:171], v[48:51]\n"
    "v_exp_f32 v119, v119\n"
    "v_mfma_f32_16x16x32_bf16 v[56:59], a[232:235], a[184:187], v[56:59]\n"
    "v_exp_f32 v120, v120\n"
    "v_mfma_f32_16x16x32_bf16 v[36:39], a[236:239], a[136:139], v[36:39]\n"
    "ds_read_b64_tr_b16 v[184:185], v250 offset:9728\n"
    "ds_read_b64_tr_b16 v[186:187], v250 offset:13824\n"
    "v_mfma_f32_16x16x32_bf16 v[44:47], a[236:239], a[152:155], v[44:47]\n"
    "v_mfma_f32_16x16x32_bf16 v[52:55], a[236:239], a[168:171], v[52:55]\n"
    "v_cvt_pk_bf16_f32 v114, v116, v117\n"
    "v_cvt_pk_bf16_f32 v115, v118, v119\n"
    "v_mfma_f32_16x16x32_bf16 v[60:63], a[236:239], a[184:187], v[60:63]\n"
    "ds_read_b64_tr_b16 v[188:189], v251 offset:9728\n"
    "ds_read_b64_tr_b16 v[190:191], v251 offset:13824\n"
    "v_mfma_f32_16x16x32_bf16 v[0:3], a[240:243], a[140:143], v[0:3]\n"
    "v_exp_f32 v121, v121\n"
    "v_mfma_f32_16x16x32_bf16 v[8:11], a[240:243], a[156:159], v[8:11]\n"
    "v_exp_f32 v122, v122\n"
    "v_mfma_f32_16x16x32_bf16 v[16:19], a[240:243], a[172:175], v[16:19]\n"
    "v_mfma_f32_16x16x32_bf16 v[24:27], a[240:243], a[188:191], v[24:27]\n"
    "v_exp_f32 v123, v123\n"
    "v_mfma_f32_16x16x32_bf16 v[4:7], a[244:247], a[140:143], v[4:7]\n"
    "v_exp_f32 v124, v124\n"
    "v_mfma_f32_16x16x32_bf16 v[12:15], a[244:247], a[156:159], v[12:15]\n"
    "v_mfma_f32_16x16x32_bf16 v[20:23], a[244:247], a[172:175], v[20:23]\n"
    "v_cvt_pk_bf16_f32 v120, v120, v121\n"
    "v_cvt_pk_bf16_f32 v121, v122, v123\n"
    "v_mfma_f32_16x16x32_bf16 v[28:31], a[244:247], a[188:191], v[28:31]\n"
    "v_exp_f32 v125, v125\n"
    "v_mfma_f32_16x16x32_bf16 v[32:35], a[248:251], a[140:143], v[32:35]\n"
    "v_mfma_f32_16x16x32_bf16 v[40:43], a[248:251], a[156:159], v[40:43]\n"
    "v_exp_f32 v126, v126\n"
    "v_mfma_f32_16x16x32_bf16 v[48:51], a[248:251], a[172:175], v[48:51]\n"
    "v_exp_f32 v127, v127\n"
    "v_mfma_f32_16x16x32_bf16 v[56:59], a[248:251], a[188:191], v[56:59]\n"
    "s_nop 0\n"
    "v_mfma_f32_16x16x32_bf16 v[36:39], a[252:255], a[140:143], v[36:39]\n"
    "v_cvt_pk_bf16_f32 v122, v124, v125\n"
    "v_cvt_pk_bf16_f32 v123, v126, v127\n"
    "v_mfma_f32_16x16x32_bf16 v[44:47], a[252:255], a[156:159], v[44:47]\n"
    "v_mfma_f32_16x16x32_bf16 v[52:55], a[252:255], a[172:175], v[52:55]\n"
    "v_mfma_f32_16x16x32_bf16 v[60:63], a[252:255], a[188:191], v[60:63]\n"
    "s_waitcnt vmcnt(8) lgkmcnt(0)\n"
    "s_barrier\n"
    "v_mfma_f32_16x16x32_bf16 a[0:3], v[128:131], v[64:67], a[0:3]\n"
    "v_mfma_f32_16x16x32_bf16 a[32:35], v[128:131], v[72:75], a[32:35]\n"
    "s_add_u32 m0, s72, s99\n"
    "v_mfma_f32_16x16x32_bf16 a[64:67], v[128:131], v[80:83], a[64:67]\n"
    "buffer_load_dwordx4 v224, s[36:39], s70 offen offset:0 nt lds\n"
    "v_mfma_f32_16x16x32_bf16 a[96:99], v[128:131], v[88:91], a[96:99]\n"
    "v_mfma_f32_16x16x32_bf16 a[4:7], v[132:135], v[64:67], a[4:7]\n"
    "buffer_load_dwordx4 v225, s[36:39], s70 offen offset:1024 nt lds\n"
    "v_max3_f32 v240, v0, v1, v2\n"
    "v_max3_f32 v241, v8, v9, v10\n"
    "v_mfma_f32_16x16x32_bf16 a[36:39], v[132:135], v[72:75], a[36:39]\n"
    "v_max3_f32 v242, v16, v17, v18\n"
    "v_max3_f32 v243, v24, v25, v26\n"
    "v_max3_f32 v240, v240, v3, v4\n"
    "v_max3_f32 v241, v241, v11, v12\n"
    "v_mfma_f32_16x16x32_bf16 a[68:71], v[132:135], v[80:83], a[68:71]\n"
    "buffer_load_dwordx4 v226, s[36:39], s70 offen offset:2048 nt lds\n"
    "v_max3_f32 v242, v242, v19, v20\n"
    "v_max3_f32 v243, v243, v27, v28\n"
    "v_mfma_f32_16x16x32_bf16 a[100:103], v[132:135], v[88:91], a[100:103]\n"
    "v_max3_f32 v240, v240, v5, v6\n"
    "v_max3_f32 v241, v241, v13, v14\n"
    "v_max3_f32 v242, v242, v21, v22\n"
    "v_max3_f32 v243, v243, v29, v30\n"
    "v_mfma_f32_16x16x32_bf16 a[8:11], v[136:139], v[64:67], a[8:11]\n"
    "buffer_load_dwordx4 v227, s[36:39], s70 offen offset:3072 nt lds\n"
    "v_max3_f32 v240, v240, v7, v32\n"
    "v_max3_f32 v241, v241, v15, v40\n"
    "v_mfma_f32_16x16x32_bf16 a[40:43], v[136:139], v[72:75], a[40:43]\n"
    "s_add_u32 m0, s73, s83\n"
    "s_add_u32 s70, s70, 16384\n"
    "v_max3_f32 v242, v242, v23, v48\n"
    "v_max3_f32 v243, v243, v31, v56\n"
    "v_mfma_f32_16x16x32_bf16 a[72:75], v[136:139], v[80:83], a[72:75]\n"
    "buffer_load_dwordx4 v224, s[40:43], s71 offen offset:0 nt lds\n"
    "v_max3_f32 v240, v240, v33, v34\n"
    "v_max3_f32 v241, v241, v41, v42\n"
    "v_mfma_f32_16x16x32_bf16 a[104:107], v[136:139], v[88:91], a[104:107]\n"
    "v_add_u32 v250, s99, v230\n"
    "v_add_u32 v251, s99, v231\n"
    "v_max3_f32 v242, v242, v49, v50\n"
    "v_max3_f32 v243, v243, v57, v58\n"
    "v_mfma_f32_16x16x32_bf16 a[12:15], v[140:143], v[64:67], a[12:15]\n"
    "buffer_load_dwordx4 v225, s[40:43], s71 offen offset:1024 nt lds\n"
    "v_max3_f32 v240, v240, v35, v36\n"
    "v_max3_f32 v241, v241, v43, v44\n"
    "v_mfma_f32_16x16x32_bf16 a[44:47], v[140:143], v[72:75], a[44:47]\n"
    "s_mov_b32 s83, s99\n"
    "s_add_u32 s99, s99, 16384\n"
    "v_max3_f32 v242, v242, v51, v52\n"
    "v_max3_f32 v243, v243, v59, v60\n"
    "v_mfma_f32_16x16x32_bf16 a[76:79], v[140:143], v[80:83], a[76:79]\n"
    "buffer_load_dwordx4 v226, s[40:43], s71 offen offset:2048 nt lds\n"
    "v_max3_f32 v240, v240, v37, v38\n"
    "v_max3_f32 v241, v241, v45, v46\n"
    "v_mfma_f32_16x16x32_bf16 a[108:111], v[140:143], v[88:91], a[108:111]\n"
    "s_cmp_eq_u32 s99, 49152\n"
    "s_cselect_b32 s99, 0, s99\n"
    "v_max3_f32 v242, v242, v53, v54\n"
    "v_max3_f32 v243, v243, v61, v62\n"
    "v_mfma_f32_16x16x32_bf16 a[16:19], v[144:147], v[64:67], a[16:19]\n"
    "buffer_load_dwordx4 v227, s[40:43], s71 offen offset:3072 nt lds\n"
    "v_max3_f32 v240, v240, v39, v39\n"
    "v_max3_f32 v241, v241, v47, v47\n"
    "v_mfma_f32_16x16x32_bf16 a[48:51], v[144:147], v[72:75], a[48:51]\n"
    "s_add_u32 s71, s71, 16384\n"
    "v_add_u32 v253, s99, v228\n"
    "v_max3_f32 v242, v242, v55, v55\n"
    "v_max3_f32 v243, v243, v63, v63\n"
    "v_mfma_f32_16x16x32_bf16 a[80:83], v[144:147], v[80:83], a[80:83]\n"
    "ds_read_b128 a[192:195], v253 offset:0\n"
    "ds_read_b128 a[196:199], v253 offset:4096\n"
    "v_mov_b32 v236, v240\n"
    "v_mov_b32 v237, v241\n"
    "v_mfma_f32_16x16x32_bf16 a[112:115], v[144:147], v[88:91], a[112:115]\n"
    "v_mov_b32 v238, v242\n"
    "v_mov_b32 v239, v243\n"
    "s_nop 0\n"
    "v_permlane32_swap_b32 v240, v236\n"
    "v_mfma_f32_16x16x32_bf16 a[20:23], v[148:151], v[64:67], a[20:23]\n"
    "v_permlane32_swap_b32 v241, v237\n"
    "v_permlane32_swap_b32 v242, v238\n"
    "v_permlane32_swap_b32 v243, v239\n"
    "s_nop 0\n"
    "v_mfma_f32_16x16x32_bf16 a[52:55], v[148:151], v[72:75], a[52:55]\n"
    "v_max_f32 v240, v240, v236\n"
    "v_max_f32 v241, v241, v237\n"
    "v_max_f32 v242, v242, v238\n"
    "v_max_f32 v243, v243, v239\n"
    "v_mfma_f32_16x16x32_bf16 a[84:87], v[148:151], v[80:83], a[84:87]\n"
    "v_mov_b32 v236, v240\n"
    "v_mov_b32 v237, v241\n"
    "v_mov_b32 v238, v242\n"
    "v_mov_b32 v239, v243\n"
    "v_mfma_f32_16x16x32_bf16 a[116:119], v[148:151], v[88:91], a[116:119]\n"
    "ds_read_b128 a[200:203], v253 offset:8192\n"
    "ds_read_b128 a[204:207], v253 offset:12288\n"
    "s_nop 0\n"
    "v_permlane16_swap_b32 v240, v236\n"
    "v_mfma_f32_16x16x32_bf16 a[24:27], v[152:155], v[64:67], a[24:27]\n"
    "v_permlane16_swap_b32 v241, v237\n"
    "v_permlane16_swap_b32 v242, v238\n"
    "v_permlane16_swap_b32 v243, v239\n"
    "s_nop 0\n"
    "v_mfma_f32_16x16x32_bf16 a[56:59], v[152:155], v[72:75], a[56:59]\n"
    "v_max_f32 v240, v240, v236\n"
    "v_max_f32 v241, v241, v237\n"
    "v_max_f32 v242, v242, v238\n"
    "v_max_f32 v243, v243, v239\n"
    "v_mfma_f32_16x16x32_bf16 a[88:91], v[152:155], v[80:83], a[88:91]\n"
    "v_sub_f32 v0, v0, v240\n"
    "v_sub_f32 v1, v1, v240\n"
    "v_sub_f32 v2, v2, v240\n"
    "v_sub_f32 v3, v3, v240\n"
    "v_mfma_f32_16x16x32_bf16 a[120:123], v[152:155], v[88:91], a[120:123]\n"
    "v_sub_f32 v4, v4, v240\n"
    "v_sub_f32 v5, v5, v240\n"
    "v_sub_f32 v6, v6, v240\n"
    "v_sub_f32 v7, v7, v240\n"
    "v_mfma_f32_16x16x32_bf16 a[28:31], v[156:159], v[64:67], a[28:31]\n"
    "ds_read_b128 a[208:211], v253 offset:512\n"
    "ds_read_b128 a[212:215], v253 offset:4608\n"
    "v_sub_f32 v8, v8, v241\n"
    "v_sub_f32 v9, v9, v241\n"
    "v_mfma_f32_16x16x32_bf16 a[60:63], v[156:159], v[72:75], a[60:63]\n"
    "v_sub_f32 v10, v10, v241\n"
    "v_sub_f32 v11, v11, v241\n"
    "v_sub_f32 v12, v12, v241\n"
    "v_sub_f32 v13, v13, v241\n"
    "v_mfma_f32_16x16x32_bf16 a[92:95], v[156:159], v[80:83], a[92:95]\n"
    "v_sub_f32 v14, v14, v241\n"
    "v_sub_f32 v15, v15, v241\n"
    "v_sub_f32 v16, v16, v242\n"
    "v_sub_f32 v17, v17, v242\n"
    "v_mfma_f32_16x16x32_bf16 a[124:127], v[156:159], v[88:91], a[124:127]\n"
    "v_sub_f32 v18, v18, v242\n"
    "v_sub_f32 v19, v19, v242\n"
    "v_sub_f32 v20, v20, v242\n"
    "v_sub_f32 v21, v21, v242\n"
    "v_mfma_f32_16x16x32_bf16 v[208:211], v[232:235], v[64:67], v[208:211]\n"
    "v_sub_f32 v22, v22, v242\n"
    "v_sub_f32 v23, v23, v242\n"
    "v_sub_f32 v24, v24, v243\n"
    "v_sub_f32 v25, v25, v243\n"
    "v_mfma_f32_16x16x32_bf16 v[212:215], v[232:235], v[72:75], v[212:215]\n"
    "ds_read_b128 a[216:219], v253 offset:8704\n"
    "ds_read_b128 a[220:223], v253 offset:12800\n"
    "v_sub_f32 v26, v26, v243\n"
    "v_sub_f32 v27, v27, v243\n"
    "v_mfma_f32_16x16x32_bf16 v[216:219], v[232:235], v[80:83], v[216:219]\n"
    "v_sub_f32 v28, v28, v243\n"
    "v_sub_f32 v29, v29, v243\n"
    "v_sub_f32 v30, v30, v243\n"
    "v_sub_f32 v31, v31, v243\n"
    "v_mfma_f32_16x16x32_bf16 v[220:223], v[232:235], v[88:91], v[220:223]\n"
    "v_sub_f32 v32, v32, v240\n"
    "v_sub_f32 v33, v33, v240\n"
    "v_sub_f32 v34, v34, v240\n"
    "v_sub_f32 v35, v35, v240\n"
    "v_mfma_f32_16x16x32_bf16 a[0:3], v[160:163], v[96:99], a[0:3]\n"
    "v_sub_f32 v36, v36, v240\n"
    "v_sub_f32 v37, v37, v240\n"
    "v_sub_f32 v38, v38, v240\n"
    "v_sub_f32 v39, v39, v240\n"
    "v_mfma_f32_16x16x32_bf16 a[32:35], v[160:163], v[104:107], a[32:35]\n"
    "v_sub_f32 v40, v40, v241\n"
    "v_sub_f32 v41, v41, v241\n"
    "v_sub_f32 v42, v42, v241\n"
    "v_sub_f32 v43, v43, v241\n"
    "v_mfma_f32_16x16x32_bf16 a[64:67], v[160:163], v[112:115], a[64:67]\n"
    "ds_read_b128 a[224:227], v253 offset:1024\n"
    "ds_read_b128 a[228:231], v253 offset:5120\n"
    "v_sub_f32 v44, v44, v241\n"
    "v_sub_f32 v45, v45, v241\n"
    "v_mfma_f32_16x16x32_bf16 a[96:99], v[160:163], v[120:123], a[96:99]\n"
    "v_sub_f32 v46, v46, v241\n"
    "v_sub_f32 v47, v47, v241\n"
    "v_sub_f32 v48, v48, v242\n"
    "v_sub_f32 v49, v49, v242\n"
    "v_sub_f32 v50, v50, v242\n"
    "v_sub_f32 v51, v51, v242\n"
    "v_sub_f32 v52, v52, v242\n"
    "v_sub_f32 v53, v53, v242\n"
    "v_sub_f32 v54, v54, v242\n"
    "v_sub_f32 v55, v55, v242\n"
    "v_sub_f32 v56, v56, v243\n"
    "v_sub_f32 v57, v57, v243\n"
    "v_sub_f32 v58, v58, v243\n"
    "v_sub_f32 v59, v59, v243\n"
    "v_sub_f32 v60, v60, v243\n"
    "v_sub_f32 v61, v61, v243\n"
    "v_sub_f32 v62, v62, v243\n"
    "v_sub_f32 v63, v63, v243\n"
    "v_sub_f32 v192, 0, v240\n"
    "v_sub_f32 v193, 0, v240\n"
    "v_sub_f32 v194, 0, v240\n"
    "v_sub_f32 v195, 0, v240\n"
    "v_sub_f32 v196, 0, v241\n"
    "v_sub_f32 v197, 0, v241\n"
    "v_sub_f32 v198, 0, v241\n"
    "v_sub_f32 v199, 0, v241\n"
    "v_sub_f32 v200, 0, v242\n"
    "v_sub_f32 v201, 0, v242\n"
    "v_sub_f32 v202, 0, v242\n"
    "v_sub_f32 v203, 0, v242\n"
    "v_sub_f32 v204, 0, v243\n"
    "v_sub_f32 v205, 0, v243\n"
    "v_sub_f32 v206, 0, v243\n"
    "v_sub_f32 v207, 0, v243\n"
    "v_mfma_f32_16x16x32_bf16 a[4:7], v[164:167], v[96:99], a[4:7]\n"
    "v_exp_f32 v0, v0\n"
    "v_exp_f32 v1, v1\n"
    "v_mfma_f32_16x16x32_bf16 a[36:39], v[164:167], v[104:107], a[36:39]\n"
    "v_exp_f32 v2, v2\n"
    "v_exp_f32 v3, v3\n"
    "v_mfma_f32_16x16x32_bf16 a[68:71], v[164:167], v[112:115], a[68:71]\n"
    "v_exp_f32 v4, v4\n"
    "v_mfma_f32_16x16x32_bf16 a[100:103], v[164:167], v[120:123], a[100:103]\n"
    "v_cvt_pk_bf16_f32 v0, v0, v1\n"
    "v_cvt_pk_bf16_f32 v1, v2, v3\n"
    "v_exp_f32 v5, v5\n"
    "v_mfma_f32_16x16x32_bf16 a[8:11], v[168:171], v[96:99], a[8:11]\n"
    "ds_read_b128 a[232:235], v253 offset:9216\n"
    "ds_read_b128 a[236:239], v253 offset:13312\n"
    "v_exp_f32 v6, v6\n"
    "v_mfma_f32_16x16x32_bf16 a[40:43], v[168:171], v[104:107], a[40:43]\n"
    "v_exp_f32 v7, v7\n"
    "v_mfma_f32_16x16x32_bf16 a[72:75], v[168:171], v[112:115], a[72:75]\n"
    "v_exp_f32 v8, v8\n"
    "v_cvt_pk_bf16_f32 v2, v4, v5\n"
    "v_cvt_pk_bf16_f32 v3, v6, v7\n"
    "v_mfma_f32_16x16x32_bf16 a[104:107], v[168:171], v[120:123], a[104:107]\n"
    "v_exp_f32 v9, v9\n"
    "v_exp_f32 v10, v10\n"
    "v_mfma_f32_16x16x32_bf16 a[12:15], v[172:175], v[96:99], a[12:15]\n"
    "v_exp_f32 v11, v11\n"
    "v_mfma_f32_16x16x32_bf16 a[44:47], v[172:175], v[104:107], a[44:47]\n"
    "ds_read_b128 a[240:243], v253 offset:1536\n"
    "ds_read_b128 a[244:247], v253 offset:5632\n"
    "v_exp_f32 v12, v12\n"
    "v_mfma_f32_16x16x32_bf16 a[76:79], v[172:175], v[112:115], a[76:79]\n"
    "v_cvt_pk_bf16_f32 v8, v8, v9\n"
    "v_cvt_pk_bf16_f32 v9, v10, v11\n"
    "v_exp_f32 v13, v13\n"
    "v_mfma_f32_16x16x32_bf16 a[108:111], v[172:175], v[120:123], a[108:111]\n"
    "v_exp_f32 v14, v14\n"
    "v_mfma_f32_16x16x32_bf16 a[16:19], v[176:179], v[96:99], a[16:19]\n"
    "v_exp_f32 v15, v15\n"
    "v_exp_f32 v16, v16\n"
    "v_mfma_f32_16x16x32_bf16 a[48:51], v[176:179], v[104:107], a[48:51]\n"
    "v_cvt_pk_bf16_f32 v10, v12, v13\n"
    "v_cvt_pk_bf16_f32 v11, v14, v15\n"
    "v_exp_f32 v17, v17\n"
    "v_mfma_f32_16x16x32_bf16 a[80:83], v[176:179], v[112:115], a[80:83]\n"
    "ds_read_b128 a[248:251], v253 offset:9728\n"
    "ds_read_b128 a[252:255], v253 offset:13824\n"
    "v_mfma_f32_16x16x32_bf16 a[112:115], v[176:179], v[120:123], a[112:115]\n"
    "v_exp_f32 v18, v18\n"
    "v_exp_f32 v19, v19\n"
    "v_mfma_f32_16x16x32_bf16 a[20:23], v[180:183], v[96:99], a[20:23]\n"
    "v_exp_f32 v20, v20\n"
    "v_cvt_pk_bf16_f32 v16, v16, v17\n"
    "v_cvt_pk_bf16_f32 v17, v18, v19\n"
    "v_mfma_f32_16x16x32_bf16 a[52:55], v[180:183], v[104:107], a[52:55]\n"
    "v_exp_f32 v21, v21\n"
    "v_mfma_f32_16x16x32_bf16 a[84:87], v[180:183], v[112:115], a[84:87]\n"
    "v_exp_f32 v22, v22\n"
    "v_exp_f32 v23, v23\n"
    "v_mfma_f32_16x16x32_bf16 a[116:119], v[180:183], v[120:123], a[116:119]\n"
    "v_exp_f32 v24, v24\n"
    "v_cvt_pk_bf16_f32 v18, v20, v21\n"
    "v_cvt_pk_bf16_f32 v19, v22, v23\n"
    "v_mfma_f32_16x16x32_bf16 a[24:27], v[184:187], v[96:99], a[24:27]\n"
    "v_exp_f32 v25, v25\n"
    "v_mfma_f32_16x16x32_bf16 a[56:59], v[184:187], v[104:107], a[56:59]\n"
    "v_exp_f32 v26, v26\n"
    "v_exp_f32 v27, v27\n"
    "v_mfma_f32_16x16x32_bf16 a[88:91], v[184:187], v[112:115], a[88:91]\n"
    "v_exp_f32 v28, v28\n"
    "v_mfma_f32_16x16x32_bf16 a[120:123], v[184:187], v[120:123], a[120:123]\n"
    "v_exp_f32 v29, v29\n"
    "v_cvt_pk_bf16_f32 v24, v24, v25\n"
    "v_cvt_pk_bf16_f32 v25, v26, v27\n"
    "v_mfma_f32_16x16x32_bf16 a[28:31], v[188:191], v[96:99], a[28:31]\n"
    "v_exp_f32 v30, v30\n"
    "v_exp_f32 v31, v31\n"
    "v_mfma_f32_16x16x32_bf16 a[60:63], v[188:191], v[104:107], a[60:63]\n"
    "v_exp_f32 v32, v32\n"
    "v_mfma_f32_16x16x32_bf16 a[92:95], v[188:191], v[112:115], a[92:95]\n"
    "v_exp_f32 v33, v33\n"
    "v_exp_f32 v34, v34\n"
    "v_mfma_f32_16x16x32_bf16 a[124:127], v[188:191], v[120:123], a[124:127]\n"
    "v_cvt_pk_bf16_f32 v26, v28, v29\n"
    "v_cvt_pk_bf16_f32 v27, v30, v31\n"
    "v_exp_f32 v35, v35\n"
    "v_mfma_f32_16x16x32_bf16 v[208:211], v[232:235], v[96:99], v[208:211]\n"
    "v_exp_f32 v36, v36\n"
    "v_mfma_f32_16x16x32_bf16 v[212:215], v[232:235], v[104:107], v[212:215]\n"
    "v_exp_f32 v37, v37\n"
    "v_exp_f32 v38, v38\n"
    "v_mfma_f32_16x16x32_bf16 v[216:219], v[232:235], v[112:115], v[216:219]\n"
    "v_exp_f32 v39, v39\n"
    "v_mfma_f32_16x16x32_bf16 v[220:223], v[232:235], v[120:123], v[220:223]\n"
    "s_waitcnt lgkmcnt(0)\n"
    "s_mov_b32 s54, 2097152\n"
    "s_cmp_lg_u32 s67, 0\n"
    "s_cselect_b32 s54, 0, s54\n"
    "s_nop 7\n"
    "v_cmp_nlt_f32 vcc, v208, v252\n"
    "v_rcp_f32 v236, v208\n"
    "s_or_b64 s[96:97], s[96:97], vcc\n"
    "v_cmp_nlt_f32 vcc, v212, v252\n"
    "v_rcp_f32 v237, v212\n"
    "s_or_b64 s[96:97], s[96:97], vcc\n"
    "v_cmp_nlt_f32 vcc, v216, v252\n"
    "v_rcp_f32 v238, v216\n"
    "s_or_b64 s[96:97], s[96:97], vcc\n"
    "v_cmp_nlt_f32 vcc, v220, v252\n"
    "v_rcp_f32 v239, v220\n"
    "s_or_b64 s[96:97], s[96:97], vcc\n"
    "v_accvgpr_read_b32 v128, a0\n"
    "v_accvgpr_read_b32 v129, a1\n"
    "v_accvgpr_read_b32 v130, a2\n"
    "v_accvgpr_read_b32 v131, a3\n"
    "v_accvgpr_read_b32 v132, a4\n"
    "v_accvgpr_read_b32 v133, a5\n"
    "v_accvgpr_read_b32 v134, a6\n"
    "v_accvgpr_read_b32 v135, a7\n"
    "v_pk_mul_f32 v[128:129], v[128:129], v[236:237] op_sel:[0,0] op_sel_hi:[1,0]\n"
    "v_pk_mul_f32 v[130:131], v[130:131], v[236:237] op_sel:[0,0] op_sel_hi:[1,0]\n"
    "v_pk_mul_f32 v[132:133], v[132:133], v[236:237] op_sel:[0,0] op_sel_hi:[1,0]\n"
    "v_pk_mul_f32 v[134:135], v[134:135], v[236:237] op_sel:[0,0] op_sel_hi:[1,0]\n"
    "v_cvt_pk_bf16_f32 v128, v128, v129\n"
    "v_cvt_pk_bf16_f32 v129, v130, v131\n"
    "v_cvt_pk_bf16_f32 v130, v132, v133\n"
    "v_cvt_pk_bf16_f32 v131, v134, v135\n"
    "ds_write_b128 v255, v[128:131] offset:0\n"
    "v_accvgpr_read_b32 v136, a8\n"
    "v_accvgpr_read_b32 v137, a9\n"
    "v_accvgpr_read_b32 v138, a10\n"
    "v_accvgpr_read_b32 v139, a11\n"
    "v_accvgpr_read_b32 v140, a12\n"
    "v_accvgpr_read_b32 v141, a13\n"
    "v_accvgpr_read_b32 v142, a14\n"
    "v_accvgpr_read_b32 v143, a15\n"
    "v_pk_mul_f32 v[136:137], v[136:137], v[236:237] op_sel:[0,0] op_sel_hi:[1,0]\n"
    "v_pk_mul_f32 v[138:139], v[138:139], v[236:237] op_sel:[0,0] op_sel_hi:[1,0]\n"
    "v_pk_mul_f32 v[140:141], v[140:141], v[236:237] op_sel:[0,0] op_sel_hi:[1,0]\n"
    "v_pk_mul_f32 v[142:143], v[142:143], v[236:237] op_sel:[0,0] op_sel_hi:[1,0]\n"
    "v_cvt_pk_bf16_f32 v136, v136, v137\n"
    "v_cvt_pk_bf16_f32 v137, v138, v139\n"
    "v_cvt_pk_bf16_f32 v138, v140, v141\n"
    "v_cvt_pk_bf16_f32 v139, v142, v143\n"
    "ds_write_b128 v255, v[136:139] offset:1024\n"
    "v_accvgpr_read_b32 v144, a16\n"
    "v_accvgpr_read_b32 v145, a17\n"
    "v_accvgpr_read_b32 v146, a18\n"
    "v_accvgpr_read_b32 v147, a19\n"
    "v_accvgpr_read_b32 v148, a20\n"
    "v_accvgpr_read_b32 v149, a21\n"
    "v_accvgpr_read_b32 v150, a22\n"
    "v_accvgpr_read_b32 v151, a23\n"
    "v_pk_mul_f32 v[144:145], v[144:145], v[236:237] op_sel:[0,0] op_sel_hi:[1,0]\n"
    "v_pk_mul_f32 v[146:147], v[146:147], v[236:237] op_sel:[0,0] op_sel_hi:[1,0]\n"
    "v_pk_mul_f32 v[148:149], v[148:149], v[236:237] op_sel:[0,0] op_sel_hi:[1,0]\n"
    "v_pk_mul_f32 v[150:151], v[150:151], v[236:237] op_sel:[0,0] op_sel_hi:[1,0]\n"
    "v_cvt_pk_bf16_f32 v144, v144, v145\n"
    "v_cvt_pk_bf16_f32 v145, v146, v147\n"
    "v_cvt_pk_bf16_f32 v146, v148, v149\n"
    "v_cvt_pk_bf16_f32 v147, v150, v151\n"
    "ds_write_b128 v255, v[144:147] offset:2048\n"
    "v_accvgpr_read_b32 v152, a24\n"
    "v_accvgpr_read_b32 v153, a25\n"
    "v_accvgpr_read_b32 v154, a26\n"
    "v_accvgpr_read_b32 v155, a27\n"
    "v_accvgpr_read_b32 v156, a28\n"
    "v_accvgpr_read_b32 v157, a29\n"
    "v_accvgpr_read_b32 v158, a30\n"
    "v_accvgpr_read_b32 v159, a31\n"
    "v_pk_mul_f32 v[152:153], v[152:153], v[236:237] op_sel:[0,0] op_sel_hi:[1,0]\n"
    "v_pk_mul_f32 v[154:155], v[154:155], v[236:237] op_sel:[0,0] op_sel_hi:[1,0]\n"
    "v_pk_mul_f32 v[156:157], v[156:157], v[236:237] op_sel:[0,0] op_sel_hi:[1,0]\n"
    "v_pk_mul_f32 v[158:159], v[158:159], v[236:237] op_sel:[0,0] op_sel_hi:[1,0]\n"
    "v_cvt_pk_bf16_f32 v152, v152, v153\n"
    "v_cvt_pk_bf16_f32 v153, v154, v155\n"
    "v_cvt_pk_bf16_f32 v154, v156, v157\n"
    "v_cvt_pk_bf16_f32 v155, v158, v159\n"
    "ds_write_b128 v255, v[152:155] offset:3072\n"
    "v_accvgpr_read_b32 v160, a32\n"
    "v_accvgpr_read_b32 v161, a33\n"
    "v_accvgpr_read_b32 v162, a34\n"
    "v_accvgpr_read_b32 v163, a35\n"
    "v_accvgpr_read_b32 v164, a36\n"
    "v_accvgpr_read_b32 v165, a37\n"
    "v_accvgpr_read_b32 v166, a38\n"
    "v_accvgpr_read_b32 v167, a39\n"
    "v_pk_mul_f32 v[160:161], v[160:161], v[236:237] op_sel:[0,1] op_sel_hi:[1,1]\n"
    "v_pk_mul_f32 v[162:163], v[162:163], v[236:237] op_sel:[0,1] op_sel_hi:[1,1]\n"
    "v_pk_mul_f32 v[164:165], v[164:165], v[236:237] op_sel:[0,1] op_sel_hi:[1,1]\n"
    "v_pk_mul_f32 v[166:167], v[166:167], v[236:237] op_sel:[0,1] op_sel_hi:[1,1]\n"
    "v_cvt_pk_bf16_f32 v160, v160, v161\n"
    "v_cvt_pk_bf16_f32 v161, v162, v163\n"
    "v_cvt_pk_bf16_f32 v162, v164, v165\n"
    "v_cvt_pk_bf16_f32 v163, v166, v167\n"
    "ds_write_b128 v255, v[160:163] offset:4096\n"
    "v_accvgpr_read_b32 v168, a40\n"
    "v_accvgpr_read_b32 v169, a41\n"
    "v_accvgpr_read_b32 v170, a42\n"
    "v_accvgpr_read_b32 v171, a43\n"
    "v_accvgpr_read_b32 v172, a44\n"
    "v_accvgpr_read_b32 v173, a45\n"
    "v_accvgpr_read_b32 v174, a46\n"
    "v_accvgpr_read_b32 v175, a47\n"
    "v_pk_mul_f32 v[168:169], v[168:169], v[236:237] op_sel:[0,1] op_sel_hi:[1,1]\n"
    "v_pk_mul_f32 v[170:171], v[170:171], v[236:237] op_sel:[0,1] op_sel_hi:[1,1]\n"
    "v_pk_mul_f32 v[172:173], v[172:173], v[236:237] op_sel:[0,1] op_sel_hi:[1,1]\n"
    "v_pk_mul_f32 v[174:175], v[174:175], v[236:237] op_sel:[0,1] op_sel_hi:[1,1]\n"
    "v_cvt_pk_bf16_f32 v168, v168, v169\n"
    "v_cvt_pk_bf16_f32 v169, v170, v171\n"
    "v_cvt_pk_bf16_f32 v170, v172, v173\n"
    "v_cvt_pk_bf16_f32 v171, v174, v175\n"
    "ds_write_b128 v255, v[168:171] offset:5120\n"
    "v_accvgpr_read_b32 v176, a48\n"
    "v_accvgpr_read_b32 v177, a49\n"
    "v_accvgpr_read_b32 v178, a50\n"
    "v_accvgpr_read_b32 v179, a51\n"
    "v_accvgpr_read_b32 v180, a52\n"
    "v_accvgpr_read_b32 v181, a53\n"
    "v_accvgpr_read_b32 v182, a54\n"
    "v_accvgpr_read_b32 v183, a55\n"
    "v_pk_mul_f32 v[176:177], v[176:177], v[236:237] op_sel:[0,1] op_sel_hi:[1,1]\n"
    "v_pk_mul_f32 v[178:179], v[178:179], v[236:237] op_sel:[0,1] op_sel_hi:[1,1]\n"
    "v_pk_mul_f32 v[180:181], v[180:181], v[236:237] op_sel:[0,1] op_sel_hi:[1,1]\n"
    "v_pk_mul_f32 v[182:183], v[182:183], v[236:237] op_sel:[0,1] op_sel_hi:[1,1]\n"
    "v_cvt_pk_bf16_f32 v176, v176, v177\n"
    "v_cvt_pk_bf16_f32 v177, v178, v179\n"
    "v_cvt_pk_bf16_f32 v178, v180, v181\n"
    "v_cvt_pk_bf16_f32 v179, v182, v183\n"
    "ds_write_b128 v255, v[176:179] offset:6144\n"
    "v_accvgpr_read_b32 v184, a56\n"
    "v_accvgpr_read_b32 v185, a57\n"
    "v_accvgpr_read_b32 v186, a58\n"
    "v_accvgpr_read_b32 v187, a59\n"
    "v_accvgpr_read_b32 v188, a60\n"
    "v_accvgpr_read_b32 v189, a61\n"
    "v_accvgpr_read_b32 v190, a62\n"
    "v_accvgpr_read_b32 v191, a63\n"
    "v_pk_mul_f32 v[184:185], v[184:185], v[236:237] op_sel:[0,1] op_sel_hi:[1,1]\n"
    "v_pk_mul_f32 v[186:187], v[186:187], v[236:237] op_sel:[0,1] op_sel_hi:[1,1]\n"
    "v_pk_mul_f32 v[188:189], v[188:189], v[236:237] op_sel:[0,1] op_sel_hi:[1,1]\n"
    "v_pk_mul_f32 v[190:191], v[190:191], v[236:237] op_sel:[0,1] op_sel_hi:[1,1]\n"
    "v_cvt_pk_bf16_f32 v184, v184, v185\n"
    "v_cvt_pk_bf16_f32 v185, v186, v187\n"
    "v_cvt_pk_bf16_f32 v186, v188, v189\n"
    "v_cvt_pk_bf16_f32 v187, v190, v191\n"
    "ds_write_b128 v255, v[184:187] offset:7168\n"
    "v_accvgpr_read_b32 v128, a64\n"
    "v_accvgpr_read_b32 v129, a65\n"
    "v_accvgpr_read_b32 v130, a66\n"
    "v_accvgpr_read_b32 v131, a67\n"
    "v_accvgpr_read_b32 v132, a68\n"
    "v_accvgpr_read_b32 v133, a69\n"
    "v_accvgpr_read_b32 v134, a70\n"
    "v_accvgpr_read_b32 v135, a71\n"
    "v_pk_mul_f32 v[128:129], v[128:129], v[238:239] op_sel:[0,0] op_sel_hi:[1,0]\n"
    "v_pk_mul_f32 v[130:131], v[130:131], v[238:239] op_sel:[0,0] op_sel_hi:[1,0]\n"
    "v_pk_mul_f32 v[132:133], v[132:133], v[238:239] op_sel:[0,0] op_sel_hi:[1,0]\n"
    "v_pk_mul_f32 v[134:135], v[134:135], v[238:239] op_sel:[0,0] op_sel_hi:[1,0]\n"
    "v_cvt_pk_bf16_f32 v128, v128, v129\n"
    "v_cvt_pk_bf16_f32 v129, v130, v131\n"
    "v_cvt_pk_bf16_f32 v130, v132, v133\n"
    "v_cvt_pk_bf16_f32 v131, v134, v135\n"
    "ds_write_b128 v255, v[128:131] offset:8192\n"
    "v_accvgpr_read_b32 v136, a72\n"
    "v_accvgpr_read_b32 v137, a73\n"
    "v_accvgpr_read_b32 v138, a74\n"
    "v_accvgpr_read_b32 v139, a75\n"
    "v_accvgpr_read_b32 v140, a76\n"
    "v_accvgpr_read_b32 v141, a77\n"
    "v_accvgpr_read_b32 v142, a78\n"
    "v_accvgpr_read_b32 v143, a79\n"
    "v_pk_mul_f32 v[136:137], v[136:137], v[238:239] op_sel:[0,0] op_sel_hi:[1,0]\n"
    "v_pk_mul_f32 v[138:139], v[138:139], v[238:239] op_sel:[0,0] op_sel_hi:[1,0]\n"
    "v_pk_mul_f32 v[140:141], v[140:141], v[238:239] op_sel:[0,0] op_sel_hi:[1,0]\n"
    "v_pk_mul_f32 v[142:143], v[142:143], v[238:239] op_sel:[0,0] op_sel_hi:[1,0]\n"
    "v_cvt_pk_bf16_f32 v136, v136, v137\n"
    "v_cvt_pk_bf16_f32 v137, v138, v139\n"
    "v_cvt_pk_bf16_f32 v138, v140, v141\n"
    "v_cvt_pk_bf16_f32 v139, v142, v143\n"
    "ds_write_b128 v255, v[136:139] offset:9216\n"
    "v_accvgpr_read_b32 v144, a80\n"
    "v_accvgpr_read_b32 v145, a81\n"
    "v_accvgpr_read_b32 v146, a82\n"
    "v_accvgpr_read_b32 v147, a83\n"
    "v_accvgpr_read_b32 v148, a84\n"
    "v_accvgpr_read_b32 v149, a85\n"
    "v_accvgpr_read_b32 v150, a86\n"
    "v_accvgpr_read_b32 v151, a87\n"
    "v_pk_mul_f32 v[144:145], v[144:145], v[238:239] op_sel:[0,0] op_sel_hi:[1,0]\n"
    "v_pk_mul_f32 v[146:147], v[146:147], v[238:239] op_sel:[0,0] op_sel_hi:[1,0]\n"
    "v_pk_mul_f32 v[148:149], v[148:149], v[238:239] op_sel:[0,0] op_sel_hi:[1,0]\n"
    "v_pk_mul_f32 v[150:151], v[150:151], v[238:239] op_sel:[0,0] op_sel_hi:[1,0]\n"
    "v_cvt_pk_bf16_f32 v144, v144, v145\n"
    "v_cvt_pk_bf16_f32 v145, v146, v147\n"
    "v_cvt_pk_bf16_f32 v146, v148, v149\n"
    "v_cvt_pk_bf16_f32 v147, v150, v151\n"
    "ds_write_b128 v255, v[144:147] offset:10240\n"
    "v_accvgpr_read_b32 v152, a88\n"
    "v_accvgpr_read_b32 v153, a89\n"
    "v_accvgpr_read_b32 v154, a90\n"
    "v_accvgpr_read_b32 v155, a91\n"
    "v_accvgpr_read_b32 v156, a92\n"
    "v_accvgpr_read_b32 v157, a93\n"
    "v_accvgpr_read_b32 v158, a94\n"
    "v_accvgpr_read_b32 v159, a95\n"
    "v_pk_mul_f32 v[152:153], v[152:153], v[238:239] op_sel:[0,0] op_sel_hi:[1,0]\n"
    "v_pk_mul_f32 v[154:155], v[154:155], v[238:239] op_sel:[0,0] op_sel_hi:[1,0]\n"
    "v_pk_mul_f32 v[156:157], v[156:157], v[238:239] op_sel:[0,0] op_sel_hi:[1,0]\n"
    "v_pk_mul_f32 v[158:159], v[158:159], v[238:239] op_sel:[0,0] op_sel_hi:[1,0]\n"
    "v_cvt_pk_bf16_f32 v152, v152, v153\n"
    "v_cvt_pk_bf16_f32 v153, v154, v155\n"
    "v_cvt_pk_bf16_f32 v154, v156, v157\n"
    "v_cvt_pk_bf16_f32 v155, v158, v159\n"
    "ds_write_b128 v255, v[152:155] offset:11264\n"
    "v_accvgpr_read_b32 v160, a96\n"
    "v_accvgpr_read_b32 v161, a97\n"
    "v_accvgpr_read_b32 v162, a98\n"
    "v_accvgpr_read_b32 v163, a99\n"
    "v_accvgpr_read_b32 v164, a100\n"
    "v_accvgpr_read_b32 v165, a101\n"
    "v_accvgpr_read_b32 v166, a102\n"
    "v_accvgpr_read_b32 v167, a103\n"
    "v_pk_mul_f32 v[160:161], v[160:161], v[238:239] op_sel:[0,1] op_sel_hi:[1,1]\n"
    "v_pk_mul_f32 v[162:163], v[162:163], v[238:239] op_sel:[0,1] op_sel_hi:[1,1]\n"
    "v_pk_mul_f32 v[164:165], v[164:165], v[238:239] op_sel:[0,1] op_sel_hi:[1,1]\n"
    "v_pk_mul_f32 v[166:167], v[166:167], v[238:239] op_sel:[0,1] op_sel_hi:[1,1]\n"
    "v_cvt_pk_bf16_f32 v160, v160, v161\n"
    "v_cvt_pk_bf16_f32 v161, v162, v163\n"
    "v_cvt_pk_bf16_f32 v162, v164, v165\n"
    "v_cvt_pk_bf16_f32 v163, v166, v167\n"
    "ds_write_b128 v255, v[160:163] offset:12288\n"
    "v_accvgpr_read_b32 v168, a104\n"
    "v_accvgpr_read_b32 v169, a105\n"
    "v_accvgpr_read_b32 v170, a106\n"
    "v_accvgpr_read_b32 v171, a107\n"
    "v_accvgpr_read_b32 v172, a108\n"
    "v_accvgpr_read_b32 v173, a109\n"
    "v_accvgpr_read_b32 v174, a110\n"
    "v_accvgpr_read_b32 v175, a111\n"
    "v_pk_mul_f32 v[168:169], v[168:169], v[238:239] op_sel:[0,1] op_sel_hi:[1,1]\n"
    "v_pk_mul_f32 v[170:171], v[170:171], v[238:239] op_sel:[0,1] op_sel_hi:[1,1]\n"
    "v_pk_mul_f32 v[172:173], v[172:173], v[238:239] op_sel:[0,1] op_sel_hi:[1,1]\n"
    "v_pk_mul_f32 v[174:175], v[174:175], v[238:239] op_sel:[0,1] op_sel_hi:[1,1]\n"
    "v_cvt_pk_bf16_f32 v168, v168, v169\n"
    "v_cvt_pk_bf16_f32 v169, v170, v171\n"
    "v_cvt_pk_bf16_f32 v170, v172, v173\n"
    "v_cvt_pk_bf16_f32 v171, v174, v175\n"
    "ds_write_b128 v255, v[168:171] offset:13312\n"
    "v_accvgpr_read_b32 v176, a112\n"
    "v_accvgpr_read_b32 v177, a113\n"
    "v_accvgpr_read_b32 v178, a114\n"
    "v_accvgpr_read_b32 v179, a115\n"
    "v_accvgpr_read_b32 v180, a116\n"
    "v_accvgpr_read_b32 v181, a117\n"
    "v_accvgpr_read_b32 v182, a118\n"
    "v_accvgpr_read_b32 v183, a119\n"
    "v_pk_mul_f32 v[176:177], v[176:177], v[238:239] op_sel:[0,1] op_sel_hi:[1,1]\n"
    "v_pk_mul_f32 v[178:179], v[178:179], v[238:239] op_sel:[0,1] op_sel_hi:[1,1]\n"
    "v_pk_mul_f32 v[180:181], v[180:181], v[238:239] op_sel:[0,1] op_sel_hi:[1,1]\n"
    "v_pk_mul_f32 v[182:183], v[182:183], v[238:239] op_sel:[0,1] op_sel_hi:[1,1]\n"
    "v_cvt_pk_bf16_f32 v176, v176, v177\n"
    "v_cvt_pk_bf16_f32 v177, v178, v179\n"
    "v_cvt_pk_bf16_f32 v178, v180, v181\n"
    "v_cvt_pk_bf16_f32 v179, v182, v183\n"
    "ds_write_b128 v255, v[176:179] offset:14336\n"
    "v_accvgpr_read_b32 v184, a120\n"
    "v_accvgpr_read_b32 v185, a121\n"
    "v_accvgpr_read_b32 v186, a122\n"
    "v_accvgpr_read_b32 v187, a123\n"
    "v_accvgpr_read_b32 v188, a124\n"
    "v_accvgpr_read_b32 v189, a125\n"
    "v_accvgpr_read_b32 v190, a126\n"
    "v_accvgpr_read_b32 v191, a127\n"
    "v_pk_mul_f32 v[184:185], v[184:185], v[238:239] op_sel:[0,1] op_sel_hi:[1,1]\n"
    "v_pk_mul_f32 v[186:187], v[186:187], v[238:239] op_sel:[0,1] op_sel_hi:[1,1]\n"
    "v_pk_mul_f32 v[188:189], v[188:189], v[238:239] op_sel:[0,1] op_sel_hi:[1,1]\n"
    "v_pk_mul_f32 v[190:191], v[190:191], v[238:239] op_sel:[0,1] op_sel_hi:[1,1]\n"
    "v_cvt_pk_bf16_f32 v184, v184, v185\n"
    "v_cvt_pk_bf16_f32 v185, v186, v187\n"
    "v_cvt_pk_bf16_f32 v186, v188, v189\n"
    "v_cvt_pk_bf16_f32 v187, v190, v191\n"
    "ds_write_b128 v255, v[184:187] offset:15360\n"
    "s_nop 1\n"
    "s_movk_i32 s74, 7\n"
    "P1S%=:\n"
    "v_mfma_f32_16x16x32_bf16 v[64:67], a[192:195], a[128:131], v[192:195]\n"
    "v_exp_f32 v40, v40\n"
    "v_mfma_f32_16x16x32_bf16 v[72:75], a[192:195], a[144:147], v[196:199]\n"
    "ds_read_b64_tr_b16 v[128:129], v250 offset:0\n"
    "ds_read_b64_tr_b16 v[130:131], v250 offset:4096\n"
    "v_mfma_f32_16x16x32_bf16 v[80:83], a[192:195], a[160:163], v[200:203]\n"
    "v_cvt_pk_bf16_f32 v32, v32, v33\n"
    "v_cvt_pk_bf16_f32 v33, v34, v35\n"
    "v_mfma_f32_16x16x32_bf16 v[88:91], a[192:195], a[176:179], v[204:207]\n"
    "v_exp_f32 v41, v41\n"
    "v_mfma_f32_16x16x32_bf16 v[68:71], a[196:199], a[128:131], v[192:195]\n"
    "ds_read_b64_tr_b16 v[132:133], v251 offset:0\n"
    "ds_read_b64_tr_b16 v[134:135], v251 offset:4096\n"
    "v_mfma_f32_16x16x32_bf16 v[76:79], a[196:199], a[144:147], v[196:199]\n"
    "v_mfma_f32_16x16x32_bf16 v[84:87], a[196:199], a[160:163], v[200:203]\n"
    "v_exp_f32 v42, v42\n"
    "v_mfma_f32_16x16x32_bf16 v[92:95], a[196:199], a[176:179], v[204:207]\n"
    "ds_read_b64_tr_b16 v[136:137], v250 offset:512\n"
    "ds_read_b64_tr_b16 v[138:139], v250 offset:4608\n"
    "v_mfma_f32_16x16x32_bf16 v[96:99], a[200:203], a[128:131], v[192:195]\n"
    "v_cvt_pk_bf16_f32 v34, v36, v37\n"
    "v_cvt_pk_bf16_f32 v35, v38, v39\n"
    "v_mfma_f32_16x16x32_bf16 v[104:107], a[200:203], a[144:147], v[196:199]\n"
    "v_mfma_f32_16x16x32_bf16 v[112:115], a[200:203], a[160:163], v[200:203]\n"
    "ds_read_b64_tr_b16 v[140:141], v251 offset:512\n"
    "ds_read_b64_tr_b16 v[142:143], v251 offset:4608\n"
    "v_mfma_f32_16x16x32_bf16 v[120:123], a[200:203], a[176:179], v[204:207]\n"
    "v_exp_f32 v43, v43\n"
    "v_mfma_f32_16x16x32_bf16 v[100:103], a[204:207], a[128:131], v[192:195]\n"
    "v_exp_f32 v44, v44\n"
    "v_mfma_f32_16x16x32_bf16 v[108:111], a[204:207], a[144:147], v[196:199]\n"
    "ds_read_b64_tr_b16 v[144:145], v250 offset:1024\n"
    "ds_read_b64_tr_b16 v[146:147], v250 offset:5120\n"
    "v_mfma_f32_16x16x32_bf16 v[116:119], a[204:207], a[160:163], v[200:203]\n"
    "v_exp_f32 v45, v45\n"
    "v_mfma_f32_16x16x32_bf16 v[124:127], a[204:207], a[176:179], v[204:207]\n"
    "v_mfma_f32_16x16x32_bf16 v[64:67], a[208:211], a[132:135], v[64:67]\n"
    "ds_read_b64_tr_b16 v[148:149], v251 offset:1024\n"
    "ds_read_b64_tr_b16 v[150:151], v251 offset:5120\n"
    "v_mfma_f32_16x16x32_bf16 v[72:75], a[208:211], a[148:151], v[72:75]\n"
    "v_cvt_pk_bf16_f32 v40, v40, v41\n"
    "v_cvt_pk_bf16_f32 v41, v42, v43\n"
    "v_mfma_f32_16x16x32_bf16 v[80:83], a[208:211], a[164:167], v[80:83]\n"
    "v_exp_f32 v46, v46\n"
    "v_mfma_f32_16x16x32_bf16 v[88:91], a[208:211], a[180:183], v[88:91]\n"
    "ds_read_b64_tr_b16 v[152:153], v250 offset:1536\n"
    "ds_read_b64_tr_b16 v[154:155], v250 offset:5632\n"
    "v_mfma_f32_16x16x32_bf16 v[68:71], a[212:215], a[132:135], v[68:71]\n"
    "v_mfma_f32_16x16x32_bf16 v[76:79], a[212:215], a[148:151], v[76:79]\n"
    "v_exp_f32 v47, v47\n"
    "v_mfma_f32_16x16x32_bf16 v[84:87], a[212:215], a[164:167], v[84:87]\n"
    "ds_read_b64_tr_b16 v[156:157], v251 offset:1536\n"
    "ds_read_b64_tr_b16 v[158:159], v251 offset:5632\n"
    "v_mfma_f32_16x16x32_bf16 v[92:95], a[212:215], a[180:183], v[92:95]\n"
    "v_exp_f32 v48, v48\n"
    "v_mfma_f32_16x16x32_bf16 v[96:99], a[216:219], a[132:135], v[96:99]\n"
    "v_cvt_pk_bf16_f32 v42, v44, v45\n"
    "v_cvt_pk_bf16_f32 v43, v46, v47\n"
    "v_mfma_f32_16x16x32_bf16 v[104:107], a[216:219], a[148:151], v[104:107]\n"
    "v_mfma_f32_16x16x32_bf16 v[112:115], a[216:219], a[164:167], v[112:115]\n"
    "ds_read_b64_tr_b16 v[160:161], v250 offset:8192\n"
    "ds_read_b64_tr_b16 v[162:163], v250 offset:12288\n"
    "v_mfma_f32_16x16x32_bf16 v[120:123], a[216:219], a[180:183], v[120:123]\n"
    "v_exp_f32 v49, v49\n"
    "v_mfma_f32_16x16x32_bf16 v[100:103], a[220:223], a[132:135], v[100:103]\n"
    "v_exp_f32 v50, v50\n"
    "v_mfma_f32_16x16x32_bf16 v[108:111], a[220:223], a[148:151], v[108:111]\n"
    "ds_read_b64_tr_b16 v[164:165], v251 offset:8192\n"
    "ds_read_b64_tr_b16 v[166:167], v251 offset:12288\n"
    "v_mfma_f32_16x16x32_bf16 v[116:119], a[220:223], a[164:167], v[116:119]\n"
    "v_mfma_f32_16x16x32_bf16 v[124:127], a[220:223], a[180:183], v[124:127]\n"
    "v_exp_f32 v51, v51\n"
    "v_mfma_f32_16x16x32_bf16 v[64:67], a[224:227], a[136:139], v[64:67]\n"
    "ds_read_b64_tr_b16 v[168:169], v250 offset:8704\n"
    "ds_read_b64_tr_b16 v[170:171], v250 offset:12800\n"
    "v_mfma_f32_16x16x32_bf16 v[72:75], a[224:227], a[152:155], v[72:75]\n"
    "v_exp_f32 v52, v52\n"
    "v_mfma_f32_16x16x32_bf16 v[80:83], a[224:227], a[168:171], v[80:83]\n"
    "v_mfma_f32_16x16x32_bf16 v[88:91], a[224:227], a[184:187], v[88:91]\n"
    "ds_read_b64_tr_b16 v[172:173], v251 offset:8704\n"
    "ds_read_b64_tr_b16 v[174:175], v251 offset:12800\n"
    "v_mfma_f32_16x16x32_bf16 v[68:71], a[228:231], a[136:139], v[68:71]\n"
    "v_cvt_pk_bf16_f32 v48, v48, v49\n"
    "v_cvt_pk_bf16_f32 v49, v50, v51\n"
    "v_mfma_f32_16x16x32_bf16 v[76:79], a[228:231], a[152:155], v[76:79]\n"
    "v_exp_f32 v53, v53\n"
    "v_mfma_f32_16x16x32_bf16 v[84:87], a[228:231], a[168:171], v[84:87]\n"
    "ds_read_b64_tr_b16 v[176:177], v250 offset:9216\n"
    "ds_read_b64_tr_b16 v[178:179], v250 offset:13312\n"
    "v_mfma_f32_16x16x32_bf16 v[92:95], a[228:231], a[184:187], v[92:95]\n"
    "v_exp_f32 v54, v54\n"
    "v_mfma_f32_16x16x32_bf16 v[96:99], a[232:235], a[136:139], v[96:99]\n"
    "v_mfma_f32_16x16x32_bf16 v[104:107], a[232:235], a[152:155], v[104:107]\n"
    "ds_read_b64_tr_b16 v[180:181], v251 offset:9216\n"
    "ds_read_b64_tr_b16 v[182:183], v251 offset:13312\n"
    "v_mfma_f32_16x16x32_bf16 v[112:115], a[232:235], a[168:171], v[112:115]\n"
    "v_exp_f32 v55, v55\n"
    "v_mfma_f32_16x16x32_bf16 v[120:123], a[232:235], a[184:187], v[120:123]\n"
    "v_exp_f32 v56, v56\n"
    "v_mfma_f32_16x16x32_bf16 v[100:103], a[236:239], a[136:139], v[100:103]\n"
    "ds_read_b64_tr_b16 v[184:185], v250 offset:9728\n"
    "ds_read_b64_tr_b16 v[186:187], v250 offset:13824\n"
    "v_mfma_f32_16x16x32_bf16 v[108:111], a[236:239], a[152:155], v[108:111]\n"
    "v_mfma_f32_16x16x32_bf16 v[116:119], a[236:239], a[168:171], v[116:119]\n"
    "v_cvt_pk_bf16_f32 v50, v52, v53\n"
    "v_cvt_pk_bf16_f32 v51, v54, v55\n"
    "v_mfma_f32_16x16x32_bf16 v[124:127], a[236:239], a[184:187], v[124:127]\n"
    "ds_read_b64_tr_b16 v[188:189], v251 offset:9728\n"
    "ds_read_b64_tr_b16 v[190:191], v251 offset:13824\n"
    "v_mfma_f32_16x16x32_bf16 v[64:67], a[240:243], a[140:143], v[64:67]\n"
    "v_exp_f32 v57, v57\n"
    "v_mfma_f32_16x16x32_bf16 v[72:75], a[240:243], a[156:159], v[72:75]\n"
    "v_exp_f32 v58, v58\n"
    "v_mfma_f32_16x16x32_bf16 v[80:83], a[240:243], a[172:175], v[80:83]\n"
    "v_mfma_f32_16x16x32_bf16 v[88:91], a[240:243], a[188:191], v[88:91]\n"
    "v_exp_f32 v59, v59\n"
    "v_mfma_f32_16x16x32_bf16 v[68:71], a[244:247], a[140:143], v[68:71]\n"
    "v_exp_f32 v60, v60\n"
    "v_mfma_f32_16x16x32_bf16 v[76:79], a[244:247], a[156:159], v[76:79]\n"
    "v_mfma_f32_16x16x32_bf16 v[84:87], a[244:247], a[172:175], v[84:87]\n"
    "v_cvt_pk_bf16_f32 v56, v56, v57\n"
    "v_cvt_pk_bf16_f32 v57, v58, v59\n"
    "v_mfma_f32_16x16x32_bf16 v[92:95], a[244:247], a[188:191], v[92:95]\n"
    "v_exp_f32 v61, v61\n"
    "v_mfma_f32_16x16x32_bf16 v[96:99], a[248:251], a[140:143], v[96:99]\n"
    "v_mfma_f32_16x16x32_bf16 v[104:107], a[248:251], a[156:159], v[104:107]\n"
    "v_exp_f32 v62, v62\n"
    "v_mfma_f32_16x16x32_bf16 v[112:115], a[248:251], a[172:175], v[112:115]\n"
    "v_exp_f32 v63, v63\n"
    "v_mfma_f32_16x16x32_bf16 v[120:123], a[248:251], a[188:191], v[120:123]\n"
    "s_nop 0\n"
    "v_mfma_f32_16x16x32_bf16 v[100:103], a[252:255], a[140:143], v[100:103]\n"
    "v_cvt_pk_bf16_f32 v58, v60, v61\n"
    "v_cvt_pk_bf16_f32 v59, v62, v63\n"
    "v_mfma_f32_16x16x32_bf16 v[108:111], a[252:255], a[156:159], v[108:111]\n"
    "v_mfma_f32_16x16x32_bf16 v[116:119], a[252:255], a[172:175], v[116:119]\n"
    "v_mfma_f32_16x16x32_bf16 v[124:127], a[252:255], a[188:191], v[124:127]\n"
    "s_waitcnt vmcnt(8) lgkmcnt(0)\n"
    "s_barrier\n"
    "v_mfma_f32_16x16x32_bf16 a[0:3], v[128:131], v[0:3], 0\n"
    "v_mfma_f32_16x16x32_bf16 a[32:35], v[128:131], v[8:11], 0\n"
    "s_add_u32 m0, s72, s99\n"
    "v_mfma_f32_16x16x32_bf16 a[64:67], v[128:131], v[16:19], 0\n"
    "buffer_load_dwordx4 v224, s[36:39], s70 offen offset:0 nt lds\n"
    "v_mfma_f32_16x16x32_bf16 a[96:99], v[128:131], v[24:27], 0\n"
    "v_mfma_f32_16x16x32_bf16 a[4:7], v[132:135], v[0:3], 0\n"
    "buffer_load_dwordx4 v225, s[36:39], s70 offen offset:1024 nt lds\n"
    "s_swappc_b64 s[86:87], s[92:93]\n"
    "v_mfma_f32_16x16x32_bf16 a[36:39], v[132:135], v[8:11], 0\n"
    "v_exp_f32 v64, v64\n"
    "v_mfma_f32_16x16x32_bf16 a[68:71], v[132:135], v[16:19], 0\n"
    "buffer_load_dwordx4 v226, s[36:39], s70 offen offset:2048 nt lds\n"
    "v_mfma_f32_16x16x32_bf16 a[100:103], v[132:135], v[24:27], 0\n"
    "v_exp_f32 v65, v65\n"
    "v_mfma_f32_16x16x32_bf16 a[8:11], v[136:139], v[0:3], 0\n"
    "buffer_load_dwordx4 v227, s[36:39], s70 offen offset:3072 nt lds\n"
    "v_mfma_f32_16x16x32_bf16 a[40:43], v[136:139], v[8:11], 0\n"
    "s_add_u32 m0, s73, s83\n"
    "s_add_u32 s70, s70, 16384\n"
    "v_mfma_f32_16x16x32_bf16 a[72:75], v[136:139], v[16:19], 0\n"
    "buffer_load_dwordx4 v224, s[40:43], s71 offen offset:0 nt lds\n"
    "v_mfma_f32_16x16x32_bf16 a[104:107], v[136:139], v[24:27], 0\n"
    "v_add_u32 v250, s99, v230\n"
    "v_add_u32 v251, s99, v231\n"
    "v_mfma_f32_16x16x32_bf16 a[12:15], v[140:143], v[0:3], 0\n"
    "buffer_load_dwordx4 v225, s[40:43], s71 offen offset:1024 nt lds\n"
    "v_mfma_f32_16x16x32_bf16 a[44:47], v[140:143], v[8:11], 0\n"
    "s_mov_b32 s83, s99\n"
    "s_add_u32 s99, s99, 16384\n"
    "v_mfma_f32_16x16x32_bf16 a[76:79], v[140:143], v[16:19], 0\n"
    "buffer_load_dwordx4 v226, s[40:43], s71 offen offset:2048 nt lds\n"
    "v_mfma_f32_16x16x32_bf16 a[108:111], v[140:143], v[24:27], 0\n"
    "s_cmp_eq_u32 s99, 49152\n"
    "s_cselect_b32 s99, 0, s99\n"
    "v_mfma_f32_16x16x32_bf16 a[16:19], v[144:147], v[0:3], 0\n"
    "buffer_load_dwordx4 v227, s[40:43], s71 offen offset:3072 nt lds\n"
    "v_mfma_f32_16x16x32_bf16 a[48:51], v[144:147], v[8:11], 0\n"
    "s_add_u32 s71, s71, 16384\n"
    "v_add_u32 v253, s99, v228\n"
    "v_mfma_f32_16x16x32_bf16 a[80:83], v[144:147], v[16:19], 0\n"
    "ds_read_b128 a[192:195], v253 offset:0\n"
    "ds_read_b128 a[196:199], v253 offset:4096\n"
    "v_mfma_f32_16x16x32_bf16 a[112:115], v[144:147], v[24:27], 0\n"
    "v_exp_f32 v66, v66\n"
    "v_mfma_f32_16x16x32_bf16 a[20:23], v[148:151], v[0:3], 0\n"
    "v_exp_f32 v67, v67\n"
    "v_mfma_f32_16x16x32_bf16 a[52:55], v[148:151], v[8:11], 0\n"
    "v_exp_f32 v68, v68\n"
    "v_mfma_f32_16x16x32_bf16 a[84:87], v[148:151], v[16:19], 0\n"
    "v_cvt_pk_bf16_f32 v64, v64, v65\n"
    "v_cvt_pk_bf16_f32 v65, v66, v67\n"
    "v_mfma_f32_16x16x32_bf16 a[116:119], v[148:151], v[24:27], 0\n"
    "ds_read_b128 a[200:203], v253 offset:8192\n"
    "ds_read_b128 a[204:207], v253 offset:12288\n"
    "v_mfma_f32_16x16x32_bf16 a[24:27], v[152:155], v[0:3], 0\n"
    "v_exp_f32 v69, v69\n"
    "v_mfma_f32_16x16x32_bf16 a[56:59], v[152:155], v[8:11], 0\n"
    "v_exp_f32 v70, v70\n"
    "v_mfma_f32_16x16x32_bf16 a[88:91], v[152:155], v[16:19], 0\n"
    "v_exp_f32 v71, v71\n"
    "v_mfma_f32_16x16x32_bf16 a[120:123], v[152:155], v[24:27], 0\n"
    "v_exp_f32 v72, v72\n"
    "v_mfma_f32_16x16x32_bf16 a[28:31], v[156:159], v[0:3], 0\n"
    "ds_read_b128 a[208:211], v253 offset:512\n"
    "ds_read_b128 a[212:215], v253 offset:4608\n"
    "v_mfma_f32_16x16x32_bf16 a[60:63], v[156:159], v[8:11], 0\n"
    "v_cvt_pk_bf16_f32 v66, v68, v69\n"
    "v_cvt_pk_bf16_f32 v67, v70, v71\n"
    "v_mfma_f32_16x16x32_bf16 a[92:95], v[156:159], v[16:19], 0\n"
    "v_exp_f32 v73, v73\n"
    "v_mfma_f32_16x16x32_bf16 a[124:127], v[156:159], v[24:27], 0\n"
    "v_exp_f32 v74, v74\n"
    "v_mfma_f32_16x16x32_bf16 v[208:211], v[232:235], v[0:3], 0\n"
    "v_exp_f32 v75, v75\n"
    "v_mfma_f32_16x16x32_bf16 v[212:215], v[232:235], v[8:11], 0\n"
    "ds_read_b128 a[216:219], v253 offset:8704\n"
    "ds_read_b128 a[220:223], v253 offset:12800\n"
    "v_mfma_f32_16x16x32_bf16 v[216:219], v[232:235], v[16:19], 0\n"
    "v_exp_f32 v76, v76\n"
    "v_mfma_f32_16x16x32_bf16 v[220:223], v[232:235], v[24:27], 0\n"
    "v_cvt_pk_bf16_f32 v72, v72, v73\n"
    "v_cvt_pk_bf16_f32 v73, v74, v75\n"
    "v_mfma_f32_16x16x32_bf16 a[0:3], v[160:163], v[32:35], a[0:3]\n"
    "v_exp_f32 v77, v77\n"
    "v_mfma_f32_16x16x32_bf16 a[32:35], v[160:163], v[40:43], a[32:35]\n"
    "v_exp_f32 v78, v78\n"
    "v_mfma_f32_16x16x32_bf16 a[64:67], v[160:163], v[48:51], a[64:67]\n"
    "ds_read_b128 a[224:227], v253 offset:1024\n"
    "ds_read_b128 a[228:231], v253 offset:5120\n"
    "v_mfma_f32_16x16x32_bf16 a[96:99], v[160:163], v[56:59], a[96:99]\n"
    "v_exp_f32 v79, v79\n"
    "v_mfma_f32_16x16x32_bf16 a[4:7], v[164:167], v[32:35], a[4:7]\n"
    "v_exp_f32 v80, v80\n"
    "v_mfma_f32_16x16x32_bf16 a[36:39], v[164:167], v[40:43], a[36:39]\n"
    "v_cvt_pk_bf16_f32 v74, v76, v77\n"
    "v_cvt_pk_bf16_f32 v75, v78, v79\n"
    "v_mfma_f32_16x16x32_bf16 a[68:71], v[164:167], v[48:51], a[68:71]\n"
    "v_exp_f32 v81, v81\n"
    "v_mfma_f32_16x16x32_bf16 a[100:103], v[164:167], v[56:59], a[100:103]\n"
    "v_exp_f32 v82, v82\n"
    "v_mfma_f32_16x16x32_bf16 a[8:11], v[168:171], v[32:35], a[8:11]\n"
    "ds_read_b128 a[232:235], v253 offset:9216\n"
    "ds_read_b128 a[236:239], v253 offset:13312\n"
    "v_mfma_f32_16x16x32_bf16 a[40:43], v[168:171], v[40:43], a[40:43]\n"
    "v_exp_f32 v83, v83\n"
    "v_mfma_f32_16x16x32_bf16 a[72:75], v[168:171], v[48:51], a[72:75]\n"
    "v_exp_f32 v84, v84\n"
    "v_mfma_f32_16x16x32_bf16 a[104:107], v[168:171], v[56:59], a[104:107]\n"
    "v_cvt_pk_bf16_f32 v80, v80, v81\n"
    "v_cvt_pk_bf16_f32 v81, v82, v83\n"
    "v_mfma_f32_16x16x32_bf16 a[12:15], v[172:175], v[32:35], a[12:15]\n"
    "v_exp_f32 v85, v85\n"
    "v_mfma_f32_16x16x32_bf16 a[44:47], v[172:175], v[40:43], a[44:47]\n"
    "ds_read_b128 a[240:243], v253 offset:1536\n"
    "ds_read_b128 a[244:247], v253 offset:5632\n"
    "v_mfma_f32_16x16x32_bf16 a[76:79], v[172:175], v[48:51], a[76:79]\n"
    "v_exp_f32 v86, v86\n"
    "v_mfma_f32_16x16x32_bf16 a[108:111], v[172:175], v[56:59], a[108:111]\n"
    "v_exp_f32 v87, v87\n"
    "v_mfma_f32_16x16x32_bf16 a[16:19], v[176:179], v[32:35], a[16:19]\n"
    "v_exp_f32 v88, v88\n"
    "v_mfma_f32_16x16x32_bf16 a[48:51], v[176:179], v[40:43], a[48:51]\n"
    "v_cvt_pk_bf16_f32 v82, v84, v85\n"
    "v_cvt_pk_bf16_f32 v83, v86, v87\n"
    "v_mfma_f32_16x16x32_bf16 a[80:83], v[176:179], v[48:51], a[80:83]\n"
    "ds_read_b128 a[248:251], v253 offset:9728\n"
    "ds_read_b128 a[252:255], v253 offset:13824\n"
    "v_mfma_f32_16x16x32_bf16 a[112:115], v[176:179], v[56:59], a[112:115]\n"
    "v_exp_f32 v89, v89\n"
    "v_mfma_f32_16x16x32_bf16 a[20:23], v[180:183], v[32:35], a[20:23]\n"
    "v_exp_f32 v90, v90\n"
    "v_mfma_f32_16x16x32_bf16 a[52:55], v[180:183], v[40:43], a[52:55]\n"
    "v_exp_f32 v91, v91\n"
    "v_mfma_f32_16x16x32_bf16 a[84:87], v[180:183], v[48:51], a[84:87]\n"
    "v_exp_f32 v92, v92\n"
    "v_mfma_f32_16x16x32_bf16 a[116:119], v[180:183], v[56:59], a[116:119]\n"
    "v_exp_f32 v93, v93\n"
    "v_mfma_f32_16x16x32_bf16 a[24:27], v[184:187], v[32:35], a[24:27]\n"
    "v_cvt_pk_bf16_f32 v88, v88, v89\n"
    "v_cvt_pk_bf16_f32 v89, v90, v91\n"
    "v_mfma_f32_16x16x32_bf16 a[56:59], v[184:187], v[40:43], a[56:59]\n"
    "v_exp_f32 v94, v94\n"
    "v_mfma_f32_16x16x32_bf16 a[88:91], v[184:187], v[48:51], a[88:91]\n"
    "v_exp_f32 v95, v95\n"
    "v_mfma_f32_16x16x32_bf16 a[120:123], v[184:187], v[56:59], a[120:123]\n"
    "v_exp_f32 v96, v96\n"
    "v_mfma_f32_16x16x32_bf16 a[28:31], v[188:191], v[32:35], a[28:31]\n"
    "v_exp_f32 v97, v97\n"
    "v_mfma_f32_16x16x32_bf16 a[60:63], v[188:191], v[40:43], a[60:63]\n"
    "v_exp_f32 v98, v98\n"
    "v_mfma_f32_16x16x32_bf16 a[92:95], v[188:191], v[48:51], a[92:95]\n"
    "v_cvt_pk_bf16_f32 v90, v92, v93\n"
    "v_cvt_pk_bf16_f32 v91, v94, v95\n"
    "v_mfma_f32_16x16x32_bf16 a[124:127], v[188:191], v[56:59], a[124:127]\n"
    "v_exp_f32 v99, v99\n"
    "v_mfma_f32_16x16x32_bf16 v[208:211], v[232:235], v[32:35], v[208:211]\n"
    "v_exp_f32 v100, v100\n"
    "v_mfma_f32_16x16x32_bf16 v[212:215], v[232:235], v[40:43], v[212:215]\n"
    "v_exp_f32 v101, v101\n"
    "v_mfma_f32_16x16x32_bf16 v[216:219], v[232:235], v[48:51], v[216:219]\n"
    "v_exp_f32 v102, v102\n"
    "v_exp_f32 v103, v103\n"
    "v_mfma_f32_16x16x32_bf16 v[220:223], v[232:235], v[56:59], v[220:223]\n"
    "s_waitcnt lgkmcnt(0)\n"
    "s_swappc_b64 s[86:87], s[94:95]\n"
    "LPS%=:\n"
    "v_mfma_f32_16x16x32_bf16 v[0:3], a[192:195], a[128:131], v[192:195]\n"
    "v_exp_f32 v104, v104\n"
    "v_mfma_f32_16x16x32_bf16 v[8:11], a[192:195], a[144:147], v[196:199]\n"
    "ds_read_b64_tr_b16 v[128:129], v250 offset:0\n"
    "ds_read_b64_tr_b16 v[130:131], v250 offset:4096\n"
    "v_mfma_f32_16x16x32_bf16 v[16:19], a[192:195], a[160:163], v[200:203]\n"
    "v_cvt_pk_bf16_f32 v96, v96, v97\n"
    "v_cvt_pk_bf16_f32 v97, v98, v99\n"
    "v_mfma_f32_16x16x32_bf16 v[24:27], a[192:195], a[176:179], v[204:207]\n"
    "v_exp_f32 v105, v105\n"
    "v_mfma_f32_16x16x32_bf16 v[4:7], a[196:199], a[128:131], v[192:195]\n"
    "ds_read_b64_tr_b16 v[132:133], v251 offset:0\n"
    "ds_read_b64_tr_b16 v[134:135], v251 offset:4096\n"
    "v_mfma_f32_16x16x32_bf16 v[12:15], a[196:199], a[144:147], v[196:199]\n"
    "v_exp_f32 v106, v106\n"
    "v_mfma_f32_16x16x32_bf16 v[20:23], a[196:199], a[160:163], v[200:203]\n"
    "v_mfma_f32_16x16x32_bf16 v[28:31], a[196:199], a[176:179], v[204:207]\n"
    "ds_read_b64_tr_b16 v[136:137], v250 offset:512\n"
    "ds_read_b64_tr_b16 v[138:139], v250 offset:4608\n"
    "v_mfma_f32_16x16x32_bf16 v[32:35], a[200:203], a[128:131], v[192:195]\n"
    "v_cvt_pk_bf16_f32 v98, v100, v101\n"
    "v_cvt_pk_bf16_f32 v99, v102, v103\n"
    "v_mfma_f32_16x16x32_bf16 v[40:43], a[200:203], a[144:147], v[196:199]\n"
    "v_exp_f32 v107, v107\n"
    "v_mfma_f32_16x16x32_bf16 v[48:51], a[200:203], a[160:163], v[200:203]\n"
    "ds_read_b64_tr_b16 v[140:141], v251 offset:512\n"
    "ds_read_b64_tr_b16 v[142:143], v251 offset:4608\n"
    "v_mfma_f32_16x16x32_bf16 v[56:59], a[200:203], a[176:179], v[204:207]\n"
    "v_exp_f32 v108, v108\n"
    "v_mfma_f32_16x16x32_bf16 v[36:39], a[204:207], a[128:131], v[192:195]\n"
    "v_mfma_f32_16x16x32_bf16 v[44:47], a[204:207], a[144:147], v[196:199]\n"
    "ds_read_b64_tr_b16 v[144:145], v250 offset:1024\n"
    "ds_read_b64_tr_b16 v[146:147], v250 offset:5120\n"
    "v_mfma_f32_16x16x32_bf16 v[52:55], a[204:207], a[160:163], v[200:203]\n"
    "v_exp_f32 v109, v109\n"
    "v_mfma_f32_16x16x32_bf16 v[60:63], a[204:207], a[176:179], v[204:207]\n"
    "v_cvt_pk_bf16_f32 v104, v104, v105\n"
    "v_cvt_pk_bf16_f32 v105, v106, v107\n"
    "v_mfma_f32_16x16x32_bf16 v[0:3], a[208:211], a[132:135], v[0:3]\n"
    "ds_read_b64_tr_b16 v[148:149], v251 offset:1024\n"
    "ds_read_b64_tr_b16 v[150:151], v251 offset:5120\n"
    "v_mfma_f32_16x16x32_bf16 v[8:11], a[208:211], a[148:151], v[8:11]\n"
    "ds_read_b128 a[192:195], v255 offset:0\n"
    "ds_read_b128 a[196:199], v255 offset:1024\n"
    "v_mfma_f32_16x16x32_bf16 v[16:19], a[208:211], a[164:167], v[16:19]\n"
    "v_exp_f32 v110, v110\n"
    "v_mfma_f32_16x16x32_bf16 v[24:27], a[208:211], a[180:183], v[24:27]\n"
    "ds_read_b64_tr_b16 v[152:153], v250 offset:1536\n"
    "ds_read_b64_tr_b16 v[154:155], v250 offset:5632\n"
    "v_mfma_f32_16x16x32_bf16 v[4:7], a[212:215], a[132:135], v[4:7]\n"
    "v_mfma_f32_16x16x32_bf16 v[12:15], a[212:215], a[148:151], v[12:15]\n"
    "v_exp_f32 v111, v111\n"
    "v_mfma_f32_16x16x32_bf16 v[20:23], a[212:215], a[164:167], v[20:23]\n"
    "ds_read_b64_tr_b16 v[156:157], v251 offset:1536\n"
    "ds_read_b64_tr_b16 v[158:159], v251 offset:5632\n"
    "v_mfma_f32_16x16x32_bf16 v[28:31], a[212:215], a[180:183], v[28:31]\n"
    "v_exp_f32 v112, v112\n"
    "v_mfma_f32_16x16x32_bf16 v[32:35], a[216:219], a[132:135], v[32:35]\n"
    "v_cvt_pk_bf16_f32 v106, v108, v109\n"
    "v_cvt_pk_bf16_f32 v107, v110, v111\n"
    "v_mfma_f32_16x16x32_bf16 v[40:43], a[216:219], a[148:151], v[40:43]\n"
    "v_mfma_f32_16x16x32_bf16 v[48:51], a[216:219], a[164:167], v[48:51]\n"
    "ds_read_b64_tr_b16 v[160:161], v250 offset:8192\n"
    "ds_read_b64_tr_b16 v[162:163], v250 offset:12288\n"
    "v_mfma_f32_16x16x32_bf16 v[56:59], a[216:219], a[180:183], v[56:59]\n"
    "v_exp_f32 v113, v113\n"
    "v_mfma_f32_16x16x32_bf16 v[36:39], a[220:223], a[132:135], v[36:39]\n"
    "v_exp_f32 v114, v114\n"
    "v_mfma_f32_16x16x32_bf16 v[44:47], a[220:223], a[148:151], v[44:47]\n"
    "ds_read_b64_tr_b16 v[164:165], v251 offset:8192\n"
    "ds_read_b64_tr_b16 v[166:167], v251 offset:12288\n"
    "v_mfma_f32_16x16x32_bf16 v[52:55], a[220:223], a[164:167], v[52:55]\n"
    "v_exp_f32 v115, v115\n"
    "v_mfma_f32_16x16x32_bf16 v[60:63], a[220:223], a[180:183], v[60:63]\n"
    "v_mfma_f32_16x16x32_bf16 v[0:3], a[224:227], a[136:139], v[0:3]\n"
    "ds_read_b64_tr_b16 v[168:169], v250 offset:8704\n"
    "ds_read_b64_tr_b16 v[170:171], v250 offset:12800\n"
    "v_mfma_f32_16x16x32_bf16 v[8:11], a[224:227], a[152:155], v[8:11]\n"
    "v_exp_f32 v116, v116\n"
    "v_mfma_f32_16x16x32_bf16 v[16:19], a[224:227], a[168:171], v[16:19]\n"
    "v_cvt_pk_bf16_f32 v112, v112, v113\n"
    "v_cvt_pk_bf16_f32 v113, v114, v115\n"
    "v_mfma_f32_16x16x32_bf16 v[24:27], a[224:227], a[184:187], v[24:27]\n"
    "ds_read_b64_tr_b16 v[172:173], v251 offset:8704\n"
    "ds_read_b64_tr_b16 v[174:175], v251 offset:12800\n"
    "v_mfma_f32_16x16x32_bf16 v[4:7], a[228:231], a[136:139], v[4:7]\n"
    "s_waitcnt lgkmcnt(12)\n"
    "buffer_store_dwordx4 a[192:195], v229, s[52:55], s82 offen offset:0 sc0 sc1\n"
    "v_mfma_f32_16x16x32_bf16 v[12:15], a[228:231], a[152:155], v[12:15]\n"
    "v_exp_f32 v117, v117\n"
    "v_mfma_f32_16x16x32_bf16 v[20:23], a[228:231], a[168:171], v[20:23]\n"
    "ds_read_b64_tr_b16 v[176:177], v250 offset:9216\n"
    "ds_read_b64_tr_b16 v[178:179], v250 offset:13312\n"
    "v_mfma_f32_16x16x32_bf16 v[28:31], a[228:231], a[184:187], v[28:31]\n"
    "v_mfma_f32_16x16x32_bf16 v[32:35], a[232:235], a[136:139], v[32:35]\n"
    "v_exp_f32 v118, v118\n"
    "v_mfma_f32_16x16x32_bf16 v[40:43], a[232:235], a[152:155], v[40:43]\n"
    "ds_read_b64_tr_b16 v[180:181], v251 offset:9216\n"
    "ds_read_b64_tr_b16 v[182:183], v251 offset:13312\n"
    "v_mfma_f32_16x16x32_bf16 v[48:51], a[232:235], a[168:171], v[48:51]\n"
    "v_exp_f32 v119, v119\n"
    "v_mfma_f32_16x16x32_bf16 v[56:59], a[232:235], a[184:187], v[56:59]\n"
    "v_exp_f32 v120, v120\n"
    "v_mfma_f32_16x16x32_bf16 v[36:39], a[236:239], a[136:139], v[36:39]\n"
    "ds_read_b64_tr_b16 v[184:185], v250 offset:9728\n"
    "ds_read_b64_tr_b16 v[186:187], v250 offset:13824\n"
    "v_mfma_f32_16x16x32_bf16 v[44:47], a[236:239], a[152:155], v[44:47]\n"
    "v_mfma_f32_16x16x32_bf16 v[52:55], a[236:239], a[168:171], v[52:55]\n"
    "v_cvt_pk_bf16_f32 v114, v116, v117\n"
    "v_cvt_pk_bf16_f32 v115, v118, v119\n"
    "v_mfma_f32_16x16x32_bf16 v[60:63], a[236:239], a[184:187], v[60:63]\n"
    "ds_read_b64_tr_b16 v[188:189], v251 offset:9728\n"
    "ds_read_b64_tr_b16 v[190:191], v251 offset:13824\n"
    "v_mfma_f32_16x16x32_bf16 v[0:3], a[240:243], a[140:143], v[0:3]\n"
    "buffer_store_dwordx4 a[196:199], v229, s[52:55], s82 offen offset:1024 sc0 sc1\n"
    "v_mfma_f32_16x16x32_bf16 v[8:11], a[240:243], a[156:159], v[8:11]\n"
    "v_exp_f32 v121, v121\n"
    "v_mfma_f32_16x16x32_bf16 v[16:19], a[240:243], a[172:175], v[16:19]\n"
    "v_exp_f32 v122, v122\n"
    "v_mfma_f32_16x16x32_bf16 v[24:27], a[240:243], a[188:191], v[24:27]\n"
    "v_mfma_f32_16x16x32_bf16 v[4:7], a[244:247], a[140:143], v[4:7]\n"
    "v_exp_f32 v123, v123\n"
    "v_mfma_f32_16x16x32_bf16 v[12:15], a[244:247], a[156:159], v[12:15]\n"
    "v_exp_f32 v124, v124\n"
    "v_mfma_f32_16x16x32_bf16 v[20:23], a[244:247], a[172:175], v[20:23]\n"
    "v_cvt_pk_bf16_f32 v120, v120, v121\n"
    "v_cvt_pk_bf16_f32 v121, v122, v123\n"
    "v_mfma_f32_16x16x32_bf16 v[28:31], a[244:247], a[188:191], v[28:31]\n"
    "v_mfma_f32_16x16x32_bf16 v[32:35], a[248:251], a[140:143], v[32:35]\n"
    "v_exp_f32 v125, v125\n"
    "v_mfma_f32_16x16x32_bf16 v[40:43], a[248:251], a[156:159], v[40:43]\n"
    "v_exp_f32 v126, v126\n"
    "v_mfma_f32_16x16x32_bf16 v[48:51], a[248:251], a[172:175], v[48:51]\n"
    "v_exp_f32 v127, v127\n"
    "v_mfma_f32_16x16x32_bf16 v[56:59], a[248:251], a[188:191], v[56:59]\n"
    "v_mfma_f32_16x16x32_bf16 v[36:39], a[252:255], a[140:143], v[36:39]\n"
    "s_nop 0\n"
    "v_mfma_f32_16x16x32_bf16 v[44:47], a[252:255], a[156:159], v[44:47]\n"
    "v_cvt_pk_bf16_f32 v122, v124, v125\n"
    "v_cvt_pk_bf16_f32 v123, v126, v127\n"
    "v_mfma_f32_16x16x32_bf16 v[52:55], a[252:255], a[172:175], v[52:55]\n"
    "v_mfma_f32_16x16x32_bf16 v[60:63], a[252:255], a[188:191], v[60:63]\n"
    "s_waitcnt vmcnt(10) lgkmcnt(0)\n"
    "s_barrier\n"
    "v_mfma_f32_16x16x32_bf16 a[0:3], v[128:131], v[64:67], a[0:3]\n"
    "v_mfma_f32_16x16x32_bf16 a[32:35], v[128:131], v[72:75], a[32:35]\n"
    "s_add_u32 m0, s72, s99\n"
    "v_mfma_f32_16x16x32_bf16 a[64:67], v[128:131], v[80:83], a[64:67]\n"
    "buffer_load_dwordx4 v224, s[36:39], s70 offen offset:0 nt lds\n"
    "v_mfma_f32_16x16x32_bf16 a[96:99], v[128:131], v[88:91], a[96:99]\n"
    "v_mfma_f32_16x16x32_bf16 a[4:7], v[132:135], v[64:67], a[4:7]\n"
    "buffer_load_dwordx4 v225, s[36:39], s70 offen offset:1024 nt lds\n"
    "s_swappc_b64 s[86:87], s[90:91]\n"
    "v_mfma_f32_16x16x32_bf16 a[36:39], v[132:135], v[72:75], a[36:39]\n"
    "v_exp_f32 v0, v0\n"
    "v_mfma_f32_16x16x32_bf16 a[68:71], v[132:135], v[80:83], a[68:71]\n"
    "buffer_load_dwordx4 v226, s[36:39], s70 offen offset:2048 nt lds\n"
    "v_mfma_f32_16x16x32_bf16 a[100:103], v[132:135], v[88:91], a[100:103]\n"
    "v_exp_f32 v1, v1\n"
    "v_mfma_f32_16x16x32_bf16 a[8:11], v[136:139], v[64:67], a[8:11]\n"
    "buffer_load_dwordx4 v227, s[36:39], s70 offen offset:3072 nt lds\n"
    "v_mfma_f32_16x16x32_bf16 a[40:43], v[136:139], v[72:75], a[40:43]\n"
    "s_add_u32 m0, s73, s83\n"
    "s_add_u32 s70, s70, 16384\n"
    "v_mfma_f32_16x16x32_bf16 a[72:75], v[136:139], v[80:83], a[72:75]\n"
    "buffer_load_dwordx4 v224, s[40:43], s71 offen offset:0 nt lds\n"
    "v_mfma_f32_16x16x32_bf16 a[104:107], v[136:139], v[88:91], a[104:107]\n"
    "v_add_u32 v250, s99, v230\n"
    "v_add_u32 v251, s99, v231\n"
    "v_mfma_f32_16x16x32_bf16 a[12:15], v[140:143], v[64:67], a[12:15]\n"
    "buffer_load_dwordx4 v225, s[40:43], s71 offen offset:1024 nt lds\n"
    "v_mfma_f32_16x16x32_bf16 a[44:47], v[140:143], v[72:75], a[44:47]\n"
    "s_mov_b32 s83, s99\n"
    "s_add_u32 s99, s99, 16384\n"
    "v_mfma_f32_16x16x32_bf16 a[76:79], v[140:143], v[80:83], a[76:79]\n"
    "buffer_load_dwordx4 v226, s[40:43], s71 offen offset:2048 nt lds\n"
    "v_mfma_f32_16x16x32_bf16 a[108:111], v[140:143], v[88:91], a[108:111]\n"
    "s_cmp_eq_u32 s99, 49152\n"
    "s_cselect_b32 s99, 0, s99\n"
    "v_mfma_f32_16x16x32_bf16 a[16:19], v[144:147], v[64:67], a[16:19]\n"
    "buffer_load_dwordx4 v227, s[40:43], s71 offen offset:3072 nt lds\n"
    "v_mfma_f32_16x16x32_bf16 a[48:51], v[144:147], v[72:75], a[48:51]\n"
    "s_add_u32 s71, s71, 16384\n"
    "v_add_u32 v253, s99, v228\n"
    "v_mfma_f32_16x16x32_bf16 a[80:83], v[144:147], v[80:83], a[80:83]\n"
    "ds_read_b128 a[192:195], v253 offset:0\n"
    "ds_read_b128 a[196:199], v253 offset:4096\n"
    "v_mfma_f32_16x16x32_bf16 a[112:115], v[144:147], v[88:91], a[112:115]\n"
    "v_exp_f32 v2, v2\n"
    "v_mfma_f32_16x16x32_bf16 a[20:23], v[148:151], v[64:67], a[20:23]\n"
    "v_exp_f32 v3, v3\n"
    "v_mfma_f32_16x16x32_bf16 a[52:55], v[148:151], v[72:75], a[52:55]\n"
    "v_exp_f32 v4, v4\n"
    "v_mfma_f32_16x16x32_bf16 a[84:87], v[148:151], v[80:83], a[84:87]\n"
    "v_cvt_pk_bf16_f32 v0, v0, v1\n"
    "v_cvt_pk_bf16_f32 v1, v2, v3\n"
    "v_mfma_f32_16x16x32_bf16 a[116:119], v[148:151], v[88:91], a[116:119]\n"
    "ds_read_b128 a[200:203], v253 offset:8192\n"
    "ds_read_b128 a[204:207], v253 offset:12288\n"
    "v_mfma_f32_16x16x32_bf16 a[24:27], v[152:155], v[64:67], a[24:27]\n"
    "v_exp_f32 v5, v5\n"
    "v_mfma_f32_16x16x32_bf16 a[56:59], v[152:155], v[72:75], a[56:59]\n"
    "v_exp_f32 v6, v6\n"
    "v_mfma_f32_16x16x32_bf16 a[88:91], v[152:155], v[80:83], a[88:91]\n"
    "v_exp_f32 v7, v7\n"
    "v_mfma_f32_16x16x32_bf16 a[120:123], v[152:155], v[88:91], a[120:123]\n"
    "v_exp_f32 v8, v8\n"
    "v_mfma_f32_16x16x32_bf16 a[28:31], v[156:159], v[64:67], a[28:31]\n"
    "ds_read_b128 a[208:211], v253 offset:512\n"
    "ds_read_b128 a[212:215], v253 offset:4608\n"
    "v_mfma_f32_16x16x32_bf16 a[60:63], v[156:159], v[72:75], a[60:63]\n"
    "v_cvt_pk_bf16_f32 v2, v4, v5\n"
    "v_cvt_pk_bf16_f32 v3, v6, v7\n"
    "v_mfma_f32_16x16x32_bf16 a[92:95], v[156:159], v[80:83], a[92:95]\n"
    "v_exp_f32 v9, v9\n"
    "v_mfma_f32_16x16x32_bf16 a[124:127], v[156:159], v[88:91], a[124:127]\n"
    "v_exp_f32 v10, v10\n"
    "v_mfma_f32_16x16x32_bf16 v[208:211], v[232:235], v[64:67], v[208:211]\n"
    "v_exp_f32 v11, v11\n"
    "v_mfma_f32_16x16x32_bf16 v[212:215], v[232:235], v[72:75], v[212:215]\n"
    "ds_read_b128 a[216:219], v253 offset:8704\n"
    "ds_read_b128 a[220:223], v253 offset:12800\n"
    "v_mfma_f32_16x16x32_bf16 v[216:219], v[232:235], v[80:83], v[216:219]\n"
    "v_exp_f32 v12, v12\n"
    "v_mfma_f32_16x16x32_bf16 v[220:223], v[232:235], v[88:91], v[220:223]\n"
    "v_cvt_pk_bf16_f32 v8, v8, v9\n"
    "v_cvt_pk_bf16_f32 v9, v10, v11\n"
    "v_mfma_f32_16x16x32_bf16 a[0:3], v[160:163], v[96:99], a[0:3]\n"
    "v_exp_f32 v13, v13\n"
    "v_mfma_f32_16x16x32_bf16 a[32:35], v[160:163], v[104:107], a[32:35]\n"
    "v_exp_f32 v14, v14\n"
    "v_mfma_f32_16x16x32_bf16 a[64:67], v[160:163], v[112:115], a[64:67]\n"
    "ds_read_b128 a[224:227], v253 offset:1024\n"
    "ds_read_b128 a[228:231], v253 offset:5120\n"
    "v_mfma_f32_16x16x32_bf16 a[96:99], v[160:163], v[120:123], a[96:99]\n"
    "v_exp_f32 v15, v15\n"
    "v_mfma_f32_16x16x32_bf16 a[4:7], v[164:167], v[96:99], a[4:7]\n"
    "v_exp_f32 v16, v16\n"
    "v_mfma_f32_16x16x32_bf16 a[36:39], v[164:167], v[104:107], a[36:39]\n"
    "v_cvt_pk_bf16_f32 v10, v12, v13\n"
    "v_cvt_pk_bf16_f32 v11, v14, v15\n"
    "v_mfma_f32_16x16x32_bf16 a[68:71], v[164:167], v[112:115], a[68:71]\n"
    "v_exp_f32 v17, v17\n"
    "v_mfma_f32_16x16x32_bf16 a[100:103], v[164:167], v[120:123], a[100:103]\n"
    "v_exp_f32 v18, v18\n"
    "v_mfma_f32_16x16x32_bf16 a[8:11], v[168:171], v[96:99], a[8:11]\n"
    "ds_read_b128 a[232:235], v253 offset:9216\n"
    "ds_read_b128 a[236:239], v253 offset:13312\n"
    "v_mfma_f32_16x16x32_bf16 a[40:43], v[168:171], v[104:107], a[40:43]\n"
    "v_exp_f32 v19, v19\n"
    "v_mfma_f32_16x16x32_bf16 a[72:75], v[168:171], v[112:115], a[72:75]\n"
    "v_exp_f32 v20, v20\n"
    "v_mfma_f32_16x16x32_bf16 a[104:107], v[168:171], v[120:123], a[104:107]\n"
    "v_cvt_pk_bf16_f32 v16, v16, v17\n"
    "v_cvt_pk_bf16_f32 v17, v18, v19\n"
    "v_mfma_f32_16x16x32_bf16 a[12:15], v[172:175], v[96:99], a[12:15]\n"
    "v_exp_f32 v21, v21\n"
    "v_mfma_f32_16x16x32_bf16 a[44:47], v[172:175], v[104:107], a[44:47]\n"
    "ds_read_b128 a[240:243], v253 offset:1536\n"
    "ds_read_b128 a[244:247], v253 offset:5632\n"
    "v_mfma_f32_16x16x32_bf16 a[76:79], v[172:175], v[112:115], a[76:79]\n"
    "v_exp_f32 v22, v22\n"
    "v_mfma_f32_16x16x32_bf16 a[108:111], v[172:175], v[120:123], a[108:111]\n"
    "v_exp_f32 v23, v23\n"
    "v_mfma_f32_16x16x32_bf16 a[16:19], v[176:179], v[96:99], a[16:19]\n"
    "v_exp_f32 v24, v24\n"
    "v_mfma_f32_16x16x32_bf16 a[48:51], v[176:179], v[104:107], a[48:51]\n"
    "v_cvt_pk_bf16_f32 v18, v20, v21\n"
    "v_cvt_pk_bf16_f32 v19, v22, v23\n"
    "v_mfma_f32_16x16x32_bf16 a[80:83], v[176:179], v[112:115], a[80:83]\n"
    "ds_read_b128 a[248:251], v253 offset:9728\n"
    "ds_read_b128 a[252:255], v253 offset:13824\n"
    "v_mfma_f32_16x16x32_bf16 a[112:115], v[176:179], v[120:123], a[112:115]\n"
    "v_exp_f32 v25, v25\n"
    "v_mfma_f32_16x16x32_bf16 a[20:23], v[180:183], v[96:99], a[20:23]\n"
    "v_exp_f32 v26, v26\n"
    "v_mfma_f32_16x16x32_bf16 a[52:55], v[180:183], v[104:107], a[52:55]\n"
    "v_exp_f32 v27, v27\n"
    "v_mfma_f32_16x16x32_bf16 a[84:87], v[180:183], v[112:115], a[84:87]\n"
    "v_exp_f32 v28, v28\n"
    "v_mfma_f32_16x16x32_bf16 a[116:119], v[180:183], v[120:123], a[116:119]\n"
    "v_exp_f32 v29, v29\n"
    "v_mfma_f32_16x16x32_bf16 a[24:27], v[184:187], v[96:99], a[24:27]\n"
    "v_cvt_pk_bf16_f32 v24, v24, v25\n"
    "v_cvt_pk_bf16_f32 v25, v26, v27\n"
    "v_mfma_f32_16x16x32_bf16 a[56:59], v[184:187], v[104:107], a[56:59]\n"
    "v_exp_f32 v30, v30\n"
    "v_mfma_f32_16x16x32_bf16 a[88:91], v[184:187], v[112:115], a[88:91]\n"
    "v_exp_f32 v31, v31\n"
    "v_mfma_f32_16x16x32_bf16 a[120:123], v[184:187], v[120:123], a[120:123]\n"
    "v_exp_f32 v32, v32\n"
    "v_mfma_f32_16x16x32_bf16 a[28:31], v[188:191], v[96:99], a[28:31]\n"
    "v_exp_f32 v33, v33\n"
    "v_mfma_f32_16x16x32_bf16 a[60:63], v[188:191], v[104:107], a[60:63]\n"
    "v_exp_f32 v34, v34\n"
    "v_mfma_f32_16x16x32_bf16 a[92:95], v[188:191], v[112:115], a[92:95]\n"
    "v_cvt_pk_bf16_f32 v26, v28, v29\n"
    "v_cvt_pk_bf16_f32 v27, v30, v31\n"
    "v_mfma_f32_16x16x32_bf16 a[124:127], v[188:191], v[120:123], a[124:127]\n"
    "v_exp_f32 v35, v35\n"
    "v_mfma_f32_16x16x32_bf16 v[208:211], v[232:235], v[96:99], v[208:211]\n"
    "v_exp_f32 v36, v36\n"
    "v_mfma_f32_16x16x32_bf16 v[212:215], v[232:235], v[104:107], v[212:215]\n"
    "v_exp_f32 v37, v37\n"
    "v_mfma_f32_16x16x32_bf16 v[216:219], v[232:235], v[112:115], v[216:219]\n"
    "v_exp_f32 v38, v38\n"
    "v_exp_f32 v39, v39\n"
    "v_mfma_f32_16x16x32_bf16 v[220:223], v[232:235], v[120:123], v[220:223]\n"
    "s_waitcnt lgkmcnt(0)\n"
    "s_swappc_b64 s[86:87], s[94:95]\n"
    "v_mfma_f32_16x16x32_bf16 v[64:67], a[192:195], a[128:131], v[192:195]\n"
    "v_exp_f32 v40, v40\n"
    "v_mfma_f32_16x16x32_bf16 v[72:75], a[192:195], a[144:147], v[196:199]\n"
    "ds_read_b64_tr_b16 v[128:129], v250 offset:0\n"
    "ds_read_b64_tr_b16 v[130:131], v250 offset:4096\n"
    "v_mfma_f32_16x16x32_bf16 v[80:83], a[192:195], a[160:163], v[200:203]\n"
    "v_cvt_pk_bf16_f32 v32, v32, v33\n"
    "v_cvt_pk_bf16_f32 v33, v34, v35\n"
    "v_mfma_f32_16x16x32_bf16 v[88:91], a[192:195], a[176:179], v[204:207]\n"
    "v_exp_f32 v41, v41\n"
    "v_mfma_f32_16x16x32_bf16 v[68:71], a[196:199], a[128:131], v[192:195]\n"
    "ds_read_b64_tr_b16 v[132:133], v251 offset:0\n"
    "ds_read_b64_tr_b16 v[134:135], v251 offset:4096\n"
    "v_mfma_f32_16x16x32_bf16 v[76:79], a[196:199], a[144:147], v[196:199]\n"
    "v_exp_f32 v42, v42\n"
    "v_mfma_f32_16x16x32_bf16 v[84:87], a[196:199], a[160:163], v[200:203]\n"
    "v_mfma_f32_16x16x32_bf16 v[92:95], a[196:199], a[176:179], v[204:207]\n"
    "ds_read_b64_tr_b16 v[136:137], v250 offset:512\n"
    "ds_read_b64_tr_b16 v[138:139], v250 offset:4608\n"
    "v_mfma_f32_16x16x32_bf16 v[96:99], a[200:203], a[128:131], v[192:195]\n"
    "v_cvt_pk_bf16_f32 v34, v36, v37\n"
    "v_cvt_pk_bf16_f32 v35, v38, v39\n"
    "v_mfma_f32_16x16x32_bf16 v[104:107], a[200:203], a[144:147], v[196:199]\n"
    "v_exp_f32 v43, v43\n"
    "v_mfma_f32_16x16x32_bf16 v[112:115], a[200:203], a[160:163], v[200:203]\n"
    "ds_read_b64_tr_b16 v[140:141], v251 offset:512\n"
    "ds_read_b64_tr_b16 v[142:143], v251 offset:4608\n"
    "v_mfma_f32_16x16x32_bf16 v[120:123], a[200:203], a[176:179], v[204:207]\n"
    "v_exp_f32 v44, v44\n"
    "v_mfma_f32_16x16x32_bf16 v[100:103], a[204:207], a[128:131], v[192:195]\n"
    "v_mfma_f32_16x16x32_bf16 v[108:111], a[204:207], a[144:147], v[196:199]\n"
    "ds_read_b64_tr_b16 v[144:145], v250 offset:1024\n"
    "ds_read_b64_tr_b16 v[146:147], v250 offset:5120\n"
    "v_mfma_f32_16x16x32_bf16 v[116:119], a[204:207], a[160:163], v[200:203]\n"
    "v_exp_f32 v45, v45\n"
    "v_mfma_f32_16x16x32_bf16 v[124:127], a[204:207], a[176:179], v[204:207]\n"
    "v_cvt_pk_bf16_f32 v40, v40, v41\n"
    "v_cvt_pk_bf16_f32 v41, v42, v43\n"
    "v_mfma_f32_16x16x32_bf16 v[64:67], a[208:211], a[132:135], v[64:67]\n"
    "ds_read_b64_tr_b16 v[148:149], v251 offset:1024\n"
    "ds_read_b64_tr_b16 v[150:151], v251 offset:5120\n"
    "v_mfma_f32_16x16x32_bf16 v[72:75], a[208:211], a[148:151], v[72:75]\n"
    "ds_read_b128 a[192:195], v255 offset:2048\n"
    "ds_read_b128 a[196:199], v255 offset:3072\n"
    "v_mfma_f32_16x16x32_bf16 v[80:83], a[208:211], a[164:167], v[80:83]\n"
    "v_exp_f32 v46, v46\n"
    "v_mfma_f32_16x16x32_bf16 v[88:91], a[208:211], a[180:183], v[88:91]\n"
    "ds_read_b64_tr_b16 v[152:153], v250 offset:1536\n"
    "ds_read_b64_tr_b16 v[154:155], v250 offset:5632\n"
    "v_mfma_f32_16x16x32_bf16 v[68:71], a[212:215], a[132:135], v[68:71]\n"
    "v_mfma_f32_16x16x32_bf16 v[76:79], a[212:215], a[148:151], v[76:79]\n"
    "v_exp_f32 v47, v47\n"
    "v_mfma_f32_16x16x32_bf16 v[84:87], a[212:215], a[164:167], v[84:87]\n"
    "ds_read_b64_tr_b16 v[156:157], v251 offset:1536\n"
    "ds_read_b64_tr_b16 v[158:159], v251 offset:5632\n"
    "v_mfma_f32_16x16x32_bf16 v[92:95], a[212:215], a[180:183], v[92:95]\n"
    "v_exp_f32 v48, v48\n"
    "v_mfma_f32_16x16x32_bf16 v[96:99], a[216:219], a[132:135], v[96:99]\n"
    "v_cvt_pk_bf16_f32 v42, v44, v45\n"
    "v_cvt_pk_bf16_f32 v43, v46, v47\n"
    "v_mfma_f32_16x16x32_bf16 v[104:107], a[216:219], a[148:151], v[104:107]\n"
    "v_mfma_f32_16x16x32_bf16 v[112:115], a[216:219], a[164:167], v[112:115]\n"
    "ds_read_b64_tr_b16 v[160:161], v250 offset:8192\n"
    "ds_read_b64_tr_b16 v[162:163], v250 offset:12288\n"
    "v_mfma_f32_16x16x32_bf16 v[120:123], a[216:219], a[180:183], v[120:123]\n"
    "v_exp_f32 v49, v49\n"
    "v_mfma_f32_16x16x32_bf16 v[100:103], a[220:223], a[132:135], v[100:103]\n"
    "v_exp_f32 v50, v50\n"
    "v_mfma_f32_16x16x32_bf16 v[108:111], a[220:223], a[148:151], v[108:111]\n"
    "ds_read_b64_tr_b16 v[164:165], v251 offset:8192\n"
    "ds_read_b64_tr_b16 v[166:167], v251 offset:12288\n"
    "v_mfma_f32_16x16x32_bf16 v[116:119], a[220:223], a[164:167], v[116:119]\n"
    "v_exp_f32 v51, v51\n"
    "v_mfma_f32_16x16x32_bf16 v[124:127], a[220:223], a[180:183], v[124:127]\n"
    "v_mfma_f32_16x16x32_bf16 v[64:67], a[224:227], a[136:139], v[64:67]\n"
    "ds_read_b64_tr_b16 v[168:169], v250 offset:8704\n"
    "ds_read_b64_tr_b16 v[170:171], v250 offset:12800\n"
    "v_mfma_f32_16x16x32_bf16 v[72:75], a[224:227], a[152:155], v[72:75]\n"
    "v_exp_f32 v52, v52\n"
    "v_mfma_f32_16x16x32_bf16 v[80:83], a[224:227], a[168:171], v[80:83]\n"
    "v_cvt_pk_bf16_f32 v48, v48, v49\n"
    "v_cvt_pk_bf16_f32 v49, v50, v51\n"
    "v_mfma_f32_16x16x32_bf16 v[88:91], a[224:227], a[184:187], v[88:91]\n"
    "ds_read_b64_tr_b16 v[172:173], v251 offset:8704\n"
    "ds_read_b64_tr_b16 v[174:175], v251 offset:12800\n"
    "v_mfma_f32_16x16x32_bf16 v[68:71], a[228:231], a[136:139], v[68:71]\n"
    "s_waitcnt lgkmcnt(12)\n"
    "buffer_store_dwordx4 a[192:195], v229, s[52:55], s82 offen offset:2048 sc0 sc1\n"
    "v_mfma_f32_16x16x32_bf16 v[76:79], a[228:231], a[152:155], v[76:79]\n"
    "v_exp_f32 v53, v53\n"
    "v_mfma_f32_16x16x32_bf16 v[84:87], a[228:231], a[168:171], v[84:87]\n"
    "ds_read_b64_tr_b16 v[176:177], v250 offset:9216\n"
    "ds_read_b64_tr_b16 v[178:179], v250 offset:13312\n"
    "v_mfma_f32_16x16x32_bf16 v[92:95], a[228:231], a[184:187], v[92:95]\n"
    "v_mfma_f32_16x16x32_bf16 v[96:99], a[232:235], a[136:139], v[96:99]\n"
    "v_exp_f32 v54, v54\n"
    "v_mfma_f32_16x16x32_bf16 v[104:107], a[232:235], a[152:155], v[104:107]\n"
    "ds_read_b64_tr_b16 v[180:181], v251 offset:9216\n"
    "ds_read_b64_tr_b16 v[182:183], v251 offset:13312\n"
    "v_mfma_f32_16x16x32_bf16 v[112:115], a[232:235], a[168:171], v[112:115]\n"
    "v_exp_f32 v55, v55\n"
    "v_mfma_f32_16x16x32_bf16 v[120:123], a[232:235], a[184:187], v[120:123]\n"
    "v_exp_f32 v56, v56\n"
    "v_mfma_f32_16x16x32_bf16 v[100:103], a[236:239], a[136:139], v[100:103]\n"
    "ds_read_b64_tr_b16 v[184:185], v250 offset:9728\n"
    "ds_read_b64_tr_b16 v[186:187], v250 offset:13824\n"
    "v_mfma_f32_16x16x32_bf16 v[108:111], a[236:239], a[152:155], v[108:111]\n"
    "v_mfma_f32_16x16x32_bf16 v[116:119], a[236:239], a[168:171], v[116:119]\n"
    "v_cvt_pk_bf16_f32 v50, v52, v53\n"
    "v_cvt_pk_bf16_f32 v51, v54, v55\n"
    "v_mfma_f32_16x16x32_bf16 v[124:127], a[236:239], a[184:187], v[124:127]\n"
    "ds_read_b64_tr_b16 v[188:189], v251 offset:9728\n"
    "ds_read_b64_tr_b16 v[190:191], v251 offset:13824\n"
    "v_mfma_f32_16x16x32_bf16 v[64:67], a[240:243], a[140:143], v[64:67]\n"
    "buffer_store_dwordx4 a[196:199], v229, s[52:55], s82 offen offset:3072 sc0 sc1\n"
    "v_mfma_f32_16x16x32_bf16 v[72:75], a[240:243], a[156:159], v[72:75]\n"
    "v_exp_f32 v57, v57\n"
    "v_mfma_f32_16x16x32_bf16 v[80:83], a[240:243], a[172:175], v[80:83]\n"
    "v_exp_f32 v58, v58\n"
    "v_mfma_f32_16x16x32_bf16 v[88:91], a[240:243], a[188:191], v[88:91]\n"
    "v_mfma_f32_16x16x32_bf16 v[68:71], a[244:247], a[140:143], v[68:71]\n"
    "v_exp_f32 v59, v59\n"
    "v_mfma_f32_16x16x32_bf16 v[76:79], a[244:247], a[156:159], v[76:79]\n"
    "v_exp_f32 v60, v60\n"
    "v_mfma_f32_16x16x32_bf16 v[84:87], a[244:247], a[172:175], v[84:87]\n"
    "v_cvt_pk_bf16_f32 v56, v56, v57\n"
    "v_cvt_pk_bf16_f32 v57, v58, v59\n"
    "v_mfma_f32_16x16x32_bf16 v[92:95], a[244:247], a[188:191], v[92:95]\n"
    "v_mfma_f32_16x16x32_bf16 v[96:99], a[248:251], a[140:143], v[96:99]\n"
    "v_exp_f32 v61, v61\n"
    "v_mfma_f32_16x16x32_bf16 v[104:107], a[248:251], a[156:159], v[104:107]\n"
    "v_exp_f32 v62, v62\n"
    "v_mfma_f32_16x16x32_bf16 v[112:115], a[248:251], a[172:175], v[112:115]\n"
    "v_exp_f32 v63, v63\n"
    "v_mfma_f32_16x16x32_bf16 v[120:123], a[248:251], a[188:191], v[120:123]\n"
    "v_mfma_f32_16x16x32_bf16 v[100:103], a[252:255], a[140:143], v[100:103]\n"
    "s_nop 0\n"
    "v_mfma_f32_16x16x32_bf16 v[108:111], a[252:255], a[156:159], v[108:111]\n"
    "v_cvt_pk_bf16_f32 v58, v60, v61\n"
    "v_cvt_pk_bf16_f32 v59, v62, v63\n"
    "v_mfma_f32_16x16x32_bf16 v[116:119], a[252:255], a[172:175], v[116:119]\n"
    "v_mfma_f32_16x16x32_bf16 v[124:127], a[252:255], a[188:191], v[124:127]\n"
    "s_waitcnt vmcnt(10) lgkmcnt(0)\n"
    "s_barrier\n"
    "v_mfma_f32_16x16x32_bf16 a[0:3], v[128:131], v[0:3], a[0:3]\n"
    "s_cmp_eq_u32 s74, 0x101\n"
    "s_cbranch_scc1 QL1_%=\n"
    "QB1_%=:\n"
    "v_mfma_f32_16x16x32_bf16 a[32:35], v[128:131], v[8:11], a[32:35]\n"
    "s_add_u32 m0, s72, s99\n"
    "v_mfma_f32_16x16x32_bf16 a[64:67], v[128:131], v[16:19], a[64:67]\n"
    "buffer_load_dwordx4 v224, s[36:39], s70 offen offset:0 nt lds\n"
    "v_mfma_f32_16x16x32_bf16 a[96:99], v[128:131], v[24:27], a[96:99]\n"
    "v_mfma_f32_16x16x32_bf16 a[4:7], v[132:135], v[0:3], a[4:7]\n"
    "buffer_load_dwordx4 v225, s[36:39], s70 offen offset:1024 nt lds\n"
    "s_swappc_b64 s[86:87], s[92:93]\n"
    "v_mfma_f32_16x16x32_bf16 a[36:39], v[132:135], v[8:11], a[36:39]\n"
    "v_exp_f32 v64, v64\n"
    "v_mfma_f32_16x16x32_bf16 a[68:71], v[132:135], v[16:19], a[68:71]\n"
    "buffer_load_dwordx4 v226, s[36:39], s70 offen offset:2048 nt lds\n"
    "v_mfma_f32_16x16x32_bf16 a[100:103], v[132:135], v[24:27], a[100:103]\n"
    "v_exp_f32 v65, v65\n"
    "v_mfma_f32_16x16x32_bf16 a[8:11], v[136:139], v[0:3], a[8:11]\n"
    "buffer_load_dwordx4 v227, s[36:39], s70 offen offset:3072 nt lds\n"
    "v_mfma_f32_16x16x32_bf16 a[40:43], v[136:139], v[8:11], a[40:43]\n"
    "s_add_u32 m0, s73, s83\n"
    "s_add_u32 s70, s70, 16384\n"
    "v_mfma_f32_16x16x32_bf16 a[72:75], v[136:139], v[16:19], a[72:75]\n"
    "buffer_load_dwordx4 v224, s[40:43], s71 offen offset:0 nt lds\n"
    "v_mfma_f32_16x16x32_bf16 a[104:107], v[136:139], v[24:27], a[104:107]\n"
    "v_add_u32 v250, s99, v230\n"
    "v_add_u32 v251, s99, v231\n"
    "v_mfma_f32_16x16x32_bf16 a[12:15], v[140:143], v[0:3], a[12:15]\n"
    "buffer_load_dwordx4 v225, s[40:43], s71 offen offset:1024 nt lds\n"
    "v_mfma_f32_16x16x32_bf16 a[44:47], v[140:143], v[8:11], a[44:47]\n"
    "s_mov_b32 s83, s99\n"
    "s_add_u32 s99, s99, 16384\n"
    "v_mfma_f32_16x16x32_bf16 a[76:79], v[140:143], v[16:19], a[76:79]\n"
    "buffer_load_dwordx4 v226, s[40:43], s71 offen offset:2048 nt lds\n"
    "v_mfma_f32_16x16x32_bf16 a[108:111], v[140:143], v[24:27], a[108:111]\n"
    "s_cmp_eq_u32 s99, 49152\n"
    "s_cselect_b32 s99, 0, s99\n"
    "v_mfma_f32_16x16x32_bf16 a[16:19], v[144:147], v[0:3], a[16:19]\n"
    "buffer_load_dwordx4 v227, s[40:43], s71 offen offset:3072 nt lds\n"
    "v_mfma_f32_16x16x32_bf16 a[48:51], v[144:147], v[8:11], a[48:51]\n"
    "s_add_u32 s71, s71, 16384\n"
    "v_add_u32 v253, s99, v228\n"
    "v_mfma_f32_16x16x32_bf16 a[80:83], v[144:147], v[16:19], a[80:83]\n"
    "ds_read_b128 a[192:195], v253 offset:0\n"
    "ds_read_b128 a[196:199], v253 offset:4096\n"
    "v_mfma_f32_16x16x32_bf16 a[112:115], v[144:147], v[24:27], a[112:115]\n"
    "v_exp_f32 v66, v66\n"
    "v_mfma_f32_16x16x32_bf16 a[20:23], v[148:151], v[0:3], a[20:23]\n"
    "v_exp_f32 v67, v67\n"
    "v_mfma_f32_16x16x32_bf16 a[52:55], v[148:151], v[8:11], a[52:55]\n"
    "v_exp_f32 v68, v68\n"
    "v_mfma_f32_16x16x32_bf16 a[84:87], v[148:151], v[16:19], a[84:87]\n"
    "v_cvt_pk_bf16_f32 v64, v64, v65\n"
    "v_cvt_pk_bf16_f32 v65, v66, v67\n"
    "v_mfma_f32_16x16x32_bf16 a[116:119], v[148:151], v[24:27], a[116:119]\n"
    "ds_read_b128 a[200:203], v253 offset:8192\n"
    "ds_read_b128 a[204:207], v253 offset:12288\n"
    "v_mfma_f32_16x16x32_bf16 a[24:27], v[152:155], v[0:3], a[24:27]\n"
    "v_exp_f32 v69, v69\n"
    "v_mfma_f32_16x16x32_bf16 a[56:59], v[152:155], v[8:11], a[56:59]\n"
    "v_exp_f32 v70, v70\n"
    "v_mfma_f32_16x16x32_bf16 a[88:91], v[152:155], v[16:19], a[88:91]\n"
    "v_exp_f32 v71, v71\n"
    "v_mfma_f32_16x16x32_bf16 a[120:123], v[152:155], v[24:27], a[120:123]\n"
    "v_exp_f32 v72, v72\n"
    "v_mfma_f32_16x16x32_bf16 a[28:31], v[156:159], v[0:3], a[28:31]\n"
    "ds_read_b128 a[208:211], v253 offset:512\n"
    "ds_read_b128 a[212:215], v253 offset:4608\n"
    "v_mfma_f32_16x16x32_bf16 a[60:63], v[156:159], v[8:11], a[60:63]\n"
    "v_cvt_pk_bf16_f32 v66, v68, v69\n"
    "v_cvt_pk_bf16_f32 v67, v70, v71\n"
    "v_mfma_f32_16x16x32_bf16 a[92:95], v[156:159], v[16:19], a[92:95]\n"
    "v_exp_f32 v73, v73\n"
    "v_mfma_f32_16x16x32_bf16 a[124:127], v[156:159], v[24:27], a[124:127]\n"
    "v_exp_f32 v74, v74\n"
    "v_mfma_f32_16x16x32_bf16 v[208:211], v[232:235], v[0:3], v[208:211]\n"
    "v_exp_f32 v75, v75\n"
    "v_mfma_f32_16x16x32_bf16 v[212:215], v[232:235], v[8:11], v[212:215]\n"
    "ds_read_b128 a[216:219], v253 offset:8704\n"
    "ds_read_b128 a[220:223], v253 offset:12800\n"
    "v_mfma_f32_16x16x32_bf16 v[216:219], v[232:235], v[16:19], v[216:219]\n"
    "v_exp_f32 v76, v76\n"
    "v_mfma_f32_16x16x32_bf16 v[220:223], v[232:235], v[24:27], v[220:223]\n"
    "v_cvt_pk_bf16_f32 v72, v72, v73\n"
    "v_cvt_pk_bf16_f32 v73, v74, v75\n"
    "v_mfma_f32_16x16x32_bf16 a[0:3], v[160:163], v[32:35], a[0:3]\n"
    "v_exp_f32 v77, v77\n"
    "v_mfma_f32_16x16x32_bf16 a[32:35], v[160:163], v[40:43], a[32:35]\n"
    "v_exp_f32 v78, v78\n"
    "v_mfma_f32_16x16x32_bf16 a[64:67], v[160:163], v[48:51], a[64:67]\n"
    "ds_read_b128 a[224:227], v253 offset:1024\n"
    "ds_read_b128 a[228:231], v253 offset:5120\n"
    "v_mfma_f32_16x16x32_bf16 a[96:99], v[160:163], v[56:59], a[96:99]\n"
    "v_exp_f32 v79, v79\n"
    "v_mfma_f32_16x16x32_bf16 a[4:7], v[164:167], v[32:35], a[4:7]\n"
    "v_exp_f32 v80, v80\n"
    "v_mfma_f32_16x16x32_bf16 a[36:39], v[164:167], v[40:43], a[36:39]\n"
    "v_cvt_pk_bf16_f32 v74, v76, v77\n"
    "v_cvt_pk_bf16_f32 v75, v78, v79\n"
    "v_mfma_f32_16x16x32_bf16 a[68:71], v[164:167], v[48:51], a[68:71]\n"
    "v_exp_f32 v81, v81\n"
    "v_mfma_f32_16x16x32_bf16 a[100:103], v[164:167], v[56:59], a[100:103]\n"
    "v_exp_f32 v82, v82\n"
    "v_mfma_f32_16x16x32_bf16 a[8:11], v[168:171], v[32:35], a[8:11]\n"
    "ds_read_b128 a[232:235], v253 offset:9216\n"
    "ds_read_b128 a[236:239], v253 offset:13312\n"
    "v_mfma_f32_16x16x32_bf16 a[40:43], v[168:171], v[40:43], a[40:43]\n"
    "v_exp_f32 v83, v83\n"
    "v_mfma_f32_16x16x32_bf16 a[72:75], v[168:171], v[48:51], a[72:75]\n"
    "v_exp_f32 v84, v84\n"
    "v_mfma_f32_16x16x32_bf16 a[104:107], v[168:171], v[56:59], a[104:107]\n"
    "v_cvt_pk_bf16_f32 v80, v80, v81\n"
    "v_cvt_pk_bf16_f32 v81, v82, v83\n"
    "v_mfma_f32_16x16x32_bf16 a[12:15], v[172:175], v[32:35], a[12:15]\n"
    "v_exp_f32 v85, v85\n"
    "v_mfma_f32_16x16x32_bf16 a[44:47], v[172:175], v[40:43], a[44:47]\n"
    "ds_read_b128 a[240:243], v253 offset:1536\n"
    "ds_read_b128 a[244:247], v253 offset:5632\n"
    "v_mfma_f32_16x16x32_bf16 a[76:79], v[172:175], v[48:51], a[76:79]\n"
    "v_exp_f32 v86, v86\n"
    "v_mfma_f32_16x16x32_bf16 a[108:111], v[172:175], v[56:59], a[108:111]\n"
    "v_exp_f32 v87, v87\n"
    "v_mfma_f32_16x16x32_bf16 a[16:19], v[176:179], v[32:35], a[16:19]\n"
    "v_exp_f32 v88, v88\n"
    "v_mfma_f32_16x16x32_bf16 a[48:51], v[176:179], v[40:43], a[48:51]\n"
    "v_cvt_pk_bf16_f32 v82, v84, v85\n"
    "v_cvt_pk_bf16_f32 v83, v86, v87\n"
    "v_mfma_f32_16x16x32_bf16 a[80:83], v[176:179], v[48:51], a[80:83]\n"
    "ds_read_b128 a[248:251], v253 offset:9728\n"
    "ds_read_b128 a[252:255], v253 offset:13824\n"
    "v_mfma_f32_16x16x32_bf16 a[112:115], v[176:179], v[56:59], a[112:115]\n"
    "v_exp_f32 v89, v89\n"
    "v_mfma_f32_16x16x32_bf16 a[20:23], v[180:183], v[32:35], a[20:23]\n"
    "v_exp_f32 v90, v90\n"
    "v_mfma_f32_16x16x32_bf16 a[52:55], v[180:183], v[40:43], a[52:55]\n"
    "v_exp_f32 v91, v91\n"
    "v_mfma_f32_16x16x32_bf16 a[84:87], v[180:183], v[48:51], a[84:87]\n"
    "v_exp_f32 v92, v92\n"
    "v_mfma_f32_16x16x32_bf16 a[116:119], v[180:183], v[56:59], a[116:119]\n"
    "v_exp_f32 v93, v93\n"
    "v_mfma_f32_16x16x32_bf16 a[24:27], v[184:187], v[32:35], a[24:27]\n"
    "v_cvt_pk_bf16_f32 v88, v88, v89\n"
    "v_cvt_pk_bf16_f32 v89, v90, v91\n"
    "v_mfma_f32_16x16x32_bf16 a[56:59], v[184:187], v[40:43], a[56:59]\n"
    "v_exp_f32 v94, v94\n"
    "v_mfma_f32_16x16x32_bf16 a[88:91], v[184:187], v[48:51], a[88:91]\n"
    "s_and_b32 s76, s74, 0x1fc\n"
    "s_cmp_eq_u32 s76, 4\n"
    "s_cselect_b32 s76, 4096, 0\n"
    "v_mfma_f32_16x16x32_bf16 a[120:123], v[184:187], v[56:59], a[120:123]\n"
    "v_exp_f32 v95, v95\n"
    "v_mfma_f32_16x16x32_bf16 a[28:31], v[188:191], v[32:35], a[28:31]\n"
    "v_add_u32 v255, s76, v255\n"
    "s_lshl_b32 s76, s76, 5\n"
    "v_mfma_f32_16x16x32_bf16 a[60:63], v[188:191], v[40:43], a[60:63]\n"
    "v_exp_f32 v96, v96\n"
    "v_mfma_f32_16x16x32_bf16 a[92:95], v[188:191], v[48:51], a[92:95]\n"
    "s_add_u32 s82, s82, s76\n"
    "s_cmp_eq_u32 s74, 4\n"
    "s_cselect_b32 s54, 0, s54\n"
    "v_mfma_f32_16x16x32_bf16 a[124:127], v[188:191], v[56:59], a[124:127]\n"
    "v_exp_f32 v97, v97\n"
    "v_mfma_f32_16x16x32_bf16 v[208:211], v[232:235], v[32:35], v[208:211]\n"
    "v_exp_f32 v98, v98\n"
    "v_mfma_f32_16x16x32_bf16 v[212:215], v[232:235], v[40:43], v[212:215]\n"
    "v_cvt_pk_bf16_f32 v90, v92, v93\n"
    "v_cvt_pk_bf16_f32 v91, v94, v95\n"
    "v_mfma_f32_16x16x32_bf16 v[216:219], v[232:235], v[48:51], v[216:219]\n"
    "v_exp_f32 v99, v99\n"
    "v_exp_f32 v100, v100\n"
    "v_exp_f32 v101, v101\n"
    "v_exp_f32 v102, v102\n"
    "v_exp_f32 v103, v103\n"
    "v_mfma_f32_16x16x32_bf16 v[220:223], v[232:235], v[56:59], v[220:223]\n"
    "s_waitcnt lgkmcnt(0)\n"
    "s_swappc_b64 s[86:87], s[94:95]\n"
    "s_sub_u32 s74, s74, 1\n"
    "s_cmp_eq_u32 s74, 2\n"
    "s_cbranch_scc1 PBS%=\n"
    "s_and_b32 s76, s74, 0xff\n"
    "s_cbranch_scc1 LPS%=\n"
    "s_cmp_lg_u32 s74, 0\n"
    "s_cbranch_scc1 PWS%=\n"
    "s_mov_b32 s54, 2097152\n"
    "s_sub_u32 s82, s82, 520192\n"
    "s_mov_b64 s[36:37], %11\n"
    "s_and_b32 s37, s37, 0xffff\n"
    "s_mov_b32 s38, 1309760\n"
    "s_mov_b32 s39, 0x20000\n"
    "s_mov_b64 s[48:49], %4\n"
    "s_add_u32 s48, s48, %5\n"
    "s_addc_u32 s49, s49, 0\n"
    "s_and_b32 s49, s49, 0xffff\n"
    "s_mov_b32 s50, 8192\n"
    "s_mov_b32 s51, 0x20000\n"
    "s_and_b32 s63, %9, 3\n"
    "s_lshl_b32 s60, %7, 19\n"
    "s_lshl_b32 s61, s63, 13\n"
    "s_mov_b64 s[40:41], %4\n"
    "s_add_u32 s40, s40, s60\n"
    "s_addc_u32 s41, s41, 0\n"
    "s_sub_u32 s40, s40, s61\n"
    "s_subb_u32 s41, s41, 0\n"
    "s_and_b32 s41, s41, 0xffff\n"
    "s_mov_b32 s42, 425984\n"
    "s_mov_b32 s43, 0x20000\n"
    "s_lshl_b32 s70, %14, 2\n"
    "s_add_u32 s70, s70, s63\n"
    "s_lshl_b32 s70, s70, 9\n"
    "v_lshrrev_b32 v230, 4, v229\n"
    "v_and_b32 v226, 3, v230\n"
    "v_lshlrev_b32 v227, 11, v226\n"
    "v_lshlrev_b32 v226, 9, v226\n"
    "s_lshl_b32 s60, %14, 11\n"
    "s_and_b32 s61, %9, 3\n"
    "s_lshl_b32 s61, s61, 9\n"
    "s_add_u32 s61, s61, 384\n"
    "v_add_u32 v226, s60, v226\n"
    "v_add_u32 v227, s61, v227\n"
    "v_and_b32 v228, 4, v230\n"
    "v_cmp_eq_u32 vcc, 0, v228\n"
    "s_nop 1\n"
    "v_cndmask_b32 v226, v227, v226, vcc\n"
    "v_and_b32 v227, 8, v230\n"
    "v_lshl_add_u32 v226, v227, 3, v226\n"
    "v_lshrrev_b32 v1, 3, v230\n"
    "v_and_b32 v1, 2, v1\n"
    "v_lshrrev_b32 v2, 5, v230\n"
    "v_mul_u32_u24 v2, 3, v2\n"
    "v_xor_b32 v1, v1, v2\n"
    "v_and_b32 v2, 3, v230\n"
    "v_xor_b32 v1, v1, v2\n"
    "v_lshlrev_b32 v1, 4, v1\n"
    "v_lshrrev_b32 v2, 2, v230\n"
    "v_mul_u32_u24 v3, 1280, v2\n"
    "v_add_u32 v231, v3, v1\n"
    "s_add_u32 s60, %14, 0\n"
    "s_and_b32 s60, s60, 3\n"
    "s_lshl_b32 s60, s60, 17\n"
    "v_add_u32 v224, s60, v229\n"
    "s_add_u32 s60, %14, 1\n"
    "s_and_b32 s60, s60, 3\n"
    "s_lshl_b32 s60, s60, 17\n"
    "v_add_u32 v225, s60, v229\n"
    "s_add_u32 s60, %14, 2\n"
    "s_and_b32 s60, s60, 3\n"
    "s_lshl_b32 s60, s60, 17\n"
    "v_add_u32 v227, s60, v229\n"
    "s_add_u32 s60, %14, 3\n"
    "s_and_b32 s60, s60, 3\n"
    "s_lshl_b32 s60, s60, 17\n"
    "v_add_u32 v253, s60, v229\n"
    "s_mul_i32 s56, %7, 20480\n"
    "s_lshr_b32 s60, %7, 1\n"
    "s_mul_i32 s59, s60, 8192\n"
    "s_lshl_b32 s60, %7, 10\n"
    "s_add_u32 s59, s59, s60\n"
    "s_add_u32 s59, %8, s59\n"
    "s_mov_b32 s57, 0\n"
    "s_mov_b32 s62, 0x100000\n"
    "s_waitcnt vmcnt(0)\n"
    "s_barrier\n"
    "v_exp_f32 v104, v104\n"
    "buffer_load_dword v228, v226, s[48:51], 0 offen sc0 sc1\n"
    "ds_read_b64_tr_b16 v[128:129], v250 offset:0\n"
    "ds_read_b64_tr_b16 v[130:131], v250 offset:4096\n"
    "v_cvt_pk_bf16_f32 v96, v96, v97\n"
    "v_cvt_pk_bf16_f32 v97, v98, v99\n"
    "v_exp_f32 v105, v105\n"
    "ds_read_b64_tr_b16 v[132:133], v251 offset:0\n"
    "ds_read_b64_tr_b16 v[134:135], v251 offset:4096\n"
    "s_add_u32 m0, s59, 0\n"
    "s_nop 0\n"
    "buffer_load_dwordx4 v231, s[36:39], s56 offen offset:0 lds\n"
    "s_add_u32 m0, s59, 1984\n"
    "v_exp_f32 v106, v106\n"
    "ds_read_b64_tr_b16 v[136:137], v250 offset:512\n"
    "ds_read_b64_tr_b16 v[138:139], v250 offset:4608\n"
    "v_cvt_pk_bf16_f32 v98, v100, v101\n"
    "v_cvt_pk_bf16_f32 v99, v102, v103\n"
    "buffer_load_dwordx4 v231, s[36:39], s56 offen offset:64 lds\n"
    "s_add_u32 m0, s59, 3968\n"
    "ds_read_b64_tr_b16 v[140:141], v251 offset:512\n"
    "ds_read_b64_tr_b16 v[142:143], v251 offset:4608\n"
    "v_exp_f32 v107, v107\n"
    "v_exp_f32 v108, v108\n"
    "buffer_load_dwordx4 v231, s[36:39], s56 offen offset:128 lds\n"
    "s_add_u32 m0, s59, 5952\n"
    "ds_read_b64_tr_b16 v[144:145], v250 offset:1024\n"
    "ds_read_b64_tr_b16 v[146:147], v250 offset:5120\n"
    "v_exp_f32 v109, v109\n"
    "ds_read_b64_tr_b16 v[148:149], v251 offset:1024\n"
    "ds_read_b64_tr_b16 v[150:151], v251 offset:5120\n"
    "buffer_load_dwordx4 v231, s[36:39], s56 offen offset:192 lds\n"
    "s_add_u32 m0, s59, 7936\n"
    "v_cvt_pk_bf16_f32 v104, v104, v105\n"
    "v_cvt_pk_bf16_f32 v105, v106, v107\n"
    "v_exp_f32 v110, v110\n"
    "ds_read_b64_tr_b16 v[152:153], v250 offset:1536\n"
    "ds_read_b64_tr_b16 v[154:155], v250 offset:5632\n"
    "buffer_load_dwordx4 v231, s[36:39], s56 offen offset:256 lds\n"
    "s_add_u32 s56, s56, 81920\n"
    "v_exp_f32 v111, v111\n"
    "ds_read_b64_tr_b16 v[156:157], v251 offset:1536\n"
    "ds_read_b64_tr_b16 v[158:159], v251 offset:5632\n"
    "v_exp_f32 v112, v112\n"
    "v_cvt_pk_bf16_f32 v106, v108, v109\n"
    "v_cvt_pk_bf16_f32 v107, v110, v111\n"
    "s_add_u32 m0, s59, 20480\n"
    "s_nop 0\n"
    "buffer_load_dwordx4 v231, s[36:39], s56 offen offset:0 lds\n"
    "s_add_u32 m0, s59, 22464\n"
    "ds_read_b64_tr_b16 v[160:161], v250 offset:8192\n"
    "ds_read_b64_tr_b16 v[162:163], v250 offset:12288\n"
    "v_exp_f32 v113, v113\n"
    "v_exp_f32 v114, v114\n"
    "buffer_load_dwordx4 v231, s[36:39], s56 offen offset:64 lds\n"
    "s_add_u32 m0, s59, 24448\n"
    "ds_read_b64_tr_b16 v[164:165], v251 offset:8192\n"
    "ds_read_b64_tr_b16 v[166:167], v251 offset:12288\n"
    "v_exp_f32 v115, v115\n"
    "ds_read_b64_tr_b16 v[168:169], v250 offset:8704\n"
    "ds_read_b64_tr_b16 v[170:171], v250 offset:12800\n"
    "buffer_load_dwordx4 v231, s[36:39], s56 offen offset:128 lds\n"
    "s_add_u32 m0, s59, 26432\n"
    "v_exp_f32 v116, v116\n"
    "ds_read_b64_tr_b16 v[172:173], v251 offset:8704\n"
    "ds_read_b64_tr_b16 v[174:175], v251 offset:12800\n"
    "v_cvt_pk_bf16_f32 v112, v112, v113\n"
    "v_cvt_pk_bf16_f32 v113, v114, v115\n"
    "buffer_load_dwordx4 v231, s[36:39], s56 offen offset:192 lds\n"
    "s_add_u32 m0, s59, 28416\n"
    "v_exp_f32 v117, v117\n"
    "ds_read_b64_tr_b16 v[176:177], v250 offset:9216\n"
    "ds_read_b64_tr_b16 v[178:179], v250 offset:13312\n"
    "v_exp_f32 v118, v118\n"
    "buffer_load_dwordx4 v231, s[36:39], s56 offen offset:256 lds\n"
    "s_add_u32 s56, s56, 245760\n"
    "ds_read_b64_tr_b16 v[180:181], v251 offset:9216\n"
    "ds_read_b64_tr_b16 v[182:183], v251 offset:13312\n"
    "v_exp_f32 v119, v119\n"
    "v_exp_f32 v120, v120\n"
    "ds_read_b64_tr_b16 v[184:185], v250 offset:9728\n"
    "ds_read_b64_tr_b16 v[186:187], v250 offset:13824\n"
    "s_add_u32 m0, s59, 40960\n"
    "s_nop 0\n"
    "buffer_load_dwordx4 v231, s[36:39], s56 offen offset:0 lds\n"
    "s_add_u32 m0, s59, 42944\n"
    "v_cvt_pk_bf16_f32 v114, v116, v117\n"
    "v_cvt_pk_bf16_f32 v115, v118, v119\n"
    "ds_read_b64_tr_b16 v[188:189], v251 offset:9728\n"
    "ds_read_b64_tr_b16 v[190:191], v251 offset:13824\n"
    "v_exp_f32 v121, v121\n"
    "buffer_load_dwordx4 v231, s[36:39], s56 offen offset:64 lds\n"
    "s_add_u32 m0, s59, 44928\n"
    "v_exp_f32 v122, v122\n"
    "v_exp_f32 v123, v123\n"
    "v_exp_f32 v124, v124\n"
    "buffer_load_dwordx4 v231, s[36:39], s56 offen offset:128 lds\n"
    "s_add_u32 m0, s59, 46912\n"
    "v_cvt_pk_bf16_f32 v120, v120, v121\n"
    "v_cvt_pk_bf16_f32 v121, v122, v123\n"
    "v_exp_f32 v125, v125\n"
    "buffer_load_dwordx4 v231, s[36:39], s56 offen offset:192 lds\n"
    "s_add_u32 m0, s59, 48896\n"
    "v_exp_f32 v126, v126\n"
    "v_exp_f32 v127, v127\n"
    "s_nop 0\n"
    "v_cvt_pk_bf16_f32 v122, v124, v125\n"
    "v_cvt_pk_bf16_f32 v123, v126, v127\n"
    "buffer_load_dwordx4 v231, s[36:39], s56 offen offset:256 lds\n"
    "s_add_u32 s56, s56, 81920\n"
    "s_waitcnt lgkmcnt(0)\n"
    "s_nop 1\n"
    "v_mfma_f32_16x16x32_bf16 a[0:3], v[128:131], v[64:67], a[0:3]\n"
    "s_waitcnt vmcnt(15)\n"
    "v_and_b32 v228, 1, v228\n"
    "v_cmp_ne_u32 vcc, 0, v228\n"
    "s_xor_b32 s60, vcc_lo, s64\n"
    "s_andn2_b32 s60, 15, s60\n"
    "s_cselect_b32 s42, 0, s42\n"
    "v_mfma_f32_16x16x32_bf16 a[32:35], v[128:131], v[72:75], a[32:35]\n"
    "v_mfma_f32_16x16x32_bf16 a[64:67], v[128:131], v[80:83], a[64:67]\n"
    "v_mfma_f32_16x16x32_bf16 a[96:99], v[128:131], v[88:91], a[96:99]\n"
    "v_mfma_f32_16x16x32_bf16 a[4:7], v[132:135], v[64:67], a[4:7]\n"
    "buffer_load_dwordx4 v[0:3], v224, s[40:43], s57 offen offset:0 sc1\n"
    "v_mfma_f32_16x16x32_bf16 a[36:39], v[132:135], v[72:75], a[36:39]\n"
    "v_mfma_f32_16x16x32_bf16 a[68:71], v[132:135], v[80:83], a[68:71]\n"
    "v_mfma_f32_16x16x32_bf16 a[100:103], v[132:135], v[88:91], a[100:103]\n"
    "v_mfma_f32_16x16x32_bf16 a[8:11], v[136:139], v[64:67], a[8:11]\n"
    "buffer_load_dwordx4 v[4:7], v225, s[40:43], s57 offen offset:0 sc1\n"
    "v_mfma_f32_16x16x32_bf16 a[40:43], v[136:139], v[72:75], a[40:43]\n"
    "v_mfma_f32_16x16x32_bf16 a[72:75], v[136:139], v[80:83], a[72:75]\n"
    "v_mfma_f32_16x16x32_bf16 a[104:107], v[136:139], v[88:91], a[104:107]\n"
    "v_mfma_f32_16x16x32_bf16 a[12:15], v[140:143], v[64:67], a[12:15]\n"
    "buffer_load_dwordx4 v[8:11], v227, s[40:43], s57 offen offset:0 sc1\n"
    "v_mfma_f32_16x16x32_bf16 a[44:47], v[140:143], v[72:75], a[44:47]\n"
    "v_mfma_f32_16x16x32_bf16 a[76:79], v[140:143], v[80:83], a[76:79]\n"
    "v_mfma_f32_16x16x32_bf16 a[108:111], v[140:143], v[88:91], a[108:111]\n"
    "v_mfma_f32_16x16x32_bf16 a[16:19], v[144:147], v[64:67], a[16:19]\n"
    "buffer_load_dwordx4 v[12:15], v253, s[40:43], s57 offen offset:0 sc1\n"
    "v_mfma_f32_16x16x32_bf16 a[48:51], v[144:147], v[72:75], a[48:51]\n"
    "v_mfma_f32_16x16x32_bf16 a[80:83], v[144:147], v[80:83], a[80:83]\n"
    "v_mfma_f32_16x16x32_bf16 a[112:115], v[144:147], v[88:91], a[112:115]\n"
    "v_mfma_f32_16x16x32_bf16 a[20:23], v[148:151], v[64:67], a[20:23]\n"
    "buffer_load_dwordx4 v[16:19], v224, s[40:43], s57 offen offset:1024 sc1\n"
    "v_mfma_f32_16x16x32_bf16 a[52:55], v[148:151], v[72:75], a[52:55]\n"
    "v_mfma_f32_16x16x32_bf16 a[84:87], v[148:151], v[80:83], a[84:87]\n"
    "v_mfma_f32_16x16x32_bf16 a[116:119], v[148:151], v[88:91], a[116:119]\n"
    "v_mfma_f32_16x16x32_bf16 a[24:27], v[152:155], v[64:67], a[24:27]\n"
    "buffer_load_dwordx4 v[20:23], v225, s[40:43], s57 offen offset:1024 sc1\n"
    "v_mfma_f32_16x16x32_bf16 a[56:59], v[152:155], v[72:75], a[56:59]\n"
    "v_mfma_f32_16x16x32_bf16 a[88:91], v[152:155], v[80:83], a[88:91]\n"
    "v_mfma_f32_16x16x32_bf16 a[120:123], v[152:155], v[88:91], a[120:123]\n"
    "v_mfma_f32_16x16x32_bf16 a[28:31], v[156:159], v[64:67], a[28:31]\n"
    "buffer_load_dwordx4 v[24:27], v227, s[40:43], s57 offen offset:1024 sc1\n"
    "v_mfma_f32_16x16x32_bf16 a[60:63], v[156:159], v[72:75], a[60:63]\n"
    "v_mfma_f32_16x16x32_bf16 a[92:95], v[156:159], v[80:83], a[92:95]\n"
    "v_mfma_f32_16x16x32_bf16 a[124:127], v[156:159], v[88:91], a[124:127]\n"
    "v_mfma_f32_16x16x32_bf16 v[208:211], v[232:235], v[64:67], v[208:211]\n"
    "buffer_load_dwordx4 v[28:31], v253, s[40:43], s57 offen offset:1024 sc1\n"
    "v_mfma_f32_16x16x32_bf16 v[212:215], v[232:235], v[72:75], v[212:215]\n"
    "v_mfma_f32_16x16x32_bf16 v[216:219], v[232:235], v[80:83], v[216:219]\n"
    "v_mfma_f32_16x16x32_bf16 v[220:223], v[232:235], v[88:91], v[220:223]\n"
    "v_mfma_f32_16x16x32_bf16 a[0:3], v[160:163], v[96:99], a[0:3]\n"
    "buffer_load_dwordx4 v[32:35], v224, s[40:43], s57 offen offset:2048 sc1\n"
    "v_mfma_f32_16x16x32_bf16 a[32:35], v[160:163], v[104:107], a[32:35]\n"
    "v_mfma_f32_16x16x32_bf16 a[64:67], v[160:163], v[112:115], a[64:67]\n"
    "v_mfma_f32_16x16x32_bf16 a[96:99], v[160:163], v[120:123], a[96:99]\n"
    "v_mfma_f32_16x16x32_bf16 a[4:7], v[164:167], v[96:99], a[4:7]\n"
    "buffer_load_dwordx4 v[36:39], v225, s[40:43], s57 offen offset:2048 sc1\n"
    "v_mfma_f32_16x16x32_bf16 a[36:39], v[164:167], v[104:107], a[36:39]\n"
    "v_mfma_f32_16x16x32_bf16 a[68:71], v[164:167], v[112:115], a[68:71]\n"
    "v_mfma_f32_16x16x32_bf16 a[100:103], v[164:167], v[120:123], a[100:103]\n"
    "v_mfma_f32_16x16x32_bf16 a[8:11], v[168:171], v[96:99], a[8:11]\n"
    "buffer_load_dwordx4 v[40:43], v227, s[40:43], s57 offen offset:2048 sc1\n"
    "v_mfma_f32_16x16x32_bf16 a[40:43], v[168:171], v[104:107], a[40:43]\n"
    "v_mfma_f32_16x16x32_bf16 a[72:75], v[168:171], v[112:115], a[72:75]\n"
    "v_mfma_f32_16x16x32_bf16 a[104:107], v[168:171], v[120:123], a[104:107]\n"
    "v_mfma_f32_16x16x32_bf16 a[12:15], v[172:175], v[96:99], a[12:15]\n"
    "buffer_load_dwordx4 v[44:47], v253, s[40:43], s57 offen offset:2048 sc1\n"
    "v_mfma_f32_16x16x32_bf16 a[44:47], v[172:175], v[104:107], a[44:47]\n"
    "v_mfma_f32_16x16x32_bf16 a[76:79], v[172:175], v[112:115], a[76:79]\n"
    "v_mfma_f32_16x16x32_bf16 a[108:111], v[172:175], v[120:123], a[108:111]\n"
    "v_mfma_f32_16x16x32_bf16 a[16:19], v[176:179], v[96:99], a[16:19]\n"
    "buffer_load_dwordx4 v[48:51], v224, s[40:43], s57 offen offset:3072 sc1\n"
    "v_mfma_f32_16x16x32_bf16 a[48:51], v[176:179], v[104:107], a[48:51]\n"
    "v_mfma_f32_16x16x32_bf16 a[80:83], v[176:179], v[112:115], a[80:83]\n"
    "v_mfma_f32_16x16x32_bf16 a[112:115], v[176:179], v[120:123], a[112:115]\n"
    "v_mfma_f32_16x16x32_bf16 a[20:23], v[180:183], v[96:99], a[20:23]\n"
    "buffer_load_dwordx4 v[52:55], v225, s[40:43], s57 offen offset:3072 sc1\n"
    "v_mfma_f32_16x16x32_bf16 a[52:55], v[180:183], v[104:107], a[52:55]\n"
    "v_mfma_f32_16x16x32_bf16 a[84:87], v[180:183], v[112:115], a[84:87]\n"
    "v_mfma_f32_16x16x32_bf16 a[116:119], v[180:183], v[120:123], a[116:119]\n"
    "v_mfma_f32_16x16x32_bf16 a[24:27], v[184:187], v[96:99], a[24:27]\n"
    "buffer_load_dwordx4 v[56:59], v227, s[40:43], s57 offen offset:3072 sc1\n"
    "v_mfma_f32_16x16x32_bf16 a[56:59], v[184:187], v[104:107], a[56:59]\n"
    "v_mfma_f32_16x16x32_bf16 a[88:91], v[184:187], v[112:115], a[88:91]\n"
    "v_mfma_f32_16x16x32_bf16 a[120:123], v[184:187], v[120:123], a[120:123]\n"
    "v_mfma_f32_16x16x32_bf16 a[28:31], v[188:191], v[96:99], a[28:31]\n"
    "buffer_load_dwordx4 v[60:63], v253, s[40:43], s57 offen offset:3072 sc1\n"
    "v_mfma_f32_16x16x32_bf16 a[60:63], v[188:191], v[104:107], a[60:63]\n"
    "v_mfma_f32_16x16x32_bf16 a[92:95], v[188:191], v[112:115], a[92:95]\n"
    "v_mfma_f32_16x16x32_bf16 a[124:127], v[188:191], v[120:123], a[124:127]\n"
    "v_mfma_f32_16x16x32_bf16 v[208:211], v[232:235], v[96:99], v[208:211]\n"
    "s_add_u32 s57, s57, 8192\n"
    "v_mfma_f32_16x16x32_bf16 v[212:215], v[232:235], v[104:107], v[212:215]\n"
    "v_mfma_f32_16x16x32_bf16 v[216:219], v[232:235], v[112:115], v[216:219]\n"
    "v_mfma_f32_16x16x32_bf16 v[220:223], v[232:235], v[120:123], v[220:223]\n"
    "s_nop 7\n"
    "v_cmp_nlt_f32 vcc, v208, v252\n"
    "v_rcp_f32 v236, v208\n"
    "s_or_b64 s[96:97], s[96:97], vcc\n"
    "v_cmp_nlt_f32 vcc, v212, v252\n"
    "v_rcp_f32 v237, v212\n"
    "s_or_b64 s[96:97], s[96:97], vcc\n"
    "v_cmp_nlt_f32 vcc, v216, v252\n"
    "v_rcp_f32 v238, v216\n"
    "s_or_b64 s[96:97], s[96:97], vcc\n"
    "v_cmp_nlt_f32 vcc, v220, v252\n"
    "v_rcp_f32 v239, v220\n"
    "s_or_b64 s[96:97], s[96:97], vcc\n"
    "s_nop 1\n"
    "s_barrier\n"
    "s_branch END%=\n"
    "QL1_%=:\n"
    "ds_read_b128 a[128:131], v255 offset:0\n"
    "ds_read_b128 a[132:135], v255 offset:1024\n"
    "ds_read_b128 a[136:139], v255 offset:2048\n"
    "ds_read_b128 a[140:143], v255 offset:3072\n"
    "ds_read_b128 a[144:147], v255 offset:4096\n"
    "ds_read_b128 a[148:151], v255 offset:5120\n"
    "ds_read_b128 a[152:155], v255 offset:6144\n"
    "ds_read_b128 a[156:159], v255 offset:7168\n"
    "ds_read_b128 a[160:163], v255 offset:8192\n"
    "ds_read_b128 a[164:167], v255 offset:9216\n"
    "ds_read_b128 a[168:171], v255 offset:10240\n"
    "ds_read_b128 a[172:175], v255 offset:11264\n"
    "ds_read_b128 a[176:179], v255 offset:12288\n"
    "ds_read_b128 a[180:183], v255 offset:13312\n"
    "ds_read_b128 a[184:187], v255 offset:14336\n"
    "ds_read_b128 a[188:191], v255 offset:15360\n"
    "s_branch QB1_%=\n"
    "PBS%=:\n"
    "s_cmp_lg_u32 s67, 0\n"
    "s_cbranch_scc1 LPS%=\n"
    "s_mov_b32 s67, 1\n"
    "s_and_b32 s77, %9, 3\n"
    "s_lshr_b32 s76, s64, s77\n"
    "s_andn2_b32 s76, 1, s76\n"
    "v_mov_b32 v236, s76\n"
    "s_lshl_b32 s76, %14, 2\n"
    "s_add_u32 s76, s76, s77\n"
    "s_lshl_b32 s76, s76, 9\n"
    "buffer_store_dword v236, off, s[56:59], s76 sc0 sc1\n"
    "s_branch LPS%=\n"
    "FIX0%=:\n"
    "s_nop 7\n"
    "v_max3_f32 v240, v0, v1, v2\n"
    "v_max3_f32 v241, v8, v9, v10\n"
    "v_max3_f32 v242, v16, v17, v18\n"
    "v_max3_f32 v243, v24, v25, v26\n"
    "v_max3_f32 v240, v240, v3, v4\n"
    "v_max3_f32 v241, v241, v11, v12\n"
    "v_max3_f32 v242, v242, v19, v20\n"
    "v_max3_f32 v243, v243, v27, v28\n"
    "v_max3_f32 v240, v240, v5, v6\n"
    "v_max3_f32 v241, v241, v13, v14\n"
    "v_max3_f32 v242, v242, v21, v22\n"
    "v_max3_f32 v243, v243, v29, v30\n"
    "v_max3_f32 v240, v240, v7, v32\n"
    "v_max3_f32 v241, v241, v15, v40\n"
    "v_max3_f32 v242, v242, v23, v48\n"
    "v_max3_f32 v243, v243, v31, v56\n"
    "v_max3_f32 v240, v240, v33, v34\n"
    "v_max3_f32 v241, v241, v41, v42\n"
    "v_max3_f32 v242, v242, v49, v50\n"
    "v_max3_f32 v243, v243, v57, v58\n"
    "v_max3_f32 v240, v240, v35, v36\n"
    "v_max3_f32 v241, v241, v43, v44\n"
    "v_max3_f32 v242, v242, v51, v52\n"
    "v_max3_f32 v243, v243, v59, v60\n"
    "v_max3_f32 v240, v240, v37, v38\n"
    "v_max3_f32 v241, v241, v45, v46\n"
    "v_max3_f32 v242, v242, v53, v54\n"
    "v_max3_f32 v243, v243, v61, v62\n"
    "v_max3_f32 v240, v240, v39, v39\n"
    "v_max3_f32 v241, v241, v47, v47\n"
    "v_max3_f32 v242, v242, v55, v55\n"
    "v_max3_f32 v243, v243, v63, v63\n"
    "v_mov_b32 v236, v240\n"
    "v_mov_b32 v237, v241\n"
    "v_mov_b32 v238, v242\n"
    "v_mov_b32 v239, v243\n"
    "s_nop 0\n"
    "v_permlane32_swap_b32 v240, v236\n"
    "v_permlane32_swap_b32 v241, v237\n"
    "v_permlane32_swap_b32 v242, v238\n"
    "v_permlane32_swap_b32 v243, v239\n"
    "s_nop 0\n"
    "v_max_f32 v240, v240, v236\n"
    "v_max_f32 v241, v241, v237\n"
    "v_max_f32 v242, v242, v238\n"
    "v_max_f32 v243, v243, v239\n"
    "v_mov_b32 v236, v240\n"
    "v_mov_b32 v237, v241\n"
    "v_mov_b32 v238, v242\n"
    "v_mov_b32 v239, v243\n"
    "s_nop 0\n"
    "v_permlane16_swap_b32 v240, v236\n"
    "v_permlane16_swap_b32 v241, v237\n"
    "v_permlane16_swap_b32 v242, v238\n"
    "v_permlane16_swap_b32 v243, v239\n"
    "s_nop 0\n"
    "v_max_f32 v240, v240, v236\n"
    "v_max_f32 v241, v241, v237\n"
    "v_max_f32 v242, v242, v238\n"
    "v_max_f32 v243, v243, v239\n"
    "v_max_f32 v240, 0, v240\n"
    "v_max_f32 v241, 0, v241\n"
    "v_max_f32 v242, 0, v242\n"
    "v_max_f32 v243, 0, v243\n"
    "v_sub_f32 v0, v0, v240\n"
    "v_sub_f32 v1, v1, v240\n"
    "v_sub_f32 v2, v2, v240\n"
    "v_sub_f32 v3, v3, v240\n"
    "v_sub_f32 v4, v4, v240\n"
    "v_sub_f32 v5, v5, v240\n"
    "v_sub_f32 v6, v6, v240\n"
    "v_sub_f32 v7, v7, v240\n"
    "v_sub_f32 v8, v8, v241\n"
    "v_sub_f32 v9, v9, v241\n"
    "v_sub_f32 v10, v10, v241\n"
    "v_sub_f32 v11, v11, v241\n"
    "v_sub_f32 v12, v12, v241\n"
    "v_sub_f32 v13, v13, v241\n"
    "v_sub_f32 v14, v14, v241\n"
    "v_sub_f32 v15, v15, v241\n"
    "v_sub_f32 v16, v16, v242\n"
    "v_sub_f32 v17, v17, v242\n"
    "v_sub_f32 v18, v18, v242\n"
    "v_sub_f32 v19, v19, v242\n"
    "v_sub_f32 v20, v20, v242\n"
    "v_sub_f32 v21, v21, v242\n"
    "v_sub_f32 v22, v22, v242\n"
    "v_sub_f32 v23, v23, v242\n"
    "v_sub_f32 v24, v24, v243\n"
    "v_sub_f32 v25, v25, v243\n"
    "v_sub_f32 v26, v26, v243\n"
    "v_sub_f32 v27, v27, v243\n"
    "v_sub_f32 v28, v28, v243\n"
    "v_sub_f32 v29, v29, v243\n"
    "v_sub_f32 v30, v30, v243\n"
    "v_sub_f32 v31, v31, v243\n"
    "v_sub_f32 v32, v32, v240\n"
    "v_sub_f32 v33, v33, v240\n"
    "v_sub_f32 v34, v34, v240\n"
    "v_sub_f32 v35, v35, v240\n"
    "v_sub_f32 v36, v36, v240\n"
    "v_sub_f32 v37, v37, v240\n"
    "v_sub_f32 v38, v38, v240\n"
    "v_sub_f32 v39, v39, v240\n"
    "v_sub_f32 v40, v40, v241\n"
    "v_sub_f32 v41, v41, v241\n"
    "v_sub_f32 v42, v42, v241\n"
    "v_sub_f32 v43, v43, v241\n"
    "v_sub_f32 v44, v44, v241\n"
    "v_sub_f32 v45, v45, v241\n"
    "v_sub_f32 v46, v46, v241\n"
    "v_sub_f32 v47, v47, v241\n"
    "v_sub_f32 v48, v48, v242\n"
    "v_sub_f32 v49, v49, v242\n"
    "v_sub_f32 v50, v50, v242\n"
    "v_sub_f32 v51, v51, v242\n"
    "v_sub_f32 v52, v52, v242\n"
    "v_sub_f32 v53, v53, v242\n"
    "v_sub_f32 v54, v54, v242\n"
    "v_sub_f32 v55, v55, v242\n"
    "v_sub_f32 v56, v56, v243\n"
    "v_sub_f32 v57, v57, v243\n"
    "v_sub_f32 v58, v58, v243\n"
    "v_sub_f32 v59, v59, v243\n"
    "v_sub_f32 v60, v60, v243\n"
    "v_sub_f32 v61, v61, v243\n"
    "v_sub_f32 v62, v62, v243\n"
    "v_sub_f32 v63, v63, v243\n"
    "v_sub_f32 v192, v192, v240\n"
    "v_sub_f32 v193, v193, v240\n"
    "v_sub_f32 v194, v194, v240\n"
    "v_sub_f32 v195, v195, v240\n"
    "v_sub_f32 v196, v196, v241\n"
    "v_sub_f32 v197, v197, v241\n"
    "v_sub_f32 v198, v198, v241\n"
    "v_sub_f32 v199, v199, v241\n"
    "v_sub_f32 v200, v200, v242\n"
    "v_sub_f32 v201, v201, v242\n"
    "v_sub_f32 v202, v202, v242\n"
    "v_sub_f32 v203, v203, v242\n"
    "v_sub_f32 v204, v204, v243\n"
    "v_sub_f32 v205, v205, v243\n"
    "v_sub_f32 v206, v206, v243\n"
    "v_sub_f32 v207, v207, v243\n"
    "v_exp_f32 v244, -v240\n"
    "v_exp_f32 v245, -v241\n"
    "v_exp_f32 v246, -v242\n"
    "v_exp_f32 v247, -v243\n"
    "s_setpc_b64 s[86:87]\n"
    "FIX1%=:\n"
    "s_nop 7\n"
    "v_max3_f32 v240, v64, v65, v66\n"
    "v_max3_f32 v241, v72, v73, v74\n"
    "v_max3_f32 v242, v80, v81, v82\n"
    "v_max3_f32 v243, v88, v89, v90\n"
    "v_max3_f32 v240, v240, v67, v68\n"
    "v_max3_f32 v241, v241, v75, v76\n"
    "v_max3_f32 v242, v242, v83, v84\n"
    "v_max3_f32 v243, v243, v91, v92\n"
    "v_max3_f32 v240, v240, v69, v70\n"
    "v_max3_f32 v241, v241, v77, v78\n"
    "v_max3_f32 v242, v242, v85, v86\n"
    "v_max3_f32 v243, v243, v93, v94\n"
    "v_max3_f32 v240, v240, v71, v96\n"
    "v_max3_f32 v241, v241, v79, v104\n"
    "v_max3_f32 v242, v242, v87, v112\n"
    "v_max3_f32 v243, v243, v95, v120\n"
    "v_max3_f32 v240, v240, v97, v98\n"
    "v_max3_f32 v241, v241, v105, v106\n"
    "v_max3_f32 v242, v242, v113, v114\n"
    "v_max3_f32 v243, v243, v121, v122\n"
    "v_max3_f32 v240, v240, v99, v100\n"
    "v_max3_f32 v241, v241, v107, v108\n"
    "v_max3_f32 v242, v242, v115, v116\n"
    "v_max3_f32 v243, v243, v123, v124\n"
    "v_max3_f32 v240, v240, v101, v102\n"
    "v_max3_f32 v241, v241, v109, v110\n"
    "v_max3_f32 v242, v242, v117, v118\n"
    "v_max3_f32 v243, v243, v125, v126\n"
    "v_max3_f32 v240, v240, v103, v103\n"
    "v_max3_f32 v241, v241, v111, v111\n"
    "v_max3_f32 v242, v242, v119, v119\n"
    "v_max3_f32 v243, v243, v127, v127\n"
    "v_mov_b32 v236, v240\n"
    "v_mov_b32 v237, v241\n"
    "v_mov_b32 v238, v242\n"
    "v_mov_b32 v239, v243\n"
    "s_nop 0\n"
    "v_permlane32_swap_b32 v240, v236\n"
    "v_permlane32_swap_b32 v241, v237\n"
    "v_permlane32_swap_b32 v242, v238\n"
    "v_permlane32_swap_b32 v243, v239\n"
    "s_nop 0\n"
    "v_max_f32 v240, v240, v236\n"
    "v_max_f32 v241, v241, v237\n"
    "v_max_f32 v242, v242, v238\n"
    "v_max_f32 v243, v243, v239\n"
    "v_mov_b32 v236, v240\n"
    "v_mov_b32 v237, v241\n"
    "v_mov_b32 v238, v242\n"
    "v_mov_b32 v239, v243\n"
    "s_nop 0\n"
    "v_permlane16_swap_b32 v240, v236\n"
    "v_permlane16_swap_b32 v241, v237\n"
    "v_permlane16_swap_b32 v242, v238\n"
    "v_permlane16_swap_b32 v243, v239\n"
    "s_nop 0\n"
    "v_max_f32 v240, v240, v236\n"
    "v_max_f32 v241, v241, v237\n"
    "v_max_f32 v242, v242, v238\n"
    "v_max_f32 v243, v243, v239\n"
    "v_max_f32 v240, 0, v240\n"
    "v_max_f32 v241, 0, v241\n"
    "v_max_f32 v242, 0, v242\n"
    "v_max_f32 v243, 0, v243\n"
    "v_sub_f32 v64, v64, v240\n"
    "v_sub_f32 v65, v65, v240\n"
    "v_sub_f32 v66, v66, v240\n"
    "v_sub_f32 v67, v67, v240\n"
    "v_sub_f32 v68, v68, v240\n"
    "v_sub_f32 v69, v69, v240\n"
    "v_sub_f32 v70, v70, v240\n"
    "v_sub_f32 v71, v71, v240\n"
    "v_sub_f32 v72, v72, v241\n"
    "v_sub_f32 v73, v73, v241\n"
    "v_sub_f32 v74, v74, v241\n"
    "v_sub_f32 v75, v75, v241\n"
    "v_sub_f32 v76, v76, v241\n"
    "v_sub_f32 v77, v77, v241\n"
    "v_sub_f32 v78, v78, v241\n"
    "v_sub_f32 v79, v79, v241\n"
    "v_sub_f32 v80, v80, v242\n"
    "v_sub_f32 v81, v81, v242\n"
    "v_sub_f32 v82, v82, v242\n"
    "v_sub_f32 v83, v83, v242\n"
    "v_sub_f32 v84, v84, v242\n"
    "v_sub_f32 v85, v85, v242\n"
    "v_sub_f32 v86, v86, v242\n"
    "v_sub_f32 v87, v87, v242\n"
    "v_sub_f32 v88, v88, v243\n"
    "v_sub_f32 v89, v89, v243\n"
    "v_sub_f32 v90, v90, v243\n"
    "v_sub_f32 v91, v91, v243\n"
    "v_sub_f32 v92, v92, v243\n"
    "v_sub_f32 v93, v93, v243\n"
    "v_sub_f32 v94, v94, v243\n"
    "v_sub_f32 v95, v95, v243\n"
    "v_sub_f32 v96, v96, v240\n"
    "v_sub_f32 v97, v97, v240\n"
    "v_sub_f32 v98, v98, v240\n"
    "v_sub_f32 v99, v99, v240\n"
    "v_sub_f32 v100, v100, v240\n"
    "v_sub_f32 v101, v101, v240\n"
    "v_sub_f32 v102, v102, v240\n"
    "v_sub_f32 v103, v103, v240\n"
    "v_sub_f32 v104, v104, v241\n"
    "v_sub_f32 v105, v105, v241\n"
    "v_sub_f32 v106, v106, v241\n"
    "v_sub_f32 v107, v107, v241\n"
    "v_sub_f32 v108, v108, v241\n"
    "v_sub_f32 v109, v109, v241\n"
    "v_sub_f32 v110, v110, v241\n"
    "v_sub_f32 v111, v111, v241\n"
    "v_sub_f32 v112, v112, v242\n"
    "v_sub_f32 v113, v113, v242\n"
    "v_sub_f32 v114, v114, v242\n"
    "v_sub_f32 v115, v115, v242\n"
    "v_sub_f32 v116, v116, v242\n"
    "v_sub_f32 v117, v117, v242\n"
    "v_sub_f32 v118, v118, v242\n"
    "v_sub_f32 v119, v119, v242\n"
    "v_sub_f32 v120, v120, v243\n"
    "v_sub_f32 v121, v121, v243\n"
    "v_sub_f32 v122, v122, v243\n"
    "v_sub_f32 v123, v123, v243\n"
    "v_sub_f32 v124, v124, v243\n"
    "v_sub_f32 v125, v125, v243\n"
    "v_sub_f32 v126, v126, v243\n"
    "v_sub_f32 v127, v127, v243\n"
    "v_sub_f32 v192, v192, v240\n"
    "v_sub_f32 v193, v193, v240\n"
    "v_sub_f32 v194, v194, v240\n"
    "v_sub_f32 v195, v195, v240\n"
    "v_sub_f32 v196, v196, v241\n"
    "v_sub_f32 v197, v197, v241\n"
    "v_sub_f32 v198, v198, v241\n"
    "v_sub_f32 v199, v199, v241\n"
    "v_sub_f32 v200, v200, v242\n"
    "v_sub_f32 v201, v201, v242\n"
    "v_sub_f32 v202, v202, v242\n"
    "v_sub_f32 v203, v203, v242\n"
    "v_sub_f32 v204, v204, v243\n"
    "v_sub_f32 v205, v205, v243\n"
    "v_sub_f32 v206, v206, v243\n"
    "v_sub_f32 v207, v207, v243\n"
    "v_exp_f32 v244, -v240\n"
    "v_exp_f32 v245, -v241\n"
    "v_exp_f32 v246, -v242\n"
    "v_exp_f32 v247, -v243\n"
    "s_setpc_b64 s[86:87]\n"
    "RESC%=:\n"
    "s_nop 7\n"
    "v_accvgpr_read_b32 v236, a0\n"
    "v_mul_f32 v236, v244, v236\n"
    "v_accvgpr_write_b32 a0, v236\n"
    "v_accvgpr_read_b32 v237, a1\n"
    "v_mul_f32 v237, v244, v237\n"
    "v_accvgpr_write_b32 a1, v237\n"
    "v_accvgpr_read_b32 v238, a2\n"
    "v_mul_f32 v238, v244, v238\n"
    "v_accvgpr_write_b32 a2, v238\n"
    "v_accvgpr_read_b32 v239, a3\n"
    "v_mul_f32 v239, v244, v239\n"
    "v_accvgpr_write_b32 a3, v239\n"
    "v_accvgpr_read_b32 v236, a4\n"
    "v_mul_f32 v236, v244, v236\n"
    "v_accvgpr_write_b32 a4, v236\n"
    "v_accvgpr_read_b32 v237, a5\n"
    "v_mul_f32 v237, v244, v237\n"
    "v_accvgpr_write_b32 a5, v237\n"
    "v_accvgpr_read_b32 v238, a6\n"
    "v_mul_f32 v238, v244, v238\n"
    "v_accvgpr_write_b32 a6, v238\n"
    "v_accvgpr_read_b32 v239, a7\n"
    "v_mul_f32 v239, v244, v239\n"
    "v_accvgpr_write_b32 a7, v239\n"
    "v_accvgpr_read_b32 v236, a8\n"
    "v_mul_f32 v236, v244, v236\n"
    "v_accvgpr_write_b32 a8, v236\n"
    "v_accvgpr_read_b32 v237, a9\n"
    "v_mul_f32 v237, v244, v237\n"
    "v_accvgpr_write_b32 a9, v237\n"
    "v_accvgpr_read_b32 v238, a10\n"
    "v_mul_f32 v238, v244, v238\n"
    "v_accvgpr_write_b32 a10, v238\n"
    "v_accvgpr_read_b32 v239, a11\n"
    "v_mul_f32 v239, v244, v239\n"
    "v_accvgpr_write_b32 a11, v239\n"
    "v_accvgpr_read_b32 v236, a12\n"
    "v_mul_f32 v236, v244, v236\n"
    "v_accvgpr_write_b32 a12, v236\n"
    "v_accvgpr_read_b32 v237, a13\n"
    "v_mul_f32 v237, v244, v237\n"
    "v_accvgpr_write_b32 a13, v237\n"
    "v_accvgpr_read_b32 v238, a14\n"
    "v_mul_f32 v238, v244, v238\n"
    "v_accvgpr_write_b32 a14, v238\n"
    "v_accvgpr_read_b32 v239, a15\n"
    "v_mul_f32 v239, v244, v239\n"
    "v_accvgpr_write_b32 a15, v239\n"
    "v_accvgpr_read_b32 v236, a16\n"
    "v_mul_f32 v236, v244, v236\n"
    "v_accvgpr_write_b32 a16, v236\n"
    "v_accvgpr_read_b32 v237, a17\n"
    "v_mul_f32 v237, v244, v237\n"
    "v_accvgpr_write_b32 a17, v237\n"
    "v_accvgpr_read_b32 v238, a18\n"
    "v_mul_f32 v238, v244, v238\n"
    "v_accvgpr_write_b32 a18, v238\n"
    "v_accvgpr_read_b32 v239, a19\n"
    "v_mul_f32 v239, v244, v239\n"
    "v_accvgpr_write_b32 a19, v239\n"
    "v_accvgpr_read_b32 v236, a20\n"
    "v_mul_f32 v236, v244, v236\n"
    "v_accvgpr_write_b32 a20, v236\n"
    "v_accvgpr_read_b32 v237, a21\n"
    "v_mul_f32 v237, v244, v237\n"
    "v_accvgpr_write_b32 a21, v237\n"
    "v_accvgpr_read_b32 v238, a22\n"
    "v_mul_f32 v238, v244, v238\n"
    "v_accvgpr_write_b32 a22, v238\n"
    "v_accvgpr_read_b32 v239, a23\n"
    "v_mul_f32 v239, v244, v239\n"
    "v_accvgpr_write_b32 a23, v239\n"
    "v_accvgpr_read_b32 v236, a24\n"
    "v_mul_f32 v236, v244, v236\n"
    "v_accvgpr_write_b32 a24, v236\n"
    "v_accvgpr_read_b32 v237, a25\n"
    "v_mul_f32 v237, v244, v237\n"
    "v_accvgpr_write_b32 a25, v237\n"
    "v_accvgpr_read_b32 v238, a26\n"
    "v_mul_f32 v238, v244, v238\n"
    "v_accvgpr_write_b32 a26, v238\n"
    "v_accvgpr_read_b32 v239, a27\n"
    "v_mul_f32 v239, v244, v239\n"
    "v_accvgpr_write_b32 a27, v239\n"
    "v_accvgpr_read_b32 v236, a28\n"
    "v_mul_f32 v236, v244, v236\n"
    "v_accvgpr_write_b32 a28, v236\n"
    "v_accvgpr_read_b32 v237, a29\n"
    "v_mul_f32 v237, v244, v237\n"
    "v_accvgpr_write_b32 a29, v237\n"
    "v_accvgpr_read_b32 v238, a30\n"
    "v_mul_f32 v238, v244, v238\n"
    "v_accvgpr_write_b32 a30, v238\n"
    "v_accvgpr_read_b32 v239, a31\n"
    "v_mul_f32 v239, v244, v239\n"
    "v_accvgpr_write_b32 a31, v239\n"
    "v_accvgpr_read_b32 v236, a32\n"
    "v_mul_f32 v236, v245, v236\n"
    "v_accvgpr_write_b32 a32, v236\n"
    "v_accvgpr_read_b32 v237, a33\n"
    "v_mul_f32 v237, v245, v237\n"
    "v_accvgpr_write_b32 a33, v237\n"
    "v_accvgpr_read_b32 v238, a34\n"
    "v_mul_f32 v238, v245, v238\n"
    "v_accvgpr_write_b32 a34, v238\n"
    "v_accvgpr_read_b32 v239, a35\n"
    "v_mul_f32 v239, v245, v239\n"
    "v_accvgpr_write_b32 a35, v239\n"
    "v_accvgpr_read_b32 v236, a36\n"
    "v_mul_f32 v236, v245, v236\n"
    "v_accvgpr_write_b32 a36, v236\n"
    "v_accvgpr_read_b32 v237, a37\n"
    "v_mul_f32 v237, v245, v237\n"
    "v_accvgpr_write_b32 a37, v237\n"
    "v_accvgpr_read_b32 v238, a38\n"
    "v_mul_f32 v238, v245, v238\n"
    "v_accvgpr_write_b32 a38, v238\n"
    "v_accvgpr_read_b32 v239, a39\n"
    "v_mul_f32 v239, v245, v239\n"
    "v_accvgpr_write_b32 a39, v239\n"
    "v_accvgpr_read_b32 v236, a40\n"
    "v_mul_f32 v236, v245, v236\n"
    "v_accvgpr_write_b32 a40, v236\n"
    "v_accvgpr_read_b32 v237, a41\n"
    "v_mul_f32 v237, v245, v237\n"
    "v_accvgpr_write_b32 a41, v237\n"
    "v_accvgpr_read_b32 v238, a42\n"
    "v_mul_f32 v238, v245, v238\n"
    "v_accvgpr_write_b32 a42, v238\n"
    "v_accvgpr_read_b32 v239, a43\n"
    "v_mul_f32 v239, v245, v239\n"
    "v_accvgpr_write_b32 a43, v239\n"
    "v_accvgpr_read_b32 v236, a44\n"
    "v_mul_f32 v236, v245, v236\n"
    "v_accvgpr_write_b32 a44, v236\n"
    "v_accvgpr_read_b32 v237, a45\n"
    "v_mul_f32 v237, v245, v237\n"
    "v_accvgpr_write_b32 a45, v237\n"
    "v_accvgpr_read_b32 v238, a46\n"
    "v_mul_f32 v238, v245, v238\n"
    "v_accvgpr_write_b32 a46, v238\n"
    "v_accvgpr_read_b32 v239, a47\n"
    "v_mul_f32 v239, v245, v239\n"
    "v_accvgpr_write_b32 a47, v239\n"
    "v_accvgpr_read_b32 v236, a48\n"
    "v_mul_f32 v236, v245, v236\n"
    "v_accvgpr_write_b32 a48, v236\n"
    "v_accvgpr_read_b32 v237, a49\n"
    "v_mul_f32 v237, v245, v237\n"
    "v_accvgpr_write_b32 a49, v237\n"
    "v_accvgpr_read_b32 v238, a50\n"
    "v_mul_f32 v238, v245, v238\n"
    "v_accvgpr_write_b32 a50, v238\n"
    "v_accvgpr_read_b32 v239, a51\n"
    "v_mul_f32 v239, v245, v239\n"
    "v_accvgpr_write_b32 a51, v239\n"
    "v_accvgpr_read_b32 v236, a52\n"
    "v_mul_f32 v236, v245, v236\n"
    "v_accvgpr_write_b32 a52, v236\n"
    "v_accvgpr_read_b32 v237, a53\n"
    "v_mul_f32 v237, v245, v237\n"
    "v_accvgpr_write_b32 a53, v237\n"
    "v_accvgpr_read_b32 v238, a54\n"
    "v_mul_f32 v238, v245, v238\n"
    "v_accvgpr_write_b32 a54, v238\n"
    "v_accvgpr_read_b32 v239, a55\n"
    "v_mul_f32 v239, v245, v239\n"
    "v_accvgpr_write_b32 a55, v239\n"
    "v_accvgpr_read_b32 v236, a56\n"
    "v_mul_f32 v236, v245, v236\n"
    "v_accvgpr_write_b32 a56, v236\n"
    "v_accvgpr_read_b32 v237, a57\n"
    "v_mul_f32 v237, v245, v237\n"
    "v_accvgpr_write_b32 a57, v237\n"
    "v_accvgpr_read_b32 v238, a58\n"
    "v_mul_f32 v238, v245, v238\n"
    "v_accvgpr_write_b32 a58, v238\n"
    "v_accvgpr_read_b32 v239, a59\n"
    "v_mul_f32 v239, v245, v239\n"
    "v_accvgpr_write_b32 a59, v239\n"
    "v_accvgpr_read_b32 v236, a60\n"
    "v_mul_f32 v236, v245, v236\n"
    "v_accvgpr_write_b32 a60, v236\n"
    "v_accvgpr_read_b32 v237, a61\n"
    "v_mul_f32 v237, v245, v237\n"
    "v_accvgpr_write_b32 a61, v237\n"
    "v_accvgpr_read_b32 v238, a62\n"
    "v_mul_f32 v238, v245, v238\n"
    "v_accvgpr_write_b32 a62, v238\n"
    "v_accvgpr_read_b32 v239, a63\n"
    "v_mul_f32 v239, v245, v239\n"
    "v_accvgpr_write_b32 a63, v239\n"
    "v_accvgpr_read_b32 v236, a64\n"
    "v_mul_f32 v236, v246, v236\n"
    "v_accvgpr_write_b32 a64, v236\n"
    "v_accvgpr_read_b32 v237, a65\n"
    "v_mul_f32 v237, v246, v237\n"
    "v_accvgpr_write_b32 a65, v237\n"
    "v_accvgpr_read_b32 v238, a66\n"
    "v_mul_f32 v238, v246, v238\n"
    "v_accvgpr_write_b32 a66, v238\n"
    "v_accvgpr_read_b32 v239, a67\n"
    "v_mul_f32 v239, v246, v239\n"
    "v_accvgpr_write_b32 a67, v239\n"
    "v_accvgpr_read_b32 v236, a68\n"
    "v_mul_f32 v236, v246, v236\n"
    "v_accvgpr_write_b32 a68, v236\n"
    "v_accvgpr_read_b32 v237, a69\n"
    "v_mul_f32 v237, v246, v237\n"
    "v_accvgpr_write_b32 a69, v237\n"
    "v_accvgpr_read_b32 v238, a70\n"
    "v_mul_f32 v238, v246, v238\n"
    "v_accvgpr_write_b32 a70, v238\n"
    "v_accvgpr_read_b32 v239, a71\n"
    "v_mul_f32 v239, v246, v239\n"
    "v_accvgpr_write_b32 a71, v239\n"
    "v_accvgpr_read_b32 v236, a72\n"
    "v_mul_f32 v236, v246, v236\n"
    "v_accvgpr_write_b32 a72, v236\n"
    "v_accvgpr_read_b32 v237, a73\n"
    "v_mul_f32 v237, v246, v237\n"
    "v_accvgpr_write_b32 a73, v237\n"
    "v_accvgpr_read_b32 v238, a74\n"
    "v_mul_f32 v238, v246, v238\n"
    "v_accvgpr_write_b32 a74, v238\n"
    "v_accvgpr_read_b32 v239, a75\n"
    "v_mul_f32 v239, v246, v239\n"
    "v_accvgpr_write_b32 a75, v239\n"
    "v_accvgpr_read_b32 v236, a76\n"
    "v_mul_f32 v236, v246, v236\n"
    "v_accvgpr_write_b32 a76, v236\n"
    "v_accvgpr_read_b32 v237, a77\n"
    "v_mul_f32 v237, v246, v237\n"
    "v_accvgpr_write_b32 a77, v237\n"
    "v_accvgpr_read_b32 v238, a78\n"
    "v_mul_f32 v238, v246, v238\n"
    "v_accvgpr_write_b32 a78, v238\n"
    "v_accvgpr_read_b32 v239, a79\n"
    "v_mul_f32 v239, v246, v239\n"
    "v_accvgpr_write_b32 a79, v239\n"
    "v_accvgpr_read_b32 v236, a80\n"
    "v_mul_f32 v236, v246, v236\n"
    "v_accvgpr_write_b32 a80, v236\n"
    "v_accvgpr_read_b32 v237, a81\n"
    "v_mul_f32 v237, v246, v237\n"
    "v_accvgpr_write_b32 a81, v237\n"
    "v_accvgpr_read_b32 v238, a82\n"
    "v_mul_f32 v238, v246, v238\n"
    "v_accvgpr_write_b32 a82, v238\n"
    "v_accvgpr_read_b32 v239, a83\n"
    "v_mul_f32 v239, v246, v239\n"
    "v_accvgpr_write_b32 a83, v239\n"
    "v_accvgpr_read_b32 v236, a84\n"
    "v_mul_f32 v236, v246, v236\n"
    "v_accvgpr_write_b32 a84, v236\n"
    "v_accvgpr_read_b32 v237, a85\n"
    "v_mul_f32 v237, v246, v237\n"
    "v_accvgpr_write_b32 a85, v237\n"
    "v_accvgpr_read_b32 v238, a86\n"
    "v_mul_f32 v238, v246, v238\n"
    "v_accvgpr_write_b32 a86, v238\n"
    "v_accvgpr_read_b32 v239, a87\n"
    "v_mul_f32 v239, v246, v239\n"
    "v_accvgpr_write_b32 a87, v239\n"
    "v_accvgpr_read_b32 v236, a88\n"
    "v_mul_f32 v236, v246, v236\n"
    "v_accvgpr_write_b32 a88, v236\n"
    "v_accvgpr_read_b32 v237, a89\n"
    "v_mul_f32 v237, v246, v237\n"
    "v_accvgpr_write_b32 a89, v237\n"
    "v_accvgpr_read_b32 v238, a90\n"
    "v_mul_f32 v238, v246, v238\n"
    "v_accvgpr_write_b32 a90, v238\n"
    "v_accvgpr_read_b32 v239, a91\n"
    "v_mul_f32 v239, v246, v239\n"
    "v_accvgpr_write_b32 a91, v239\n"
    "v_accvgpr_read_b32 v236, a92\n"
    "v_mul_f32 v236, v246, v236\n"
    "v_accvgpr_write_b32 a92, v236\n"
    "v_accvgpr_read_b32 v237, a93\n"
    "v_mul_f32 v237, v246, v237\n"
    "v_accvgpr_write_b32 a93, v237\n"
    "v_accvgpr_read_b32 v238, a94\n"
    "v_mul_f32 v238, v246, v238\n"
    "v_accvgpr_write_b32 a94, v238\n"
    "v_accvgpr_read_b32 v239, a95\n"
    "v_mul_f32 v239, v246, v239\n"
    "v_accvgpr_write_b32 a95, v239\n"
    "v_accvgpr_read_b32 v236, a96\n"
    "v_mul_f32 v236, v247, v236\n"
    "v_accvgpr_write_b32 a96, v236\n"
    "v_accvgpr_read_b32 v237, a97\n"
    "v_mul_f32 v237, v247, v237\n"
    "v_accvgpr_write_b32 a97, v237\n"
    "v_accvgpr_read_b32 v238, a98\n"
    "v_mul_f32 v238, v247, v238\n"
    "v_accvgpr_write_b32 a98, v238\n"
    "v_accvgpr_read_b32 v239, a99\n"
    "v_mul_f32 v239, v247, v239\n"
    "v_accvgpr_write_b32 a99, v239\n"
    "v_accvgpr_read_b32 v236, a100\n"
    "v_mul_f32 v236, v247, v236\n"
    "v_accvgpr_write_b32 a100, v236\n"
    "v_accvgpr_read_b32 v237, a101\n"
    "v_mul_f32 v237, v247, v237\n"
    "v_accvgpr_write_b32 a101, v237\n"
    "v_accvgpr_read_b32 v238, a102\n"
    "v_mul_f32 v238, v247, v238\n"
    "v_accvgpr_write_b32 a102, v238\n"
    "v_accvgpr_read_b32 v239, a103\n"
    "v_mul_f32 v239, v247, v239\n"
    "v_accvgpr_write_b32 a103, v239\n"
    "v_accvgpr_read_b32 v236, a104\n"
    "v_mul_f32 v236, v247, v236\n"
    "v_accvgpr_write_b32 a104, v236\n"
    "v_accvgpr_read_b32 v237, a105\n"
    "v_mul_f32 v237, v247, v237\n"
    "v_accvgpr_write_b32 a105, v237\n"
    "v_accvgpr_read_b32 v238, a106\n"
    "v_mul_f32 v238, v247, v238\n"
    "v_accvgpr_write_b32 a106, v238\n"
    "v_accvgpr_read_b32 v239, a107\n"
    "v_mul_f32 v239, v247, v239\n"
    "v_accvgpr_write_b32 a107, v239\n"
    "v_accvgpr_read_b32 v236, a108\n"
    "v_mul_f32 v236, v247, v236\n"
    "v_accvgpr_write_b32 a108, v236\n"
    "v_accvgpr_read_b32 v237, a109\n"
    "v_mul_f32 v237, v247, v237\n"
    "v_accvgpr_write_b32 a109, v237\n"
    "v_accvgpr_read_b32 v238, a110\n"
    "v_mul_f32 v238, v247, v238\n"
    "v_accvgpr_write_b32 a110, v238\n"
    "v_accvgpr_read_b32 v239, a111\n"
    "v_mul_f32 v239, v247, v239\n"
    "v_accvgpr_write_b32 a111, v239\n"
    "v_accvgpr_read_b32 v236, a112\n"
    "v_mul_f32 v236, v247, v236\n"
    "v_accvgpr_write_b32 a112, v236\n"
    "v_accvgpr_read_b32 v237, a113\n"
    "v_mul_f32 v237, v247, v237\n"
    "v_accvgpr_write_b32 a113, v237\n"
    "v_accvgpr_read_b32 v238, a114\n"
    "v_mul_f32 v238, v247, v238\n"
    "v_accvgpr_write_b32 a114, v238\n"
    "v_accvgpr_read_b32 v239, a115\n"
    "v_mul_f32 v239, v247, v239\n"
    "v_accvgpr_write_b32 a115, v239\n"
    "v_accvgpr_read_b32 v236, a116\n"
    "v_mul_f32 v236, v247, v236\n"
    "v_accvgpr_write_b32 a116, v236\n"
    "v_accvgpr_read_b32 v237, a117\n"
    "v_mul_f32 v237, v247, v237\n"
    "v_accvgpr_write_b32 a117, v237\n"
    "v_accvgpr_read_b32 v238, a118\n"
    "v_mul_f32 v238, v247, v238\n"
    "v_accvgpr_write_b32 a118, v238\n"
    "v_accvgpr_read_b32 v239, a119\n"
    "v_mul_f32 v239, v247, v239\n"
    "v_accvgpr_write_b32 a119, v239\n"
    "v_accvgpr_read_b32 v236, a120\n"
    "v_mul_f32 v236, v247, v236\n"
    "v_accvgpr_write_b32 a120, v236\n"
    "v_accvgpr_read_b32 v237, a121\n"
    "v_mul_f32 v237, v247, v237\n"
    "v_accvgpr_write_b32 a121, v237\n"
    "v_accvgpr_read_b32 v238, a122\n"
    "v_mul_f32 v238, v247, v238\n"
    "v_accvgpr_write_b32 a122, v238\n"
    "v_accvgpr_read_b32 v239, a123\n"
    "v_mul_f32 v239, v247, v239\n"
    "v_accvgpr_write_b32 a123, v239\n"
    "v_accvgpr_read_b32 v236, a124\n"
    "v_mul_f32 v236, v247, v236\n"
    "v_accvgpr_write_b32 a124, v236\n"
    "v_accvgpr_read_b32 v237, a125\n"
    "v_mul_f32 v237, v247, v237\n"
    "v_accvgpr_write_b32 a125, v237\n"
    "v_accvgpr_read_b32 v238, a126\n"
    "v_mul_f32 v238, v247, v238\n"
    "v_accvgpr_write_b32 a126, v238\n"
    "v_accvgpr_read_b32 v239, a127\n"
    "v_mul_f32 v239, v247, v239\n"
    "v_accvgpr_write_b32 a127, v239\n"
    "v_mul_f32 v208, v244, v208\n"
    "v_mul_f32 v212, v245, v212\n"
    "v_mul_f32 v216, v246, v216\n"
    "v_mul_f32 v220, v247, v220\n"
    "s_nop 1\n"
    "s_setpc_b64 s[86:87]\n"
    "END%=:\n"
    "s_cmp_lg_u32 s42, 0\n"
    "s_cbranch_scc1 K2R%=\n"
    "K2ZP%=:\n"
    "buffer_load_dword v228, v226, s[48:51], 0 offen sc0 sc1\n"
    "s_sub_u32 s62, s62, 1\ns_cmp_lt_i32 s62, 0\n"
    "s_cbranch_scc1 K2ZQ%=\n"
    "s_waitcnt vmcnt(0)\n"
    "v_and_b32 v228, 1, v228\n"
    "v_cmp_ne_u32 vcc, 0, v228\n"
    "s_xor_b32 s60, vcc_lo, s64\n"
    "s_andn2_b32 s60, 15, s60\n"
    "s_cbranch_scc1 K2ZP%=\n"
    "K2ZQ%=:\n"
    "s_mov_b32 s42, 425984\n"
    "s_mov_b32 s57, 0\n"
    "buffer_load_dwordx4 v[0:3], v224, s[40:43], s57 offen offset:0 sc1\n"
    "buffer_load_dwordx4 v[4:7], v225, s[40:43], s57 offen offset:0 sc1\n"
    "buffer_load_dwordx4 v[8:11], v227, s[40:43], s57 offen offset:0 sc1\n"
    "buffer_load_dwordx4 v[12:15], v253, s[40:43], s57 offen offset:0 sc1\n"
    "buffer_load_dwordx4 v[16:19], v224, s[40:43], s57 offen offset:1024 sc1\n"
    "buffer_load_dwordx4 v[20:23], v225, s[40:43], s57 offen offset:1024 sc1\n"
    "buffer_load_dwordx4 v[24:27], v227, s[40:43], s57 offen offset:1024 sc1\n"
    "buffer_load_dwordx4 v[28:31], v253, s[40:43], s57 offen offset:1024 sc1\n"
    "buffer_load_dwordx4 v[32:35], v224, s[40:43], s57 offen offset:2048 sc1\n"
    "buffer_load_dwordx4 v[36:39], v225, s[40:43], s57 offen offset:2048 sc1\n"
    "buffer_load_dwordx4 v[40:43], v227, s[40:43], s57 offen offset:2048 sc1\n"
    "buffer_load_dwordx4 v[44:47], v253, s[40:43], s57 offen offset:2048 sc1\n"
    "buffer_load_dwordx4 v[48:51], v224, s[40:43], s57 offen offset:3072 sc1\n"
    "buffer_load_dwordx4 v[52:55], v225, s[40:43], s57 offen offset:3072 sc1\n"
    "buffer_load_dwordx4 v[56:59], v227, s[40:43], s57 offen offset:3072 sc1\n"
    "buffer_load_dwordx4 v[60:63], v253, s[40:43], s57 offen offset:3072 sc1\n"
    "s_add_u32 s57, s57, 8192\n"
    "K2R%=:\n"
    "buffer_load_dwordx4 v[64:67], v224, s[40:43], s57 offen offset:0 sc1\n"
    "buffer_load_dwordx4 v[68:71], v225, s[40:43], s57 offen offset:0 sc1\n"
    "buffer_load_dwordx4 v[72:75], v227, s[40:43], s57 offen offset:0 sc1\n"
    "buffer_load_dwordx4 v[76:79], v253, s[40:43], s57 offen offset:0 sc1\n"
    "buffer_load_dwordx4 v[80:83], v224, s[40:43], s57 offen offset:1024 sc1\n"
    "buffer_load_dwordx4 v[84:87], v225, s[40:43], s57 offen offset:1024 sc1\n"
    "buffer_load_dwordx4 v[88:91], v227, s[40:43], s57 offen offset:1024 sc1\n"
    "buffer_load_dwordx4 v[92:95], v253, s[40:43], s57 offen offset:1024 sc1\n"
    "v_lshrrev_b32 v128, 3, v230\n"
    "v_and_b32 v128, 2, v128\n"
    "v_lshrrev_b32 v129, 5, v230\n"
    "v_mul_u32_u24 v129, 3, v129\n"
    "v_xor_b32 v128, v128, v129\n"
    "v_and_b32 v129, 3, v230\n"
    "v_xor_b32 v128, v128, v129\n"
    "v_lshlrev_b32 v128, 4, v128\n"
    "v_lshrrev_b32 v129, 2, v230\n"
    "v_lshl_add_u32 v221, v129, 6, v128\n"
    "v_add_u32 v221, %8, v221\n"
    "v_add_u32 v222, 61440, v221\n"
    "v_mov_b32 v216, v229\n"
    "v_accvgpr_read_b32 v128, a0\n"
    "v_accvgpr_read_b32 v129, a1\n"
    "v_accvgpr_read_b32 v130, a2\n"
    "v_accvgpr_read_b32 v131, a3\n"
    "v_accvgpr_read_b32 v132, a4\n"
    "v_accvgpr_read_b32 v133, a5\n"
    "v_accvgpr_read_b32 v134, a6\n"
    "v_accvgpr_read_b32 v135, a7\n"
    "v_pk_mul_f32 v[128:129], v[128:129], v[236:237] op_sel:[0,0] op_sel_hi:[1,0]\n"
    "v_pk_mul_f32 v[130:131], v[130:131], v[236:237] op_sel:[0,0] op_sel_hi:[1,0]\n"
    "v_pk_mul_f32 v[132:133], v[132:133], v[236:237] op_sel:[0,0] op_sel_hi:[1,0]\n"
    "v_pk_mul_f32 v[134:135], v[134:135], v[236:237] op_sel:[0,0] op_sel_hi:[1,0]\n"
    "v_cvt_pk_bf16_f32 v128, v128, v129\n"
    "v_cvt_pk_bf16_f32 v129, v130, v131\n"
    "v_cvt_pk_bf16_f32 v130, v132, v133\n"
    "v_cvt_pk_bf16_f32 v131, v134, v135\n"
    "buffer_store_dwordx4 v[128:131], v229, s[52:55], s82 offen offset:0 sc0 sc1\n"
    "v_accvgpr_read_b32 v136, a8\n"
    "v_accvgpr_read_b32 v137, a9\n"
    "v_accvgpr_read_b32 v138, a10\n"
    "v_accvgpr_read_b32 v139, a11\n"
    "v_accvgpr_read_b32 v140, a12\n"
    "v_accvgpr_read_b32 v141, a13\n"
    "v_accvgpr_read_b32 v142, a14\n"
    "v_accvgpr_read_b32 v143, a15\n"
    "v_pk_mul_f32 v[136:137], v[136:137], v[236:237] op_sel:[0,0] op_sel_hi:[1,0]\n"
    "v_pk_mul_f32 v[138:139], v[138:139], v[236:237] op_sel:[0,0] op_sel_hi:[1,0]\n"
    "v_pk_mul_f32 v[140:141], v[140:141], v[236:237] op_sel:[0,0] op_sel_hi:[1,0]\n"
    "v_pk_mul_f32 v[142:143], v[142:143], v[236:237] op_sel:[0,0] op_sel_hi:[1,0]\n"
    "v_cvt_pk_bf16_f32 v136, v136, v137\n"
    "v_cvt_pk_bf16_f32 v137, v138, v139\n"
    "v_cvt_pk_bf16_f32 v138, v140, v141\n"
    "v_cvt_pk_bf16_f32 v139, v142, v143\n"
    "buffer_store_dwordx4 v[136:139], v229, s[52:55], s82 offen offset:1024 sc0 sc1\n"
    "v_accvgpr_read_b32 v144, a16\n"
    "v_accvgpr_read_b32 v145, a17\n"
    "v_accvgpr_read_b32 v146, a18\n"
    "v_accvgpr_read_b32 v147, a19\n"
    "v_accvgpr_read_b32 v148, a20\n"
    "v_accvgpr_read_b32 v149, a21\n"
    "v_accvgpr_read_b32 v150, a22\n"
    "v_accvgpr_read_b32 v151, a23\n"
    "v_pk_mul_f32 v[144:145], v[144:145], v[236:237] op_sel:[0,0] op_sel_hi:[1,0]\n"
    "v_pk_mul_f32 v[146:147], v[146:147], v[236:237] op_sel:[0,0] op_sel_hi:[1,0]\n"
    "v_pk_mul_f32 v[148:149], v[148:149], v[236:237] op_sel:[0,0] op_sel_hi:[1,0]\n"
    "v_pk_mul_f32 v[150:151], v[150:151], v[236:237] op_sel:[0,0] op_sel_hi:[1,0]\n"
    "v_cvt_pk_bf16_f32 v144, v144, v145\n"
    "v_cvt_pk_bf16_f32 v145, v146, v147\n"
    "v_cvt_pk_bf16_f32 v146, v148, v149\n"
    "v_cvt_pk_bf16_f32 v147, v150, v151\n"
    "buffer_store_dwordx4 v[144:147], v229, s[52:55], s82 offen offset:2048 sc0 sc1\n"
    "v_accvgpr_read_b32 v152, a24\n"
    "v_accvgpr_read_b32 v153, a25\n"
    "v_accvgpr_read_b32 v154, a26\n"
    "v_accvgpr_read_b32 v155, a27\n"
    "v_accvgpr_read_b32 v156, a28\n"
    "v_accvgpr_read_b32 v157, a29\n"
    "v_accvgpr_read_b32 v158, a30\n"
    "v_accvgpr_read_b32 v159, a31\n"
    "v_pk_mul_f32 v[152:153], v[152:153], v[236:237] op_sel:[0,0] op_sel_hi:[1,0]\n"
    "v_pk_mul_f32 v[154:155], v[154:155], v[236:237] op_sel:[0,0] op_sel_hi:[1,0]\n"
    "v_pk_mul_f32 v[156:157], v[156:157], v[236:237] op_sel:[0,0] op_sel_hi:[1,0]\n"
    "v_pk_mul_f32 v[158:159], v[158:159], v[236:237] op_sel:[0,0] op_sel_hi:[1,0]\n"
    "v_cvt_pk_bf16_f32 v152, v152, v153\n"
    "v_cvt_pk_bf16_f32 v153, v154, v155\n"
    "v_cvt_pk_bf16_f32 v154, v156, v157\n"
    "v_cvt_pk_bf16_f32 v155, v158, v159\n"
    "buffer_store_dwordx4 v[152:155], v229, s[52:55], s82 offen offset:3072 sc0 sc1\n"
    "s_add_u32 s82, s82, 131072\n"
    "v_accvgpr_read_b32 v160, a32\n"
    "v_accvgpr_read_b32 v161, a33\n"
    "v_accvgpr_read_b32 v162, a34\n"
    "v_accvgpr_read_b32 v163, a35\n"
    "v_accvgpr_read_b32 v164, a36\n"
    "v_accvgpr_read_b32 v165, a37\n"
    "v_accvgpr_read_b32 v166, a38\n"
    "v_accvgpr_read_b32 v167, a39\n"
    "v_pk_mul_f32 v[160:161], v[160:161], v[236:237] op_sel:[0,1] op_sel_hi:[1,1]\n"
    "v_pk_mul_f32 v[162:163], v[162:163], v[236:237] op_sel:[0,1] op_sel_hi:[1,1]\n"
    "v_pk_mul_f32 v[164:165], v[164:165], v[236:237] op_sel:[0,1] op_sel_hi:[1,1]\n"
    "v_pk_mul_f32 v[166:167], v[166:167], v[236:237] op_sel:[0,1] op_sel_hi:[1,1]\n"
    "v_cvt_pk_bf16_f32 v160, v160, v161\n"
    "v_cvt_pk_bf16_f32 v161, v162, v163\n"
    "v_cvt_pk_bf16_f32 v162, v164, v165\n"
    "v_cvt_pk_bf16_f32 v163, v166, v167\n"
    "buffer_store_dwordx4 v[160:163], v229, s[52:55], s82 offen offset:0 sc0 sc1\n"
    "v_accvgpr_read_b32 v168, a40\n"
    "v_accvgpr_read_b32 v169, a41\n"
    "v_accvgpr_read_b32 v170, a42\n"
    "v_accvgpr_read_b32 v171, a43\n"
    "v_accvgpr_read_b32 v172, a44\n"
    "v_accvgpr_read_b32 v173, a45\n"
    "v_accvgpr_read_b32 v174, a46\n"
    "v_accvgpr_read_b32 v175, a47\n"
    "v_pk_mul_f32 v[168:169], v[168:169], v[236:237] op_sel:[0,1] op_sel_hi:[1,1]\n"
    "v_pk_mul_f32 v[170:171], v[170:171], v[236:237] op_sel:[0,1] op_sel_hi:[1,1]\n"
    "v_pk_mul_f32 v[172:173], v[172:173], v[236:237] op_sel:[0,1] op_sel_hi:[1,1]\n"
    "v_pk_mul_f32 v[174:175], v[174:175], v[236:237] op_sel:[0,1] op_sel_hi:[1,1]\n"
    "v_cvt_pk_bf16_f32 v168, v168, v169\n"
    "v_cvt_pk_bf16_f32 v169, v170, v171\n"
    "v_cvt_pk_bf16_f32 v170, v172, v173\n"
    "v_cvt_pk_bf16_f32 v171, v174, v175\n"
    "buffer_store_dwordx4 v[168:171], v229, s[52:55], s82 offen offset:1024 sc0 sc1\n"
    "v_accvgpr_read_b32 v176, a48\n"
    "v_accvgpr_read_b32 v177, a49\n"
    "v_accvgpr_read_b32 v178, a50\n"
    "v_accvgpr_read_b32 v179, a51\n"
    "v_accvgpr_read_b32 v180, a52\n"
    "v_accvgpr_read_b32 v181, a53\n"
    "v_accvgpr_read_b32 v182, a54\n"
    "v_accvgpr_read_b32 v183, a55\n"
    "v_pk_mul_f32 v[176:177], v[176:177], v[236:237] op_sel:[0,1] op_sel_hi:[1,1]\n"
    "v_pk_mul_f32 v[178:179], v[178:179], v[236:237] op_sel:[0,1] op_sel_hi:[1,1]\n"
    "v_pk_mul_f32 v[180:181], v[180:181], v[236:237] op_sel:[0,1] op_sel_hi:[1,1]\n"
    "v_pk_mul_f32 v[182:183], v[182:183], v[236:237] op_sel:[0,1] op_sel_hi:[1,1]\n"
    "v_cvt_pk_bf16_f32 v176, v176, v177\n"
    "v_cvt_pk_bf16_f32 v177, v178, v179\n"
    "v_cvt_pk_bf16_f32 v178, v180, v181\n"
    "v_cvt_pk_bf16_f32 v179, v182, v183\n"
    "buffer_store_dwordx4 v[176:179], v229, s[52:55], s82 offen offset:2048 sc0 sc1\n"
    "v_accvgpr_read_b32 v184, a56\n"
    "v_accvgpr_read_b32 v185, a57\n"
    "v_accvgpr_read_b32 v186, a58\n"
    "v_accvgpr_read_b32 v187, a59\n"
    "v_accvgpr_read_b32 v188, a60\n"
    "v_accvgpr_read_b32 v189, a61\n"
    "v_accvgpr_read_b32 v190, a62\n"
    "v_accvgpr_read_b32 v191, a63\n"
    "v_pk_mul_f32 v[184:185], v[184:185], v[236:237] op_sel:[0,1] op_sel_hi:[1,1]\n"
    "v_pk_mul_f32 v[186:187], v[186:187], v[236:237] op_sel:[0,1] op_sel_hi:[1,1]\n"
    "v_pk_mul_f32 v[188:189], v[188:189], v[236:237] op_sel:[0,1] op_sel_hi:[1,1]\n"
    "v_pk_mul_f32 v[190:191], v[190:191], v[236:237] op_sel:[0,1] op_sel_hi:[1,1]\n"
    "v_cvt_pk_bf16_f32 v184, v184, v185\n"
    "v_cvt_pk_bf16_f32 v185, v186, v187\n"
    "v_cvt_pk_bf16_f32 v186, v188, v189\n"
    "v_cvt_pk_bf16_f32 v187, v190, v191\n"
    "buffer_store_dwordx4 v[184:187], v229, s[52:55], s82 offen offset:3072 sc0 sc1\n"
    "s_add_u32 s82, s82, 131072\n"
    "v_accvgpr_read_b32 v128, a64\n"
    "v_accvgpr_read_b32 v129, a65\n"
    "v_accvgpr_read_b32 v130, a66\n"
    "v_accvgpr_read_b32 v131, a67\n"
    "v_accvgpr_read_b32 v132, a68\n"
    "v_accvgpr_read_b32 v133, a69\n"
    "v_accvgpr_read_b32 v134, a70\n"
    "v_accvgpr_read_b32 v135, a71\n"
    "v_pk_mul_f32 v[128:129], v[128:129], v[238:239] op_sel:[0,0] op_sel_hi:[1,0]\n"
    "v_pk_mul_f32 v[130:131], v[130:131], v[238:239] op_sel:[0,0] op_sel_hi:[1,0]\n"
    "v_pk_mul_f32 v[132:133], v[132:133], v[238:239] op_sel:[0,0] op_sel_hi:[1,0]\n"
    "v_pk_mul_f32 v[134:135], v[134:135], v[238:239] op_sel:[0,0] op_sel_hi:[1,0]\n"
    "v_cvt_pk_bf16_f32 v128, v128, v129\n"
    "v_cvt_pk_bf16_f32 v129, v130, v131\n"
    "v_cvt_pk_bf16_f32 v130, v132, v133\n"
    "v_cvt_pk_bf16_f32 v131, v134, v135\n"
    "buffer_store_dwordx4 v[128:131], v229, s[52:55], s82 offen offset:0 sc0 sc1\n"
    "v_accvgpr_read_b32 v136, a72\n"
    "v_accvgpr_read_b32 v137, a73\n"
    "v_accvgpr_read_b32 v138, a74\n"
    "v_accvgpr_read_b32 v139, a75\n"
    "v_accvgpr_read_b32 v140, a76\n"
    "v_accvgpr_read_b32 v141, a77\n"
    "v_accvgpr_read_b32 v142, a78\n"
    "v_accvgpr_read_b32 v143, a79\n"
    "v_pk_mul_f32 v[136:137], v[136:137], v[238:239] op_sel:[0,0] op_sel_hi:[1,0]\n"
    "v_pk_mul_f32 v[138:139], v[138:139], v[238:239] op_sel:[0,0] op_sel_hi:[1,0]\n"
    "v_pk_mul_f32 v[140:141], v[140:141], v[238:239] op_sel:[0,0] op_sel_hi:[1,0]\n"
    "v_pk_mul_f32 v[142:143], v[142:143], v[238:239] op_sel:[0,0] op_sel_hi:[1,0]\n"
    "v_cvt_pk_bf16_f32 v136, v136, v137\n"
    "v_cvt_pk_bf16_f32 v137, v138, v139\n"
    "v_cvt_pk_bf16_f32 v138, v140, v141\n"
    "v_cvt_pk_bf16_f32 v139, v142, v143\n"
    "buffer_store_dwordx4 v[136:139], v229, s[52:55], s82 offen offset:1024 sc0 sc1\n"
    "v_accvgpr_read_b32 v144, a80\n"
    "v_accvgpr_read_b32 v145, a81\n"
    "v_accvgpr_read_b32 v146, a82\n"
    "v_accvgpr_read_b32 v147, a83\n"
    "v_accvgpr_read_b32 v148, a84\n"
    "v_accvgpr_read_b32 v149, a85\n"
    "v_accvgpr_read_b32 v150, a86\n"
    "v_accvgpr_read_b32 v151, a87\n"
    "v_pk_mul_f32 v[144:145], v[144:145], v[238:239] op_sel:[0,0] op_sel_hi:[1,0]\n"
    "v_pk_mul_f32 v[146:147], v[146:147], v[238:239] op_sel:[0,0] op_sel_hi:[1,0]\n"
    "v_pk_mul_f32 v[148:149], v[148:149], v[238:239] op_sel:[0,0] op_sel_hi:[1,0]\n"
    "v_pk_mul_f32 v[150:151], v[150:151], v[238:239] op_sel:[0,0] op_sel_hi:[1,0]\n"
    "v_cvt_pk_bf16_f32 v144, v144, v145\n"
    "v_cvt_pk_bf16_f32 v145, v146, v147\n"
    "v_cvt_pk_bf16_f32 v146, v148, v149\n"
    "v_cvt_pk_bf16_f32 v147, v150, v151\n"
    "buffer_store_dwordx4 v[144:147], v229, s[52:55], s82 offen offset:2048 sc0 sc1\n"
    "v_accvgpr_read_b32 v152, a88\n"
    "v_accvgpr_read_b32 v153, a89\n"
    "v_accvgpr_read_b32 v154, a90\n"
    "v_accvgpr_read_b32 v155, a91\n"
    "v_accvgpr_read_b32 v156, a92\n"
    "v_accvgpr_read_b32 v157, a93\n"
    "v_accvgpr_read_b32 v158, a94\n"
    "v_accvgpr_read_b32 v159, a95\n"
    "v_pk_mul_f32 v[152:153], v[152:153], v[238:239] op_sel:[0,0] op_sel_hi:[1,0]\n"
    "v_pk_mul_f32 v[154:155], v[154:155], v[238:239] op_sel:[0,0] op_sel_hi:[1,0]\n"
    "v_pk_mul_f32 v[156:157], v[156:157], v[238:239] op_sel:[0,0] op_sel_hi:[1,0]\n"
    "v_pk_mul_f32 v[158:159], v[158:159], v[238:239] op_sel:[0,0] op_sel_hi:[1,0]\n"
    "v_cvt_pk_bf16_f32 v152, v152, v153\n"
    "v_cvt_pk_bf16_f32 v153, v154, v155\n"
    "v_cvt_pk_bf16_f32 v154, v156, v157\n"
    "v_cvt_pk_bf16_f32 v155, v158, v159\n"
    "buffer_store_dwordx4 v[152:155], v229, s[52:55], s82 offen offset:3072 sc0 sc1\n"
    "s_add_u32 s82, s82, 131072\n"
    "v_accvgpr_read_b32 v160, a96\n"
    "v_accvgpr_read_b32 v161, a97\n"
    "v_accvgpr_read_b32 v162, a98\n"
    "v_accvgpr_read_b32 v163, a99\n"
    "v_accvgpr_read_b32 v164, a100\n"
    "v_accvgpr_read_b32 v165, a101\n"
    "v_accvgpr_read_b32 v166, a102\n"
    "v_accvgpr_read_b32 v167, a103\n"
    "v_pk_mul_f32 v[160:161], v[160:161], v[238:239] op_sel:[0,1] op_sel_hi:[1,1]\n"
    "v_pk_mul_f32 v[162:163], v[162:163], v[238:239] op_sel:[0,1] op_sel_hi:[1,1]\n"
    "v_pk_mul_f32 v[164:165], v[164:165], v[238:239] op_sel:[0,1] op_sel_hi:[1,1]\n"
    "v_pk_mul_f32 v[166:167], v[166:167], v[238:239] op_sel:[0,1] op_sel_hi:[1,1]\n"
    "v_cvt_pk_bf16_f32 v160, v160, v161\n"
    "v_cvt_pk_bf16_f32 v161, v162, v163\n"
    "v_cvt_pk_bf16_f32 v162, v164, v165\n"
    "v_cvt_pk_bf16_f32 v163, v166, v167\n"
    "buffer_store_dwordx4 v[160:163], v229, s[52:55], s82 offen offset:0 sc0 sc1\n"
    "v_accvgpr_read_b32 v168, a104\n"
    "v_accvgpr_read_b32 v169, a105\n"
    "v_accvgpr_read_b32 v170, a106\n"
    "v_accvgpr_read_b32 v171, a107\n"
    "v_accvgpr_read_b32 v172, a108\n"
    "v_accvgpr_read_b32 v173, a109\n"
    "v_accvgpr_read_b32 v174, a110\n"
    "v_accvgpr_read_b32 v175, a111\n"
    "v_pk_mul_f32 v[168:169], v[168:169], v[238:239] op_sel:[0,1] op_sel_hi:[1,1]\n"
    "v_pk_mul_f32 v[170:171], v[170:171], v[238:239] op_sel:[0,1] op_sel_hi:[1,1]\n"
    "v_pk_mul_f32 v[172:173], v[172:173], v[238:239] op_sel:[0,1] op_sel_hi:[1,1]\n"
    "v_pk_mul_f32 v[174:175], v[174:175], v[238:239] op_sel:[0,1] op_sel_hi:[1,1]\n"
    "v_cvt_pk_bf16_f32 v168, v168, v169\n"
    "v_cvt_pk_bf16_f32 v169, v170, v171\n"
    "v_cvt_pk_bf16_f32 v170, v172, v173\n"
    "v_cvt_pk_bf16_f32 v171, v174, v175\n"
    "buffer_store_dwordx4 v[168:171], v229, s[52:55], s82 offen offset:1024 sc0 sc1\n"
    "v_accvgpr_read_b32 v176, a112\n"
    "v_accvgpr_read_b32 v177, a113\n"
    "v_accvgpr_read_b32 v178, a114\n"
    "v_accvgpr_read_b32 v179, a115\n"
    "v_accvgpr_read_b32 v180, a116\n"
    "v_accvgpr_read_b32 v181, a117\n"
    "v_accvgpr_read_b32 v182, a118\n"
    "v_accvgpr_read_b32 v183, a119\n"
    "v_pk_mul_f32 v[176:177], v[176:177], v[238:239] op_sel:[0,1] op_sel_hi:[1,1]\n"
    "v_pk_mul_f32 v[178:179], v[178:179], v[238:239] op_sel:[0,1] op_sel_hi:[1,1]\n"
    "v_pk_mul_f32 v[180:181], v[180:181], v[238:239] op_sel:[0,1] op_sel_hi:[1,1]\n"
    "v_pk_mul_f32 v[182:183], v[182:183], v[238:239] op_sel:[0,1] op_sel_hi:[1,1]\n"
    "v_cvt_pk_bf16_f32 v176, v176, v177\n"
    "v_cvt_pk_bf16_f32 v177, v178, v179\n"
    "v_cvt_pk_bf16_f32 v178, v180, v181\n"
    "v_cvt_pk_bf16_f32 v179, v182, v183\n"
    "buffer_store_dwordx4 v[176:179], v229, s[52:55], s82 offen offset:2048 sc0 sc1\n"
    "v_accvgpr_read_b32 v184, a120\n"
    "v_accvgpr_read_b32 v185, a121\n"
    "v_accvgpr_read_b32 v186, a122\n"
    "v_accvgpr_read_b32 v187, a123\n"
    "v_accvgpr_read_b32 v188, a124\n"
    "v_accvgpr_read_b32 v189, a125\n"
    "v_accvgpr_read_b32 v190, a126\n"
    "v_accvgpr_read_b32 v191, a127\n"
    "v_pk_mul_f32 v[184:185], v[184:185], v[238:239] op_sel:[0,1] op_sel_hi:[1,1]\n"
    "v_pk_mul_f32 v[186:187], v[186:187], v[238:239] op_sel:[0,1] op_sel_hi:[1,1]\n"
    "v_pk_mul_f32 v[188:189], v[188:189], v[238:239] op_sel:[0,1] op_sel_hi:[1,1]\n"
    "v_pk_mul_f32 v[190:191], v[190:191], v[238:239] op_sel:[0,1] op_sel_hi:[1,1]\n"
    "v_cvt_pk_bf16_f32 v184, v184, v185\n"
    "v_cvt_pk_bf16_f32 v185, v186, v187\n"
    "v_cvt_pk_bf16_f32 v186, v188, v189\n"
    "v_cvt_pk_bf16_f32 v187, v190, v191\n"
    "buffer_store_dwordx4 v[184:187], v229, s[52:55], s82 offen offset:3072 sc0 sc1\n"
    "s_sub_u32 s82, s82, 389120\n"
    "s_nop 1\n"
    "s_mov_b64 s[44:45], %4\n"
    "s_add_u32 s44, s44, %12\n"
    "s_addc_u32 s45, s45, 0\n"
    "s_and_b32 s45, s45, 0xffff\n"
    "s_mov_b32 s46, 81920\n"
    "s_mov_b32 s47, 0x20000\n"
    "s_mov_b64 s[52:53], %13\n"
    "s_and_b32 s53, s53, 0xffff\n"
    "s_mov_b32 s54, 38720\n"
    "s_mov_b32 s55, 0x20000\n"
    "s_add_u32 s61, s63, 8\n"
    "s_lshr_b32 s60, s64, s61\n"
    "s_andn2_b32 s60, 1, s60\n"
    "v_mov_b32 v255, s60\n"
    "v_add_u32 v223, 4096, v216\n"
    "v_and_b32 v217, 15, v230\n"
    "v_mul_u32_u24 v217, 2560, v217\n"
    "v_lshrrev_b32 v228, 4, v230\n"
    "v_lshl_add_u32 v217, v228, 4, v217\n"
    "v_mov_b32 v232, 0\n"
    "v_mov_b32 v233, 0\n"
    "v_mov_b32 v234, 0\n"
    "v_mov_b32 v235, 0\n"
    "s_nop 1\n"
    "v_mfma_f32_32x32x16_bf16 a[0:15], v[232:235], v[232:235], 0\n"
    "v_mfma_f32_32x32x16_bf16 a[16:31], v[232:235], v[232:235], 0\n"
    "v_mfma_f32_32x32x16_bf16 a[32:47], v[232:235], v[232:235], 0\n"
    "v_mfma_f32_32x32x16_bf16 a[48:63], v[232:235], v[232:235], 0\n"
    "v_mfma_f32_32x32x16_bf16 a[64:79], v[232:235], v[232:235], 0\n"
    "v_mfma_f32_32x32x16_bf16 a[80:95], v[232:235], v[232:235], 0\n"
    "v_mfma_f32_32x32x16_bf16 a[96:111], v[232:235], v[232:235], 0\n"
    "v_mfma_f32_32x32x16_bf16 a[112:127], v[232:235], v[232:235], 0\n"
    "v_mfma_f32_32x32x16_bf16 a[128:143], v[232:235], v[232:235], 0\n"
    "v_mfma_f32_32x32x16_bf16 a[144:159], v[232:235], v[232:235], 0\n"
    "s_waitcnt vmcnt(32)\n"
    "s_barrier\n"
    "ds_read_b64_tr_b16 v[128:129], v221 offset:0\n"
    "ds_read_b64_tr_b16 v[130:131], v221 offset:1024\n"
    "ds_read_b64_tr_b16 v[132:133], v221 offset:8\n"
    "ds_read_b64_tr_b16 v[134:135], v221 offset:1032\n"
    "ds_read_b64_tr_b16 v[136:137], v221 offset:2048\n"
    "ds_read_b64_tr_b16 v[138:139], v221 offset:3072\n"
    "ds_read_b64_tr_b16 v[140:141], v221 offset:2056\n"
    "ds_read_b64_tr_b16 v[142:143], v221 offset:3080\n"
    "ds_read_b64_tr_b16 v[144:145], v221 offset:4096\n"
    "ds_read_b64_tr_b16 v[146:147], v221 offset:5120\n"
    "ds_read_b64_tr_b16 v[148:149], v221 offset:4104\n"
    "ds_read_b64_tr_b16 v[150:151], v221 offset:5128\n"
    "ds_read_b64_tr_b16 v[152:153], v221 offset:6144\n"
    "ds_read_b64_tr_b16 v[154:155], v221 offset:7168\n"
    "ds_read_b64_tr_b16 v[156:157], v221 offset:6152\n"
    "ds_read_b64_tr_b16 v[158:159], v221 offset:7176\n"
    "ds_read_b64_tr_b16 v[160:161], v221 offset:8192\n"
    "ds_read_b64_tr_b16 v[162:163], v221 offset:9216\n"
    "ds_read_b64_tr_b16 v[164:165], v221 offset:8200\n"
    "ds_read_b64_tr_b16 v[166:167], v221 offset:9224\n"
    "s_waitcnt lgkmcnt(0)\n"
    "s_mov_b32 s58, 3\n"
    "K2L%=:\n"
    "v_mfma_f32_16x16x32_bf16 a[0:3], v[128:131], v[0:3], a[0:3]\n"
    "buffer_load_dword v228, v226, s[48:51], 0 offen sc0 sc1\n"
    "v_mfma_f32_16x16x32_bf16 a[40:43], v[128:131], v[4:7], a[40:43]\n"
    "ds_read_b64_tr_b16 v[168:169], v221 offset:10240\n"
    "ds_read_b64_tr_b16 v[170:171], v221 offset:11264\n"
    "v_mfma_f32_16x16x32_bf16 a[80:83], v[128:131], v[8:11], a[80:83]\n"
    "s_add_u32 m0, s59, 61440\n"
    "v_mfma_f32_16x16x32_bf16 a[120:123], v[128:131], v[12:15], a[120:123]\n"
    "v_mfma_f32_16x16x32_bf16 a[4:7], v[132:135], v[0:3], a[4:7]\n"
    "ds_read_b64_tr_b16 v[172:173], v221 offset:10248\n"
    "ds_read_b64_tr_b16 v[174:175], v221 offset:11272\n"
    "v_mfma_f32_16x16x32_bf16 a[44:47], v[132:135], v[4:7], a[44:47]\n"
    "v_mfma_f32_16x16x32_bf16 a[84:87], v[132:135], v[8:11], a[84:87]\n"
    "buffer_load_dwordx4 v231, s[36:39], s56 offen offset:0 lds\n"
    "s_add_u32 m0, s59, 63424\n"
    "v_mfma_f32_16x16x32_bf16 a[124:127], v[132:135], v[12:15], a[124:127]\n"
    "ds_read_b64_tr_b16 v[176:177], v221 offset:12288\n"
    "ds_read_b64_tr_b16 v[178:179], v221 offset:13312\n"
    "v_mfma_f32_16x16x32_bf16 a[8:11], v[136:139], v[0:3], a[8:11]\n"
    "v_mfma_f32_16x16x32_bf16 a[48:51], v[136:139], v[4:7], a[48:51]\n"
    "v_mfma_f32_16x16x32_bf16 a[88:91], v[136:139], v[8:11], a[88:91]\n"
    "ds_read_b64_tr_b16 v[180:181], v221 offset:12296\n"
    "ds_read_b64_tr_b16 v[182:183], v221 offset:13320\n"
    "v_mfma_f32_16x16x32_bf16 a[128:131], v[136:139], v[12:15], a[128:131]\n"
    "buffer_load_dwordx4 v231, s[36:39], s56 offen offset:64 lds\n"
    "s_add_u32 m0, s59, 65408\n"
    "v_mfma_f32_16x16x32_bf16 a[12:15], v[140:143], v[0:3], a[12:15]\n"
    "v_mfma_f32_16x16x32_bf16 a[52:55], v[140:143], v[4:7], a[52:55]\n"
    "ds_read_b64_tr_b16 v[184:185], v221 offset:14336\n"
    "ds_read_b64_tr_b16 v[186:187], v221 offset:15360\n"
    "v_mfma_f32_16x16x32_bf16 a[92:95], v[140:143], v[8:11], a[92:95]\n"
    "buffer_load_dwordx4 v231, s[36:39], s56 offen offset:128 lds\n"
    "s_add_u32 m0, s59, 67392\n"
    "v_mfma_f32_16x16x32_bf16 a[132:135], v[140:143], v[12:15], a[132:135]\n"
    "v_mfma_f32_16x16x32_bf16 a[16:19], v[144:147], v[0:3], a[16:19]\n"
    "ds_read_b64_tr_b16 v[188:189], v221 offset:14344\n"
    "ds_read_b64_tr_b16 v[190:191], v221 offset:15368\n"
    "v_mfma_f32_16x16x32_bf16 a[56:59], v[144:147], v[4:7], a[56:59]\n"
    "v_mfma_f32_16x16x32_bf16 a[96:99], v[144:147], v[8:11], a[96:99]\n"
    "buffer_load_dwordx4 v231, s[36:39], s56 offen offset:192 lds\n"
    "s_add_u32 m0, s59, 69376\n"
    "v_mfma_f32_16x16x32_bf16 a[136:139], v[144:147], v[12:15], a[136:139]\n"
    "ds_read_b64_tr_b16 v[192:193], v221 offset:16384\n"
    "ds_read_b64_tr_b16 v[194:195], v221 offset:17408\n"
    "v_mfma_f32_16x16x32_bf16 a[20:23], v[148:151], v[0:3], a[20:23]\n"
    "v_mfma_f32_16x16x32_bf16 a[60:63], v[148:151], v[4:7], a[60:63]\n"
    "v_mfma_f32_16x16x32_bf16 a[100:103], v[148:151], v[8:11], a[100:103]\n"
    "ds_read_b64_tr_b16 v[196:197], v221 offset:16392\n"
    "ds_read_b64_tr_b16 v[198:199], v221 offset:17416\n"
    "v_mfma_f32_16x16x32_bf16 a[140:143], v[148:151], v[12:15], a[140:143]\n"
    "buffer_load_dwordx4 v231, s[36:39], s56 offen offset:256 lds\n"
    "s_add_u32 s56, s56, 245760\n"
    "v_mfma_f32_16x16x32_bf16 a[24:27], v[152:155], v[0:3], a[24:27]\n"
    "v_mfma_f32_16x16x32_bf16 a[64:67], v[152:155], v[4:7], a[64:67]\n"
    "ds_read_b64_tr_b16 v[200:201], v221 offset:18432\n"
    "ds_read_b64_tr_b16 v[202:203], v221 offset:19456\n"
    "v_mfma_f32_16x16x32_bf16 a[104:107], v[152:155], v[8:11], a[104:107]\n"
    "buffer_load_dwordx4 v[96:99], v224, s[40:43], s57 offen offset:2048 sc1\n"
    "v_mfma_f32_16x16x32_bf16 a[144:147], v[152:155], v[12:15], a[144:147]\n"
    "v_mfma_f32_16x16x32_bf16 a[28:31], v[156:159], v[0:3], a[28:31]\n"
    "ds_read_b64_tr_b16 v[204:205], v221 offset:18440\n"
    "ds_read_b64_tr_b16 v[206:207], v221 offset:19464\n"
    "v_mfma_f32_16x16x32_bf16 a[68:71], v[156:159], v[4:7], a[68:71]\n"
    "v_mfma_f32_16x16x32_bf16 a[108:111], v[156:159], v[8:11], a[108:111]\n"
    "s_lshl_b32 s61, s58, 1\n"
    "s_lshr_b32 s61, 0x3000, s61\n"
    "v_mfma_f32_16x16x32_bf16 a[148:151], v[156:159], v[12:15], a[148:151]\n"
    "v_mfma_f32_16x16x32_bf16 a[32:35], v[160:163], v[0:3], a[32:35]\n"
    "v_mfma_f32_16x16x32_bf16 a[72:75], v[160:163], v[4:7], a[72:75]\n"
    "s_and_b32 s61, s61, 0xf00\n"
    "v_mfma_f32_16x16x32_bf16 a[112:115], v[160:163], v[8:11], a[112:115]\n"
    "v_mfma_f32_16x16x32_bf16 a[152:155], v[160:163], v[12:15], a[152:155]\n"
    "v_mfma_f32_16x16x32_bf16 a[36:39], v[164:167], v[0:3], a[36:39]\n"
    "v_mfma_f32_16x16x32_bf16 a[76:79], v[164:167], v[4:7], a[76:79]\n"
    "v_mfma_f32_16x16x32_bf16 a[116:119], v[164:167], v[8:11], a[116:119]\n"
    "v_mfma_f32_16x16x32_bf16 a[156:159], v[164:167], v[12:15], a[156:159]\n"
    "s_waitcnt lgkmcnt(0)\n"
    "v_mfma_f32_16x16x32_bf16 a[0:3], v[168:171], v[16:19], a[0:3]\n"
    "s_waitcnt vmcnt(20)\n"
    "s_barrier\n"
    "v_mfma_f32_16x16x32_bf16 a[40:43], v[168:171], v[20:23], a[40:43]\n"
    "ds_read_b64_tr_b16 v[128:129], v221 offset:20480\n"
    "ds_read_b64_tr_b16 v[130:131], v221 offset:21504\n"
    "v_mfma_f32_16x16x32_bf16 a[80:83], v[168:171], v[24:27], a[80:83]\n"
    "buffer_load_dwordx4 v[100:103], v225, s[40:43], s57 offen offset:2048 sc1\n"
    "v_mfma_f32_16x16x32_bf16 a[120:123], v[168:171], v[28:31], a[120:123]\n"
    "v_mfma_f32_16x16x32_bf16 a[4:7], v[172:175], v[16:19], a[4:7]\n"
    "ds_read_b64_tr_b16 v[132:133], v221 offset:20488\n"
    "ds_read_b64_tr_b16 v[134:135], v221 offset:21512\n"
    "v_mfma_f32_16x16x32_bf16 a[44:47], v[172:175], v[20:23], a[44:47]\n"
    "v_mfma_f32_16x16x32_bf16 a[84:87], v[172:175], v[24:27], a[84:87]\n"
    "buffer_load_dwordx4 v[104:107], v227, s[40:43], s57 offen offset:2048 sc1\n"
    "v_mfma_f32_16x16x32_bf16 a[124:127], v[172:175], v[28:31], a[124:127]\n"
    "ds_read_b64_tr_b16 v[136:137], v221 offset:22528\n"
    "ds_read_b64_tr_b16 v[138:139], v221 offset:23552\n"
    "v_mfma_f32_16x16x32_bf16 a[8:11], v[176:179], v[16:19], a[8:11]\n"
    "v_mfma_f32_16x16x32_bf16 a[48:51], v[176:179], v[20:23], a[48:51]\n"
    "v_mfma_f32_16x16x32_bf16 a[88:91], v[176:179], v[24:27], a[88:91]\n"
    "ds_read_b64_tr_b16 v[140:141], v221 offset:22536\n"
    "ds_read_b64_tr_b16 v[142:143], v221 offset:23560\n"
    "v_mfma_f32_16x16x32_bf16 a[128:131], v[176:179], v[28:31], a[128:131]\n"
    "buffer_load_dwordx4 v[108:111], v253, s[40:43], s57 offen offset:2048 sc1\n"
    "v_mfma_f32_16x16x32_bf16 a[12:15], v[180:183], v[16:19], a[12:15]\n"
    "v_mfma_f32_16x16x32_bf16 a[52:55], v[180:183], v[20:23], a[52:55]\n"
    "ds_read_b64_tr_b16 v[144:145], v221 offset:24576\n"
    "ds_read_b64_tr_b16 v[146:147], v221 offset:25600\n"
    "v_mfma_f32_16x16x32_bf16 a[92:95], v[180:183], v[24:27], a[92:95]\n"
    "buffer_load_dwordx4 v[112:115], v224, s[40:43], s57 offen offset:3072 sc1\n"
    "v_mfma_f32_16x16x32_bf16 a[132:135], v[180:183], v[28:31], a[132:135]\n"
    "v_mfma_f32_16x16x32_bf16 a[16:19], v[184:187], v[16:19], a[16:19]\n"
    "ds_read_b64_tr_b16 v[148:149], v221 offset:24584\n"
    "ds_read_b64_tr_b16 v[150:151], v221 offset:25608\n"
    "v_mfma_f32_16x16x32_bf16 a[56:59], v[184:187], v[20:23], a[56:59]\n"
    "v_mfma_f32_16x16x32_bf16 a[96:99], v[184:187], v[24:27], a[96:99]\n"
    "buffer_load_dwordx4 v[116:119], v225, s[40:43], s57 offen offset:3072 sc1\n"
    "v_mfma_f32_16x16x32_bf16 a[136:139], v[184:187], v[28:31], a[136:139]\n"
    "ds_read_b64_tr_b16 v[152:153], v221 offset:26624\n"
    "ds_read_b64_tr_b16 v[154:155], v221 offset:27648\n"
    "v_mfma_f32_16x16x32_bf16 a[20:23], v[188:191], v[16:19], a[20:23]\n"
    "v_mfma_f32_16x16x32_bf16 a[60:63], v[188:191], v[20:23], a[60:63]\n"
    "v_mfma_f32_16x16x32_bf16 a[100:103], v[188:191], v[24:27], a[100:103]\n"
    "ds_read_b64_tr_b16 v[156:157], v221 offset:26632\n"
    "ds_read_b64_tr_b16 v[158:159], v221 offset:27656\n"
    "v_mfma_f32_16x16x32_bf16 a[140:143], v[188:191], v[28:31], a[140:143]\n"
    "buffer_load_dwordx4 v[120:123], v227, s[40:43], s57 offen offset:3072 sc1\n"
    "v_mfma_f32_16x16x32_bf16 a[24:27], v[192:195], v[16:19], a[24:27]\n"
    "v_mfma_f32_16x16x32_bf16 a[64:67], v[192:195], v[20:23], a[64:67]\n"
    "ds_read_b64_tr_b16 v[160:161], v221 offset:28672\n"
    "ds_read_b64_tr_b16 v[162:163], v221 offset:29696\n"
    "v_mfma_f32_16x16x32_bf16 a[104:107], v[192:195], v[24:27], a[104:107]\n"
    "buffer_load_dwordx4 v[124:127], v253, s[40:43], s57 offen offset:3072 sc1\n"
    "v_mfma_f32_16x16x32_bf16 a[144:147], v[192:195], v[28:31], a[144:147]\n"
    "v_mfma_f32_16x16x32_bf16 a[28:31], v[196:199], v[16:19], a[28:31]\n"
    "ds_read_b64_tr_b16 v[164:165], v221 offset:28680\n"
    "ds_read_b64_tr_b16 v[166:167], v221 offset:29704\n"
    "v_mfma_f32_16x16x32_bf16 a[68:71], v[196:199], v[20:23], a[68:71]\n"
    "v_mfma_f32_16x16x32_bf16 a[108:111], v[196:199], v[24:27], a[108:111]\n"
    "s_add_u32 s57, s57, 8192\n"
    "s_cmp_eq_u32 s57, 32768\n"
    "s_cselect_b32 s57, 4096, s57\n"
    "s_cselect_b32 s60, 1146880, 0\n"
    "v_mfma_f32_16x16x32_bf16 a[148:151], v[196:199], v[28:31], a[148:151]\n"
    "v_mfma_f32_16x16x32_bf16 a[32:35], v[200:203], v[16:19], a[32:35]\n"
    "v_mfma_f32_16x16x32_bf16 a[72:75], v[200:203], v[20:23], a[72:75]\n"
    "v_mfma_f32_16x16x32_bf16 a[112:115], v[200:203], v[24:27], a[112:115]\n"
    "v_mfma_f32_16x16x32_bf16 a[152:155], v[200:203], v[28:31], a[152:155]\n"
    "s_sub_u32 s56, s56, s60\n"
    "v_mfma_f32_16x16x32_bf16 a[36:39], v[204:207], v[16:19], a[36:39]\n"
    "v_mfma_f32_16x16x32_bf16 a[76:79], v[204:207], v[20:23], a[76:79]\n"
    "v_mfma_f32_16x16x32_bf16 a[116:119], v[204:207], v[24:27], a[116:119]\n"
    "v_mfma_f32_16x16x32_bf16 a[156:159], v[204:207], v[28:31], a[156:159]\n"
    "s_waitcnt lgkmcnt(0)\n"
    "v_mfma_f32_16x16x32_bf16 a[0:3], v[128:131], v[32:35], a[0:3]\n"
    "v_mfma_f32_16x16x32_bf16 a[40:43], v[128:131], v[36:39], a[40:43]\n"
    "ds_read_b64_tr_b16 v[168:169], v221 offset:30720\n"
    "ds_read_b64_tr_b16 v[170:171], v221 offset:31744\n"
    "v_mfma_f32_16x16x32_bf16 a[80:83], v[128:131], v[40:43], a[80:83]\n"
    "s_add_u32 m0, s59, 0\n"
    "v_mfma_f32_16x16x32_bf16 a[120:123], v[128:131], v[44:47], a[120:123]\n"
    "v_mfma_f32_16x16x32_bf16 a[4:7], v[132:135], v[32:35], a[4:7]\n"
    "ds_read_b64_tr_b16 v[172:173], v221 offset:30728\n"
    "ds_read_b64_tr_b16 v[174:175], v221 offset:31752\n"
    "v_mfma_f32_16x16x32_bf16 a[44:47], v[132:135], v[36:39], a[44:47]\n"
    "v_mfma_f32_16x16x32_bf16 a[84:87], v[132:135], v[40:43], a[84:87]\n"
    "buffer_load_dwordx4 v231, s[36:39], s56 offen offset:0 lds\n"
    "s_add_u32 m0, s59, 1984\n"
    "v_mfma_f32_16x16x32_bf16 a[124:127], v[132:135], v[44:47], a[124:127]\n"
    "ds_read_b64_tr_b16 v[176:177], v221 offset:32768\n"
    "ds_read_b64_tr_b16 v[178:179], v221 offset:33792\n"
    "v_mfma_f32_16x16x32_bf16 a[8:11], v[136:139], v[32:35], a[8:11]\n"
    "v_mfma_f32_16x16x32_bf16 a[48:51], v[136:139], v[36:39], a[48:51]\n"
    "v_mfma_f32_16x16x32_bf16 a[88:91], v[136:139], v[40:43], a[88:91]\n"
    "ds_read_b64_tr_b16 v[180:181], v221 offset:32776\n"
    "ds_read_b64_tr_b16 v[182:183], v221 offset:33800\n"
    "v_mfma_f32_16x16x32_bf16 a[128:131], v[136:139], v[44:47], a[128:131]\n"
    "buffer_load_dwordx4 v231, s[36:39], s56 offen offset:64 lds\n"
    "s_add_u32 m0, s59, 3968\n"
    "v_mfma_f32_16x16x32_bf16 a[12:15], v[140:143], v[32:35], a[12:15]\n"
    "v_mfma_f32_16x16x32_bf16 a[52:55], v[140:143], v[36:39], a[52:55]\n"
    "ds_read_b64_tr_b16 v[184:185], v221 offset:34816\n"
    "ds_read_b64_tr_b16 v[186:187], v221 offset:35840\n"
    "v_mfma_f32_16x16x32_bf16 a[92:95], v[140:143], v[40:43], a[92:95]\n"
    "buffer_load_dwordx4 v231, s[36:39], s56 offen offset:128 lds\n"
    "s_add_u32 m0, s59, 5952\n"
    "v_mfma_f32_16x16x32_bf16 a[132:135], v[140:143], v[44:47], a[132:135]\n"
    "v_mfma_f32_16x16x32_bf16 a[16:19], v[144:147], v[32:35], a[16:19]\n"
    "ds_read_b64_tr_b16 v[188:189], v221 offset:34824\n"
    "ds_read_b64_tr_b16 v[190:191], v221 offset:35848\n"
    "v_mfma_f32_16x16x32_bf16 a[56:59], v[144:147], v[36:39], a[56:59]\n"
    "v_mfma_f32_16x16x32_bf16 a[96:99], v[144:147], v[40:43], a[96:99]\n"
    "buffer_load_dwordx4 v231, s[36:39], s56 offen offset:192 lds\n"
    "s_add_u32 m0, s59, 7936\n"
    "v_mfma_f32_16x16x32_bf16 a[136:139], v[144:147], v[44:47], a[136:139]\n"
    "ds_read_b64_tr_b16 v[192:193], v221 offset:36864\n"
    "ds_read_b64_tr_b16 v[194:195], v221 offset:37888\n"
    "v_mfma_f32_16x16x32_bf16 a[20:23], v[148:151], v[32:35], a[20:23]\n"
    "v_mfma_f32_16x16x32_bf16 a[60:63], v[148:151], v[36:39], a[60:63]\n"
    "v_mfma_f32_16x16x32_bf16 a[100:103], v[148:151], v[40:43], a[100:103]\n"
    "ds_read_b64_tr_b16 v[196:197], v221 offset:36872\n"
    "ds_read_b64_tr_b16 v[198:199], v221 offset:37896\n"
    "v_mfma_f32_16x16x32_bf16 a[140:143], v[148:151], v[44:47], a[140:143]\n"
    "buffer_load_dwordx4 v231, s[36:39], s56 offen offset:256 lds\n"
    "s_add_u32 s56, s56, 81920\n"
    "v_mfma_f32_16x16x32_bf16 a[24:27], v[152:155], v[32:35], a[24:27]\n"
    "v_mfma_f32_16x16x32_bf16 a[64:67], v[152:155], v[36:39], a[64:67]\n"
    "ds_read_b64_tr_b16 v[200:201], v221 offset:38912\n"
    "ds_read_b64_tr_b16 v[202:203], v221 offset:39936\n"
    "v_mfma_f32_16x16x32_bf16 a[104:107], v[152:155], v[40:43], a[104:107]\n"
    "v_mfma_f32_16x16x32_bf16 a[144:147], v[152:155], v[44:47], a[144:147]\n"
    "v_mfma_f32_16x16x32_bf16 a[28:31], v[156:159], v[32:35], a[28:31]\n"
    "ds_read_b64_tr_b16 v[204:205], v221 offset:38920\n"
    "ds_read_b64_tr_b16 v[206:207], v221 offset:39944\n"
    "v_mfma_f32_16x16x32_bf16 a[68:71], v[156:159], v[36:39], a[68:71]\n"
    "v_mfma_f32_16x16x32_bf16 a[108:111], v[156:159], v[40:43], a[108:111]\n"
    "v_mfma_f32_16x16x32_bf16 a[148:151], v[156:159], v[44:47], a[148:151]\n"
    "v_mfma_f32_16x16x32_bf16 a[32:35], v[160:163], v[32:35], a[32:35]\n"
    "v_mfma_f32_16x16x32_bf16 a[72:75], v[160:163], v[36:39], a[72:75]\n"
    "v_mfma_f32_16x16x32_bf16 a[112:115], v[160:163], v[40:43], a[112:115]\n"
    "v_mfma_f32_16x16x32_bf16 a[152:155], v[160:163], v[44:47], a[152:155]\n"
    "v_mfma_f32_16x16x32_bf16 a[36:39], v[164:167], v[32:35], a[36:39]\n"
    "v_mfma_f32_16x16x32_bf16 a[76:79], v[164:167], v[36:39], a[76:79]\n"
    "v_mfma_f32_16x16x32_bf16 a[116:119], v[164:167], v[40:43], a[116:119]\n"
    "v_mfma_f32_16x16x32_bf16 a[156:159], v[164:167], v[44:47], a[156:159]\n"
    "s_waitcnt lgkmcnt(0)\n"
    "v_mfma_f32_16x16x32_bf16 a[0:3], v[168:171], v[48:51], a[0:3]\n"
    "s_waitcnt vmcnt(18)\n"
    "s_barrier\n"
    "buffer_store_dword v255, off, s[48:51], s70 offset:64 sc0 sc1\n"
    "v_and_b32 v228, 1, v228\n"
    "v_cmp_ne_u32 vcc, 0, v228\n"
    "v_mfma_f32_16x16x32_bf16 a[40:43], v[168:171], v[52:55], a[40:43]\n"
    "ds_read_b64_tr_b16 v[128:129], v221 offset:40960\n"
    "ds_read_b64_tr_b16 v[130:131], v221 offset:41984\n"
    "v_mfma_f32_16x16x32_bf16 a[80:83], v[168:171], v[56:59], a[80:83]\n"
    "s_xor_b32 s60, vcc_lo, s64\n"
    "s_andn2_b32 s60, s61, s60\n"
    "s_cbranch_scc0 K2AQ%=\n"
    "K2AP%=:\n"
    "buffer_load_dword v228, v226, s[48:51], 0 offen sc0 sc1\n"
    "s_sub_u32 s62, s62, 1\ns_cmp_lt_i32 s62, 0\n"
    "s_cbranch_scc1 K2AQ%=\n"
    "s_waitcnt vmcnt(0)\n"
    "v_and_b32 v228, 1, v228\n"
    "v_cmp_ne_u32 vcc, 0, v228\n"
    "s_xor_b32 s60, vcc_lo, s64\n"
    "s_andn2_b32 s60, s61, s60\n"
    "s_cbranch_scc1 K2AP%=\n"
    "K2AQ%=:\n"
    "buffer_load_dwordx4 v[0:3], v224, s[40:43], s57 offen offset:0 sc1\n"
    "v_mfma_f32_16x16x32_bf16 a[120:123], v[168:171], v[60:63], a[120:123]\n"
    "v_mfma_f32_16x16x32_bf16 a[4:7], v[172:175], v[48:51], a[4:7]\n"
    "ds_read_b64_tr_b16 v[132:133], v221 offset:40968\n"
    "ds_read_b64_tr_b16 v[134:135], v221 offset:41992\n"
    "v_mfma_f32_16x16x32_bf16 a[44:47], v[172:175], v[52:55], a[44:47]\n"
    "v_mfma_f32_16x16x32_bf16 a[84:87], v[172:175], v[56:59], a[84:87]\n"
    "buffer_load_dwordx4 v[4:7], v225, s[40:43], s57 offen offset:0 sc1\n"
    "v_mfma_f32_16x16x32_bf16 a[124:127], v[172:175], v[60:63], a[124:127]\n"
    "ds_read_b64_tr_b16 v[136:137], v221 offset:43008\n"
    "ds_read_b64_tr_b16 v[138:139], v221 offset:44032\n"
    "v_mfma_f32_16x16x32_bf16 a[8:11], v[176:179], v[48:51], a[8:11]\n"
    "v_mfma_f32_16x16x32_bf16 a[48:51], v[176:179], v[52:55], a[48:51]\n"
    "v_mfma_f32_16x16x32_bf16 a[88:91], v[176:179], v[56:59], a[88:91]\n"
    "ds_read_b64_tr_b16 v[140:141], v221 offset:43016\n"
    "ds_read_b64_tr_b16 v[142:143], v221 offset:44040\n"
    "v_mfma_f32_16x16x32_bf16 a[128:131], v[176:179], v[60:63], a[128:131]\n"
    "buffer_load_dwordx4 v[8:11], v227, s[40:43], s57 offen offset:0 sc1\n"
    "v_mfma_f32_16x16x32_bf16 a[12:15], v[180:183], v[48:51], a[12:15]\n"
    "v_mfma_f32_16x16x32_bf16 a[52:55], v[180:183], v[52:55], a[52:55]\n"
    "ds_read_b64_tr_b16 v[144:145], v221 offset:45056\n"
    "ds_read_b64_tr_b16 v[146:147], v221 offset:46080\n"
    "v_mfma_f32_16x16x32_bf16 a[92:95], v[180:183], v[56:59], a[92:95]\n"
    "buffer_load_dwordx4 v[12:15], v253, s[40:43], s57 offen offset:0 sc1\n"
    "v_mfma_f32_16x16x32_bf16 a[132:135], v[180:183], v[60:63], a[132:135]\n"
    "v_mfma_f32_16x16x32_bf16 a[16:19], v[184:187], v[48:51], a[16:19]\n"
    "ds_read_b64_tr_b16 v[148:149], v221 offset:45064\n"
    "ds_read_b64_tr_b16 v[150:151], v221 offset:46088\n"
    "v_mfma_f32_16x16x32_bf16 a[56:59], v[184:187], v[52:55], a[56:59]\n"
    "v_mfma_f32_16x16x32_bf16 a[96:99], v[184:187], v[56:59], a[96:99]\n"
    "buffer_load_dwordx4 v[16:19], v224, s[40:43], s57 offen offset:1024 sc1\n"
    "v_mfma_f32_16x16x32_bf16 a[136:139], v[184:187], v[60:63], a[136:139]\n"
    "ds_read_b64_tr_b16 v[152:153], v221 offset:47104\n"
    "ds_read_b64_tr_b16 v[154:155], v221 offset:48128\n"
    "v_mfma_f32_16x16x32_bf16 a[20:23], v[188:191], v[48:51], a[20:23]\n"
    "v_mfma_f32_16x16x32_bf16 a[60:63], v[188:191], v[52:55], a[60:63]\n"
    "v_mfma_f32_16x16x32_bf16 a[100:103], v[188:191], v[56:59], a[100:103]\n"
    "ds_read_b64_tr_b16 v[156:157], v221 offset:47112\n"
    "ds_read_b64_tr_b16 v[158:159], v221 offset:48136\n"
    "v_mfma_f32_16x16x32_bf16 a[140:143], v[188:191], v[60:63], a[140:143]\n"
    "buffer_load_dwordx4 v[20:23], v225, s[40:43], s57 offen offset:1024 sc1\n"
    "v_mfma_f32_16x16x32_bf16 a[24:27], v[192:195], v[48:51], a[24:27]\n"
    "v_mfma_f32_16x16x32_bf16 a[64:67], v[192:195], v[52:55], a[64:67]\n"
    "ds_read_b64_tr_b16 v[160:161], v221 offset:49152\n"
    "ds_read_b64_tr_b16 v[162:163], v221 offset:50176\n"
    "v_mfma_f32_16x16x32_bf16 a[104:107], v[192:195], v[56:59], a[104:107]\n"
    "buffer_load_dwordx4 v[24:27], v227, s[40:43], s57 offen offset:1024 sc1\n"
    "v_mfma_f32_16x16x32_bf16 a[144:147], v[192:195], v[60:63], a[144:147]\n"
    "v_mfma_f32_16x16x32_bf16 a[28:31], v[196:199], v[48:51], a[28:31]\n"
    "ds_read_b64_tr_b16 v[164:165], v221 offset:49160\n"
    "ds_read_b64_tr_b16 v[166:167], v221 offset:50184\n"
    "v_mfma_f32_16x16x32_bf16 a[68:71], v[196:199], v[52:55], a[68:71]\n"
    "v_mfma_f32_16x16x32_bf16 a[108:111], v[196:199], v[56:59], a[108:111]\n"
    "buffer_load_dwordx4 v[28:31], v253, s[40:43], s57 offen offset:1024 sc1\n"
    "v_mfma_f32_16x16x32_bf16 a[148:151], v[196:199], v[60:63], a[148:151]\n"
    "v_mfma_f32_16x16x32_bf16 a[32:35], v[200:203], v[48:51], a[32:35]\n"
    "v_mfma_f32_16x16x32_bf16 a[72:75], v[200:203], v[52:55], a[72:75]\n"
    "v_mfma_f32_16x16x32_bf16 a[112:115], v[200:203], v[56:59], a[112:115]\n"
    "v_mfma_f32_16x16x32_bf16 a[152:155], v[200:203], v[60:63], a[152:155]\n"
    "v_mfma_f32_16x16x32_bf16 a[36:39], v[204:207], v[48:51], a[36:39]\n"
    "v_mfma_f32_16x16x32_bf16 a[76:79], v[204:207], v[52:55], a[76:79]\n"
    "v_mfma_f32_16x16x32_bf16 a[116:119], v[204:207], v[56:59], a[116:119]\n"
    "v_mfma_f32_16x16x32_bf16 a[156:159], v[204:207], v[60:63], a[156:159]\n"
    "s_waitcnt lgkmcnt(0)\n"
    "v_mfma_f32_16x16x32_bf16 a[0:3], v[128:131], v[64:67], a[0:3]\n"
    "v_mfma_f32_16x16x32_bf16 a[40:43], v[128:131], v[68:71], a[40:43]\n"
    "ds_read_b64_tr_b16 v[168:169], v221 offset:51200\n"
    "ds_read_b64_tr_b16 v[170:171], v221 offset:52224\n"
    "v_mfma_f32_16x16x32_bf16 a[80:83], v[128:131], v[72:75], a[80:83]\n"
    "s_add_u32 m0, s59, 20480\n"
    "v_mfma_f32_16x16x32_bf16 a[120:123], v[128:131], v[76:79], a[120:123]\n"
    "v_mfma_f32_16x16x32_bf16 a[4:7], v[132:135], v[64:67], a[4:7]\n"
    "ds_read_b64_tr_b16 v[172:173], v221 offset:51208\n"
    "ds_read_b64_tr_b16 v[174:175], v221 offset:52232\n"
    "v_mfma_f32_16x16x32_bf16 a[44:47], v[132:135], v[68:71], a[44:47]\n"
    "v_mfma_f32_16x16x32_bf16 a[84:87], v[132:135], v[72:75], a[84:87]\n"
    "buffer_load_dwordx4 v231, s[36:39], s56 offen offset:0 lds\n"
    "s_add_u32 m0, s59, 22464\n"
    "v_mfma_f32_16x16x32_bf16 a[124:127], v[132:135], v[76:79], a[124:127]\n"
    "ds_read_b64_tr_b16 v[176:177], v221 offset:53248\n"
    "ds_read_b64_tr_b16 v[178:179], v221 offset:54272\n"
    "v_mfma_f32_16x16x32_bf16 a[8:11], v[136:139], v[64:67], a[8:11]\n"
    "v_mfma_f32_16x16x32_bf16 a[48:51], v[136:139], v[68:71], a[48:51]\n"
    "v_mfma_f32_16x16x32_bf16 a[88:91], v[136:139], v[72:75], a[88:91]\n"
    "ds_read_b64_tr_b16 v[180:181], v221 offset:53256\n"
    "ds_read_b64_tr_b16 v[182:183], v221 offset:54280\n"
    "v_mfma_f32_16x16x32_bf16 a[128:131], v[136:139], v[76:79], a[128:131]\n"
    "buffer_load_dwordx4 v231, s[36:39], s56 offen offset:64 lds\n"
    "s_add_u32 m0, s59, 24448\n"
    "v_mfma_f32_16x16x32_bf16 a[12:15], v[140:143], v[64:67], a[12:15]\n"
    "v_mfma_f32_16x16x32_bf16 a[52:55], v[140:143], v[68:71], a[52:55]\n"
    "ds_read_b64_tr_b16 v[184:185], v221 offset:55296\n"
    "ds_read_b64_tr_b16 v[186:187], v221 offset:56320\n"
    "v_mfma_f32_16x16x32_bf16 a[92:95], v[140:143], v[72:75], a[92:95]\n"
    "buffer_load_dwordx4 v231, s[36:39], s56 offen offset:128 lds\n"
    "s_add_u32 m0, s59, 26432\n"
    "v_mfma_f32_16x16x32_bf16 a[132:135], v[140:143], v[76:79], a[132:135]\n"
    "v_mfma_f32_16x16x32_bf16 a[16:19], v[144:147], v[64:67], a[16:19]\n"
    "ds_read_b64_tr_b16 v[188:189], v221 offset:55304\n"
    "ds_read_b64_tr_b16 v[190:191], v221 offset:56328\n"
    "v_mfma_f32_16x16x32_bf16 a[56:59], v[144:147], v[68:71], a[56:59]\n"
    "v_mfma_f32_16x16x32_bf16 a[96:99], v[144:147], v[72:75], a[96:99]\n"
    "buffer_load_dwordx4 v231, s[36:39], s56 offen offset:192 lds\n"
    "s_add_u32 m0, s59, 28416\n"
    "v_mfma_f32_16x16x32_bf16 a[136:139], v[144:147], v[76:79], a[136:139]\n"
    "ds_read_b64_tr_b16 v[192:193], v221 offset:57344\n"
    "ds_read_b64_tr_b16 v[194:195], v221 offset:58368\n"
    "v_mfma_f32_16x16x32_bf16 a[20:23], v[148:151], v[64:67], a[20:23]\n"
    "v_mfma_f32_16x16x32_bf16 a[60:63], v[148:151], v[68:71], a[60:63]\n"
    "v_mfma_f32_16x16x32_bf16 a[100:103], v[148:151], v[72:75], a[100:103]\n"
    "ds_read_b64_tr_b16 v[196:197], v221 offset:57352\n"
    "ds_read_b64_tr_b16 v[198:199], v221 offset:58376\n"
    "v_mfma_f32_16x16x32_bf16 a[140:143], v[148:151], v[76:79], a[140:143]\n"
    "buffer_load_dwordx4 v231, s[36:39], s56 offen offset:256 lds\n"
    "s_add_u32 s56, s56, 245760\n"
    "v_mfma_f32_16x16x32_bf16 a[24:27], v[152:155], v[64:67], a[24:27]\n"
    "v_mfma_f32_16x16x32_bf16 a[64:67], v[152:155], v[68:71], a[64:67]\n"
    "ds_read_b64_tr_b16 v[200:201], v221 offset:59392\n"
    "ds_read_b64_tr_b16 v[202:203], v221 offset:60416\n"
    "v_mfma_f32_16x16x32_bf16 a[104:107], v[152:155], v[72:75], a[104:107]\n"
    "buffer_load_dwordx4 v[32:35], v224, s[40:43], s57 offen offset:2048 sc1\n"
    "v_mfma_f32_16x16x32_bf16 a[144:147], v[152:155], v[76:79], a[144:147]\n"
    "v_mfma_f32_16x16x32_bf16 a[28:31], v[156:159], v[64:67], a[28:31]\n"
    "ds_read_b64_tr_b16 v[204:205], v221 offset:59400\n"
    "ds_read_b64_tr_b16 v[206:207], v221 offset:60424\n"
    "v_mfma_f32_16x16x32_bf16 a[68:71], v[156:159], v[68:71], a[68:71]\n"
    "v_mfma_f32_16x16x32_bf16 a[108:111], v[156:159], v[72:75], a[108:111]\n"
    "v_mfma_f32_16x16x32_bf16 a[148:151], v[156:159], v[76:79], a[148:151]\n"
    "v_mfma_f32_16x16x32_bf16 a[32:35], v[160:163], v[64:67], a[32:35]\n"
    "v_mfma_f32_16x16x32_bf16 a[72:75], v[160:163], v[68:71], a[72:75]\n"
    "v_mfma_f32_16x16x32_bf16 a[112:115], v[160:163], v[72:75], a[112:115]\n"
    "v_mfma_f32_16x16x32_bf16 a[152:155], v[160:163], v[76:79], a[152:155]\n"
    "v_mfma_f32_16x16x32_bf16 a[36:39], v[164:167], v[64:67], a[36:39]\n"
    "v_mfma_f32_16x16x32_bf16 a[76:79], v[164:167], v[68:71], a[76:79]\n"
    "v_mfma_f32_16x16x32_bf16 a[116:119], v[164:167], v[72:75], a[116:119]\n"
    "v_mfma_f32_16x16x32_bf16 a[156:159], v[164:167], v[76:79], a[156:159]\n"
    "s_waitcnt lgkmcnt(0)\n"
    "v_mfma_f32_16x16x32_bf16 a[0:3], v[168:171], v[80:83], a[0:3]\n"
    "s_waitcnt vmcnt(20)\n"
    "s_barrier\n"
    "v_mfma_f32_16x16x32_bf16 a[40:43], v[168:171], v[84:87], a[40:43]\n"
    "ds_read_b64_tr_b16 v[128:129], v222 offset:0\n"
    "ds_read_b64_tr_b16 v[130:131], v222 offset:1024\n"
    "v_mfma_f32_16x16x32_bf16 a[80:83], v[168:171], v[88:91], a[80:83]\n"
    "buffer_load_dwordx4 v[36:39], v225, s[40:43], s57 offen offset:2048 sc1\n"
    "v_mfma_f32_16x16x32_bf16 a[120:123], v[168:171], v[92:95], a[120:123]\n"
    "v_mfma_f32_16x16x32_bf16 a[4:7], v[172:175], v[80:83], a[4:7]\n"
    "ds_read_b64_tr_b16 v[132:133], v222 offset:8\n"
    "ds_read_b64_tr_b16 v[134:135], v222 offset:1032\n"
    "v_mfma_f32_16x16x32_bf16 a[44:47], v[172:175], v[84:87], a[44:47]\n"
    "v_mfma_f32_16x16x32_bf16 a[84:87], v[172:175], v[88:91], a[84:87]\n"
    "buffer_load_dwordx4 v[40:43], v227, s[40:43], s57 offen offset:2048 sc1\n"
    "v_mfma_f32_16x16x32_bf16 a[124:127], v[172:175], v[92:95], a[124:127]\n"
    "ds_read_b64_tr_b16 v[136:137], v222 offset:2048\n"
    "ds_read_b64_tr_b16 v[138:139], v222 offset:3072\n"
    "v_mfma_f32_16x16x32_bf16 a[8:11], v[176:179], v[80:83], a[8:11]\n"
    "v_mfma_f32_16x16x32_bf16 a[48:51], v[176:179], v[84:87], a[48:51]\n"
    "v_mfma_f32_16x16x32_bf16 a[88:91], v[176:179], v[88:91], a[88:91]\n"
    "ds_read_b64_tr_b16 v[140:141], v222 offset:2056\n"
    "ds_read_b64_tr_b16 v[142:143], v222 offset:3080\n"
    "v_mfma_f32_16x16x32_bf16 a[128:131], v[176:179], v[92:95], a[128:131]\n"
    "buffer_load_dwordx4 v[44:47], v253, s[40:43], s57 offen offset:2048 sc1\n"
    "v_mfma_f32_16x16x32_bf16 a[12:15], v[180:183], v[80:83], a[12:15]\n"
    "v_mfma_f32_16x16x32_bf16 a[52:55], v[180:183], v[84:87], a[52:55]\n"
    "ds_read_b64_tr_b16 v[144:145], v222 offset:4096\n"
    "ds_read_b64_tr_b16 v[146:147], v222 offset:5120\n"
    "v_mfma_f32_16x16x32_bf16 a[92:95], v[180:183], v[88:91], a[92:95]\n"
    "buffer_load_dwordx4 v[48:51], v224, s[40:43], s57 offen offset:3072 sc1\n"
    "v_mfma_f32_16x16x32_bf16 a[132:135], v[180:183], v[92:95], a[132:135]\n"
    "v_mfma_f32_16x16x32_bf16 a[16:19], v[184:187], v[80:83], a[16:19]\n"
    "ds_read_b64_tr_b16 v[148:149], v222 offset:4104\n"
    "ds_read_b64_tr_b16 v[150:151], v222 offset:5128\n"
    "v_mfma_f32_16x16x32_bf16 a[56:59], v[184:187], v[84:87], a[56:59]\n"
    "v_mfma_f32_16x16x32_bf16 a[96:99], v[184:187], v[88:91], a[96:99]\n"
    "buffer_load_dwordx4 v[52:55], v225, s[40:43], s57 offen offset:3072 sc1\n"
    "v_mfma_f32_16x16x32_bf16 a[136:139], v[184:187], v[92:95], a[136:139]\n"
    "ds_read_b64_tr_b16 v[152:153], v222 offset:6144\n"
    "ds_read_b64_tr_b16 v[154:155], v222 offset:7168\n"
    "v_mfma_f32_16x16x32_bf16 a[20:23], v[188:191], v[80:83], a[20:23]\n"
    "v_mfma_f32_16x16x32_bf16 a[60:63], v[188:191], v[84:87], a[60:63]\n"
    "v_mfma_f32_16x16x32_bf16 a[100:103], v[188:191], v[88:91], a[100:103]\n"
    "ds_read_b64_tr_b16 v[156:157], v222 offset:6152\n"
    "ds_read_b64_tr_b16 v[158:159], v222 offset:7176\n"
    "v_mfma_f32_16x16x32_bf16 a[140:143], v[188:191], v[92:95], a[140:143]\n"
    "buffer_load_dwordx4 v[56:59], v227, s[40:43], s57 offen offset:3072 sc1\n"
    "v_mfma_f32_16x16x32_bf16 a[24:27], v[192:195], v[80:83], a[24:27]\n"
    "v_mfma_f32_16x16x32_bf16 a[64:67], v[192:195], v[84:87], a[64:67]\n"
    "ds_read_b64_tr_b16 v[160:161], v222 offset:8192\n"
    "ds_read_b64_tr_b16 v[162:163], v222 offset:9216\n"
    "v_mfma_f32_16x16x32_bf16 a[104:107], v[192:195], v[88:91], a[104:107]\n"
    "buffer_load_dwordx4 v[60:63], v253, s[40:43], s57 offen offset:3072 sc1\n"
    "v_mfma_f32_16x16x32_bf16 a[144:147], v[192:195], v[92:95], a[144:147]\n"
    "v_mfma_f32_16x16x32_bf16 a[28:31], v[196:199], v[80:83], a[28:31]\n"
    "ds_read_b64_tr_b16 v[164:165], v222 offset:8200\n"
    "ds_read_b64_tr_b16 v[166:167], v222 offset:9224\n"
    "v_mfma_f32_16x16x32_bf16 a[68:71], v[196:199], v[84:87], a[68:71]\n"
    "v_mfma_f32_16x16x32_bf16 a[108:111], v[196:199], v[88:91], a[108:111]\n"
    "s_add_u32 s57, s57, 8192\n"
    "v_mfma_f32_16x16x32_bf16 a[148:151], v[196:199], v[92:95], a[148:151]\n"
    "v_mfma_f32_16x16x32_bf16 a[32:35], v[200:203], v[80:83], a[32:35]\n"
    "v_mfma_f32_16x16x32_bf16 a[72:75], v[200:203], v[84:87], a[72:75]\n"
    "v_mfma_f32_16x16x32_bf16 a[112:115], v[200:203], v[88:91], a[112:115]\n"
    "v_mfma_f32_16x16x32_bf16 a[152:155], v[200:203], v[92:95], a[152:155]\n"
    "v_mfma_f32_16x16x32_bf16 a[36:39], v[204:207], v[80:83], a[36:39]\n"
    "v_mfma_f32_16x16x32_bf16 a[76:79], v[204:207], v[84:87], a[76:79]\n"
    "v_mfma_f32_16x16x32_bf16 a[116:119], v[204:207], v[88:91], a[116:119]\n"
    "v_mfma_f32_16x16x32_bf16 a[156:159], v[204:207], v[92:95], a[156:159]\n"
    "s_waitcnt lgkmcnt(0)\n"
    "v_mfma_f32_16x16x32_bf16 a[0:3], v[128:131], v[96:99], a[0:3]\n"
    "v_mfma_f32_16x16x32_bf16 a[40:43], v[128:131], v[100:103], a[40:43]\n"
    "ds_read_b64_tr_b16 v[168:169], v222 offset:10240\n"
    "ds_read_b64_tr_b16 v[170:171], v222 offset:11264\n"
    "v_mfma_f32_16x16x32_bf16 a[80:83], v[128:131], v[104:107], a[80:83]\n"
    "s_add_u32 m0, s59, 40960\n"
    "v_mfma_f32_16x16x32_bf16 a[120:123], v[128:131], v[108:111], a[120:123]\n"
    "v_mfma_f32_16x16x32_bf16 a[4:7], v[132:135], v[96:99], a[4:7]\n"
    "ds_read_b64_tr_b16 v[172:173], v222 offset:10248\n"
    "ds_read_b64_tr_b16 v[174:175], v222 offset:11272\n"
    "v_mfma_f32_16x16x32_bf16 a[44:47], v[132:135], v[100:103], a[44:47]\n"
    "v_mfma_f32_16x16x32_bf16 a[84:87], v[132:135], v[104:107], a[84:87]\n"
    "buffer_load_dwordx4 v231, s[36:39], s56 offen offset:0 lds\n"
    "s_add_u32 m0, s59, 42944\n"
    "v_mfma_f32_16x16x32_bf16 a[124:127], v[132:135], v[108:111], a[124:127]\n"
    "ds_read_b64_tr_b16 v[176:177], v222 offset:12288\n"
    "ds_read_b64_tr_b16 v[178:179], v222 offset:13312\n"
    "v_mfma_f32_16x16x32_bf16 a[8:11], v[136:139], v[96:99], a[8:11]\n"
    "v_mfma_f32_16x16x32_bf16 a[48:51], v[136:139], v[100:103], a[48:51]\n"
    "v_mfma_f32_16x16x32_bf16 a[88:91], v[136:139], v[104:107], a[88:91]\n"
    "ds_read_b64_tr_b16 v[180:181], v222 offset:12296\n"
    "ds_read_b64_tr_b16 v[182:183], v222 offset:13320\n"
    "v_mfma_f32_16x16x32_bf16 a[128:131], v[136:139], v[108:111], a[128:131]\n"
    "buffer_load_dwordx4 v231, s[36:39], s56 offen offset:64 lds\n"
    "s_add_u32 m0, s59, 44928\n"
    "v_mfma_f32_16x16x32_bf16 a[12:15], v[140:143], v[96:99], a[12:15]\n"
    "v_mfma_f32_16x16x32_bf16 a[52:55], v[140:143], v[100:103], a[52:55]\n"
    "ds_read_b64_tr_b16 v[184:185], v222 offset:14336\n"
    "ds_read_b64_tr_b16 v[186:187], v222 offset:15360\n"
    "v_mfma_f32_16x16x32_bf16 a[92:95], v[140:143], v[104:107], a[92:95]\n"
    "buffer_load_dwordx4 v231, s[36:39], s56 offen offset:128 lds\n"
    "s_add_u32 m0, s59, 46912\n"
    "v_mfma_f32_16x16x32_bf16 a[132:135], v[140:143], v[108:111], a[132:135]\n"
    "v_mfma_f32_16x16x32_bf16 a[16:19], v[144:147], v[96:99], a[16:19]\n"
    "ds_read_b64_tr_b16 v[188:189], v222 offset:14344\n"
    "ds_read_b64_tr_b16 v[190:191], v222 offset:15368\n"
    "v_mfma_f32_16x16x32_bf16 a[56:59], v[144:147], v[100:103], a[56:59]\n"
    "v_mfma_f32_16x16x32_bf16 a[96:99], v[144:147], v[104:107], a[96:99]\n"
    "buffer_load_dwordx4 v231, s[36:39], s56 offen offset:192 lds\n"
    "s_add_u32 m0, s59, 48896\n"
    "v_mfma_f32_16x16x32_bf16 a[136:139], v[144:147], v[108:111], a[136:139]\n"
    "ds_read_b64_tr_b16 v[192:193], v222 offset:16384\n"
    "ds_read_b64_tr_b16 v[194:195], v222 offset:17408\n"
    "v_mfma_f32_16x16x32_bf16 a[20:23], v[148:151], v[96:99], a[20:23]\n"
    "v_mfma_f32_16x16x32_bf16 a[60:63], v[148:151], v[100:103], a[60:63]\n"
    "v_mfma_f32_16x16x32_bf16 a[100:103], v[148:151], v[104:107], a[100:103]\n"
    "ds_read_b64_tr_b16 v[196:197], v222 offset:16392\n"
    "ds_read_b64_tr_b16 v[198:199], v222 offset:17416\n"
    "v_mfma_f32_16x16x32_bf16 a[140:143], v[148:151], v[108:111], a[140:143]\n"
    "buffer_load_dwordx4 v231, s[36:39], s56 offen offset:256 lds\n"
    "s_add_u32 s56, s56, 81920\n"
    "v_mfma_f32_16x16x32_bf16 a[24:27], v[152:155], v[96:99], a[24:27]\n"
    "v_mfma_f32_16x16x32_bf16 a[64:67], v[152:155], v[100:103], a[64:67]\n"
    "ds_read_b64_tr_b16 v[200:201], v222 offset:18432\n"
    "ds_read_b64_tr_b16 v[202:203], v222 offset:19456\n"
    "v_mfma_f32_16x16x32_bf16 a[104:107], v[152:155], v[104:107], a[104:107]\n"
    "buffer_load_dwordx4 v[64:67], v224, s[40:43], s57 offen offset:0 sc1\n"
    "v_mfma_f32_16x16x32_bf16 a[144:147], v[152:155], v[108:111], a[144:147]\n"
    "v_mfma_f32_16x16x32_bf16 a[28:31], v[156:159], v[96:99], a[28:31]\n"
    "ds_read_b64_tr_b16 v[204:205], v222 offset:18440\n"
    "ds_read_b64_tr_b16 v[206:207], v222 offset:19464\n"
    "v_mfma_f32_16x16x32_bf16 a[68:71], v[156:159], v[100:103], a[68:71]\n"
    "v_mfma_f32_16x16x32_bf16 a[108:111], v[156:159], v[104:107], a[108:111]\n"
    "v_mfma_f32_16x16x32_bf16 a[148:151], v[156:159], v[108:111], a[148:151]\n"
    "v_mfma_f32_16x16x32_bf16 a[32:35], v[160:163], v[96:99], a[32:35]\n"
    "v_mfma_f32_16x16x32_bf16 a[72:75], v[160:163], v[100:103], a[72:75]\n"
    "v_mfma_f32_16x16x32_bf16 a[112:115], v[160:163], v[104:107], a[112:115]\n"
    "v_mfma_f32_16x16x32_bf16 a[152:155], v[160:163], v[108:111], a[152:155]\n"
    "v_mfma_f32_16x16x32_bf16 a[36:39], v[164:167], v[96:99], a[36:39]\n"
    "v_mfma_f32_16x16x32_bf16 a[76:79], v[164:167], v[100:103], a[76:79]\n"
    "v_mfma_f32_16x16x32_bf16 a[116:119], v[164:167], v[104:107], a[116:119]\n"
    "v_mfma_f32_16x16x32_bf16 a[156:159], v[164:167], v[108:111], a[156:159]\n"
    "s_waitcnt lgkmcnt(0)\n"
    "v_mfma_f32_16x16x32_bf16 a[0:3], v[168:171], v[112:115], a[0:3]\n"
    "s_waitcnt vmcnt(19)\n"
    "s_barrier\n"
    "v_mfma_f32_16x16x32_bf16 a[40:43], v[168:171], v[116:119], a[40:43]\n"
    "ds_read_b64_tr_b16 v[128:129], v221 offset:0\n"
    "ds_read_b64_tr_b16 v[130:131], v221 offset:1024\n"
    "v_mfma_f32_16x16x32_bf16 a[80:83], v[168:171], v[120:123], a[80:83]\n"
    "buffer_load_dwordx4 v[68:71], v225, s[40:43], s57 offen offset:0 sc1\n"
    "v_mfma_f32_16x16x32_bf16 a[120:123], v[168:171], v[124:127], a[120:123]\n"
    "v_mfma_f32_16x16x32_bf16 a[4:7], v[172:175], v[112:115], a[4:7]\n"
    "ds_read_b64_tr_b16 v[132:133], v221 offset:8\n"
    "ds_read_b64_tr_b16 v[134:135], v221 offset:1032\n"
    "v_mfma_f32_16x16x32_bf16 a[44:47], v[172:175], v[116:119], a[44:47]\n"
    "v_mfma_f32_16x16x32_bf16 a[84:87], v[172:175], v[120:123], a[84:87]\n"
    "buffer_load_dwordx4 v[72:75], v227, s[40:43], s57 offen offset:0 sc1\n"
    "v_mfma_f32_16x16x32_bf16 a[124:127], v[172:175], v[124:127], a[124:127]\n"
    "ds_read_b64_tr_b16 v[136:137], v221 offset:2048\n"
    "ds_read_b64_tr_b16 v[138:139], v221 offset:3072\n"
    "v_mfma_f32_16x16x32_bf16 a[8:11], v[176:179], v[112:115], a[8:11]\n"
    "v_mfma_f32_16x16x32_bf16 a[48:51], v[176:179], v[116:119], a[48:51]\n"
    "v_mfma_f32_16x16x32_bf16 a[88:91], v[176:179], v[120:123], a[88:91]\n"
    "ds_read_b64_tr_b16 v[140:141], v221 offset:2056\n"
    "ds_read_b64_tr_b16 v[142:143], v221 offset:3080\n"
    "v_mfma_f32_16x16x32_bf16 a[128:131], v[176:179], v[124:127], a[128:131]\n"
    "buffer_load_dwordx4 v[76:79], v253, s[40:43], s57 offen offset:0 sc1\n"
    "v_mfma_f32_16x16x32_bf16 a[12:15], v[180:183], v[112:115], a[12:15]\n"
    "v_mfma_f32_16x16x32_bf16 a[52:55], v[180:183], v[116:119], a[52:55]\n"
    "ds_read_b64_tr_b16 v[144:145], v221 offset:4096\n"
    "ds_read_b64_tr_b16 v[146:147], v221 offset:5120\n"
    "v_mfma_f32_16x16x32_bf16 a[92:95], v[180:183], v[120:123], a[92:95]\n"
    "buffer_load_dwordx4 v[80:83], v224, s[40:43], s57 offen offset:1024 sc1\n"
    "v_mfma_f32_16x16x32_bf16 a[132:135], v[180:183], v[124:127], a[132:135]\n"
    "v_mfma_f32_16x16x32_bf16 a[16:19], v[184:187], v[112:115], a[16:19]\n"
    "ds_read_b64_tr_b16 v[148:149], v221 offset:4104\n"
    "ds_read_b64_tr_b16 v[150:151], v221 offset:5128\n"
    "v_mfma_f32_16x16x32_bf16 a[56:59], v[184:187], v[116:119], a[56:59]\n"
    "v_mfma_f32_16x16x32_bf16 a[96:99], v[184:187], v[120:123], a[96:99]\n"
    "buffer_load_dwordx4 v[84:87], v225, s[40:43], s57 offen offset:1024 sc1\n"
    "v_mfma_f32_16x16x32_bf16 a[136:139], v[184:187], v[124:127], a[136:139]\n"
    "ds_read_b64_tr_b16 v[152:153], v221 offset:6144\n"
    "ds_read_b64_tr_b16 v[154:155], v221 offset:7168\n"
    "v_mfma_f32_16x16x32_bf16 a[20:23], v[188:191], v[112:115], a[20:23]\n"
    "v_mfma_f32_16x16x32_bf16 a[60:63], v[188:191], v[116:119], a[60:63]\n"
    "v_mfma_f32_16x16x32_bf16 a[100:103], v[188:191], v[120:123], a[100:103]\n"
    "ds_read_b64_tr_b16 v[156:157], v221 offset:6152\n"
    "ds_read_b64_tr_b16 v[158:159], v221 offset:7176\n"
    "v_mfma_f32_16x16x32_bf16 a[140:143], v[188:191], v[124:127], a[140:143]\n"
    "buffer_load_dwordx4 v[88:91], v227, s[40:43], s57 offen offset:1024 sc1\n"
    "v_mfma_f32_16x16x32_bf16 a[24:27], v[192:195], v[112:115], a[24:27]\n"
    "v_mfma_f32_16x16x32_bf16 a[64:67], v[192:195], v[116:119], a[64:67]\n"
    "ds_read_b64_tr_b16 v[160:161], v221 offset:8192\n"
    "ds_read_b64_tr_b16 v[162:163], v221 offset:9216\n"
    "v_mfma_f32_16x16x32_bf16 a[104:107], v[192:195], v[120:123], a[104:107]\n"
    "buffer_load_dwordx4 v[92:95], v253, s[40:43], s57 offen offset:1024 sc1\n"
    "v_mfma_f32_16x16x32_bf16 a[144:147], v[192:195], v[124:127], a[144:147]\n"
    "v_mfma_f32_16x16x32_bf16 a[28:31], v[196:199], v[112:115], a[28:31]\n"
    "ds_read_b64_tr_b16 v[164:165], v221 offset:8200\n"
    "ds_read_b64_tr_b16 v[166:167], v221 offset:9224\n"
    "v_mfma_f32_16x16x32_bf16 a[68:71], v[196:199], v[116:119], a[68:71]\n"
    "v_mfma_f32_16x16x32_bf16 a[108:111], v[196:199], v[120:123], a[108:111]\n"
    "v_mfma_f32_16x16x32_bf16 a[148:151], v[196:199], v[124:127], a[148:151]\n"
    "v_mfma_f32_16x16x32_bf16 a[32:35], v[200:203], v[112:115], a[32:35]\n"
    "v_mfma_f32_16x16x32_bf16 a[72:75], v[200:203], v[116:119], a[72:75]\n"
    "v_mfma_f32_16x16x32_bf16 a[112:115], v[200:203], v[120:123], a[112:115]\n"
    "v_mfma_f32_16x16x32_bf16 a[152:155], v[200:203], v[124:127], a[152:155]\n"
    "v_mfma_f32_16x16x32_bf16 a[36:39], v[204:207], v[112:115], a[36:39]\n"
    "v_mfma_f32_16x16x32_bf16 a[76:79], v[204:207], v[116:119], a[76:79]\n"
    "v_mfma_f32_16x16x32_bf16 a[116:119], v[204:207], v[120:123], a[116:119]\n"
    "v_mfma_f32_16x16x32_bf16 a[156:159], v[204:207], v[124:127], a[156:159]\n"
    "s_waitcnt lgkmcnt(0)\n"
    "s_sub_u32 s58, s58, 1\n"
    "s_cmp_lg_u32 s58, 0\n"
    "s_cbranch_scc1 K2L%=\n"
    "v_mfma_f32_16x16x32_bf16 a[0:3], v[128:131], v[0:3], a[0:3]\n"
    "v_mfma_f32_16x16x32_bf16 a[40:43], v[128:131], v[4:7], a[40:43]\n"
    "ds_read_b64_tr_b16 v[168:169], v221 offset:10240\n"
    "ds_read_b64_tr_b16 v[170:171], v221 offset:11264\n"
    "v_mfma_f32_16x16x32_bf16 a[80:83], v[128:131], v[8:11], a[80:83]\n"
    "s_add_u32 m0, s59, 61440\n"
    "v_mfma_f32_16x16x32_bf16 a[120:123], v[128:131], v[12:15], a[120:123]\n"
    "v_mfma_f32_16x16x32_bf16 a[4:7], v[132:135], v[0:3], a[4:7]\n"
    "ds_read_b64_tr_b16 v[172:173], v221 offset:10248\n"
    "ds_read_b64_tr_b16 v[174:175], v221 offset:11272\n"
    "v_mfma_f32_16x16x32_bf16 a[44:47], v[132:135], v[4:7], a[44:47]\n"
    "v_mfma_f32_16x16x32_bf16 a[84:87], v[132:135], v[8:11], a[84:87]\n"
    "buffer_load_dwordx4 v231, s[36:39], s56 offen offset:0 lds\n"
    "s_add_u32 m0, s59, 63424\n"
    "v_mfma_f32_16x16x32_bf16 a[124:127], v[132:135], v[12:15], a[124:127]\n"
    "ds_read_b64_tr_b16 v[176:177], v221 offset:12288\n"
    "ds_read_b64_tr_b16 v[178:179], v221 offset:13312\n"
    "v_mfma_f32_16x16x32_bf16 a[8:11], v[136:139], v[0:3], a[8:11]\n"
    "v_mfma_f32_16x16x32_bf16 a[48:51], v[136:139], v[4:7], a[48:51]\n"
    "v_mfma_f32_16x16x32_bf16 a[88:91], v[136:139], v[8:11], a[88:91]\n"
    "ds_read_b64_tr_b16 v[180:181], v221 offset:12296\n"
    "ds_read_b64_tr_b16 v[182:183], v221 offset:13320\n"
    "v_mfma_f32_16x16x32_bf16 a[128:131], v[136:139], v[12:15], a[128:131]\n"
    "buffer_load_dwordx4 v231, s[36:39], s56 offen offset:64 lds\n"
    "s_add_u32 m0, s59, 65408\n"
    "v_mfma_f32_16x16x32_bf16 a[12:15], v[140:143], v[0:3], a[12:15]\n"
    "v_mfma_f32_16x16x32_bf16 a[52:55], v[140:143], v[4:7], a[52:55]\n"
    "ds_read_b64_tr_b16 v[184:185], v221 offset:14336\n"
    "ds_read_b64_tr_b16 v[186:187], v221 offset:15360\n"
    "v_mfma_f32_16x16x32_bf16 a[92:95], v[140:143], v[8:11], a[92:95]\n"
    "buffer_load_dwordx4 v231, s[36:39], s56 offen offset:128 lds\n"
    "s_add_u32 m0, s59, 67392\n"
    "v_mfma_f32_16x16x32_bf16 a[132:135], v[140:143], v[12:15], a[132:135]\n"
    "v_mfma_f32_16x16x32_bf16 a[16:19], v[144:147], v[0:3], a[16:19]\n"
    "ds_read_b64_tr_b16 v[188:189], v221 offset:14344\n"
    "ds_read_b64_tr_b16 v[190:191], v221 offset:15368\n"
    "v_mfma_f32_16x16x32_bf16 a[56:59], v[144:147], v[4:7], a[56:59]\n"
    "v_mfma_f32_16x16x32_bf16 a[96:99], v[144:147], v[8:11], a[96:99]\n"
    "buffer_load_dwordx4 v231, s[36:39], s56 offen offset:192 lds\n"
    "s_add_u32 m0, s59, 69376\n"
    "v_mfma_f32_16x16x32_bf16 a[136:139], v[144:147], v[12:15], a[136:139]\n"
    "ds_read_b64_tr_b16 v[192:193], v221 offset:16384\n"
    "ds_read_b64_tr_b16 v[194:195], v221 offset:17408\n"
    "v_mfma_f32_16x16x32_bf16 a[20:23], v[148:151], v[0:3], a[20:23]\n"
    "v_mfma_f32_16x16x32_bf16 a[60:63], v[148:151], v[4:7], a[60:63]\n"
    "v_mfma_f32_16x16x32_bf16 a[100:103], v[148:151], v[8:11], a[100:103]\n"
    "ds_read_b64_tr_b16 v[196:197], v221 offset:16392\n"
    "ds_read_b64_tr_b16 v[198:199], v221 offset:17416\n"
    "v_mfma_f32_16x16x32_bf16 a[140:143], v[148:151], v[12:15], a[140:143]\n"
    "buffer_load_dwordx4 v231, s[36:39], s56 offen offset:256 lds\n"
    "s_add_u32 s56, s56, 245760\n"
    "v_mfma_f32_16x16x32_bf16 a[24:27], v[152:155], v[0:3], a[24:27]\n"
    "v_mfma_f32_16x16x32_bf16 a[64:67], v[152:155], v[4:7], a[64:67]\n"
    "ds_read_b64_tr_b16 v[200:201], v221 offset:18432\n"
    "ds_read_b64_tr_b16 v[202:203], v221 offset:19456\n"
    "v_mfma_f32_16x16x32_bf16 a[104:107], v[152:155], v[8:11], a[104:107]\n"
    "buffer_load_dwordx4 v[96:99], v224, s[40:43], s57 offen offset:2048 sc1\n"
    "v_mfma_f32_16x16x32_bf16 a[144:147], v[152:155], v[12:15], a[144:147]\n"
    "v_mfma_f32_16x16x32_bf16 a[28:31], v[156:159], v[0:3], a[28:31]\n"
    "ds_read_b64_tr_b16 v[204:205], v221 offset:18440\n"
    "ds_read_b64_tr_b16 v[206:207], v221 offset:19464\n"
    "v_mfma_f32_16x16x32_bf16 a[68:71], v[156:159], v[4:7], a[68:71]\n"
    "v_mfma_f32_16x16x32_bf16 a[108:111], v[156:159], v[8:11], a[108:111]\n"
    "v_mfma_f32_16x16x32_bf16 a[148:151], v[156:159], v[12:15], a[148:151]\n"
    "v_mfma_f32_16x16x32_bf16 a[32:35], v[160:163], v[0:3], a[32:35]\n"
    "v_mfma_f32_16x16x32_bf16 a[72:75], v[160:163], v[4:7], a[72:75]\n"
    "v_mfma_f32_16x16x32_bf16 a[112:115], v[160:163], v[8:11], a[112:115]\n"
    "v_mfma_f32_16x16x32_bf16 a[152:155], v[160:163], v[12:15], a[152:155]\n"
    "v_mfma_f32_16x16x32_bf16 a[36:39], v[164:167], v[0:3], a[36:39]\n"
    "v_mfma_f32_16x16x32_bf16 a[76:79], v[164:167], v[4:7], a[76:79]\n"
    "v_mfma_f32_16x16x32_bf16 a[116:119], v[164:167], v[8:11], a[116:119]\n"
    "v_mfma_f32_16x16x32_bf16 a[156:159], v[164:167], v[12:15], a[156:159]\n"
    "s_waitcnt lgkmcnt(0)\n"
    "v_mfma_f32_16x16x32_bf16 a[0:3], v[168:171], v[16:19], a[0:3]\n"
    "s_waitcnt vmcnt(19)\n"
    "s_barrier\n"
    "v_mfma_f32_16x16x32_bf16 a[40:43], v[168:171], v[20:23], a[40:43]\n"
    "ds_read_b64_tr_b16 v[128:129], v221 offset:20480\n"
    "ds_read_b64_tr_b16 v[130:131], v221 offset:21504\n"
    "v_mfma_f32_16x16x32_bf16 a[80:83], v[168:171], v[24:27], a[80:83]\n"
    "buffer_load_dwordx4 v[100:103], v225, s[40:43], s57 offen offset:2048 sc1\n"
    "v_mfma_f32_16x16x32_bf16 a[120:123], v[168:171], v[28:31], a[120:123]\n"
    "v_mfma_f32_16x16x32_bf16 a[4:7], v[172:175], v[16:19], a[4:7]\n"
    "ds_read_b64_tr_b16 v[132:133], v221 offset:20488\n"
    "ds_read_b64_tr_b16 v[134:135], v221 offset:21512\n"
    "v_mfma_f32_16x16x32_bf16 a[44:47], v[172:175], v[20:23], a[44:47]\n"
    "v_mfma_f32_16x16x32_bf16 a[84:87], v[172:175], v[24:27], a[84:87]\n"
    "buffer_load_dwordx4 v[104:107], v227, s[40:43], s57 offen offset:2048 sc1\n"
    "v_mfma_f32_16x16x32_bf16 a[124:127], v[172:175], v[28:31], a[124:127]\n"
    "ds_read_b64_tr_b16 v[136:137], v221 offset:22528\n"
    "ds_read_b64_tr_b16 v[138:139], v221 offset:23552\n"
    "v_mfma_f32_16x16x32_bf16 a[8:11], v[176:179], v[16:19], a[8:11]\n"
    "v_mfma_f32_16x16x32_bf16 a[48:51], v[176:179], v[20:23], a[48:51]\n"
    "v_mfma_f32_16x16x32_bf16 a[88:91], v[176:179], v[24:27], a[88:91]\n"
    "ds_read_b64_tr_b16 v[140:141], v221 offset:22536\n"
    "ds_read_b64_tr_b16 v[142:143], v221 offset:23560\n"
    "v_mfma_f32_16x16x32_bf16 a[128:131], v[176:179], v[28:31], a[128:131]\n"
    "buffer_load_dwordx4 v[108:111], v253, s[40:43], s57 offen offset:2048 sc1\n"
    "v_mfma_f32_16x16x32_bf16 a[12:15], v[180:183], v[16:19], a[12:15]\n"
    "v_mfma_f32_16x16x32_bf16 a[52:55], v[180:183], v[20:23], a[52:55]\n"
    "ds_read_b64_tr_b16 v[144:145], v221 offset:24576\n"
    "ds_read_b64_tr_b16 v[146:147], v221 offset:25600\n"
    "v_mfma_f32_16x16x32_bf16 a[92:95], v[180:183], v[24:27], a[92:95]\n"
    "buffer_load_dwordx4 v[112:115], v224, s[40:43], s57 offen offset:3072 sc1\n"
    "v_mfma_f32_16x16x32_bf16 a[132:135], v[180:183], v[28:31], a[132:135]\n"
    "v_mfma_f32_16x16x32_bf16 a[16:19], v[184:187], v[16:19], a[16:19]\n"
    "ds_read_b64_tr_b16 v[148:149], v221 offset:24584\n"
    "ds_read_b64_tr_b16 v[150:151], v221 offset:25608\n"
    "v_mfma_f32_16x16x32_bf16 a[56:59], v[184:187], v[20:23], a[56:59]\n"
    "v_mfma_f32_16x16x32_bf16 a[96:99], v[184:187], v[24:27], a[96:99]\n"
    "buffer_load_dwordx4 v[116:119], v225, s[40:43], s57 offen offset:3072 sc1\n"
    "v_mfma_f32_16x16x32_bf16 a[136:139], v[184:187], v[28:31], a[136:139]\n"
    "ds_read_b64_tr_b16 v[152:153], v221 offset:26624\n"
    "ds_read_b64_tr_b16 v[154:155], v221 offset:27648\n"
    "v_mfma_f32_16x16x32_bf16 a[20:23], v[188:191], v[16:19], a[20:23]\n"
    "v_mfma_f32_16x16x32_bf16 a[60:63], v[188:191], v[20:23], a[60:63]\n"
    "v_mfma_f32_16x16x32_bf16 a[100:103], v[188:191], v[24:27], a[100:103]\n"
    "ds_read_b64_tr_b16 v[156:157], v221 offset:26632\n"
    "ds_read_b64_tr_b16 v[158:159], v221 offset:27656\n"
    "v_mfma_f32_16x16x32_bf16 a[140:143], v[188:191], v[28:31], a[140:143]\n"
    "buffer_load_dwordx4 v[120:123], v227, s[40:43], s57 offen offset:3072 sc1\n"
    "v_mfma_f32_16x16x32_bf16 a[24:27], v[192:195], v[16:19], a[24:27]\n"
    "v_mfma_f32_16x16x32_bf16 a[64:67], v[192:195], v[20:23], a[64:67]\n"
    "ds_read_b64_tr_b16 v[160:161], v221 offset:28672\n"
    "ds_read_b64_tr_b16 v[162:163], v221 offset:29696\n"
    "v_mfma_f32_16x16x32_bf16 a[104:107], v[192:195], v[24:27], a[104:107]\n"
    "buffer_load_dwordx4 v[124:127], v253, s[40:43], s57 offen offset:3072 sc1\n"
    "v_mfma_f32_16x16x32_bf16 a[144:147], v[192:195], v[28:31], a[144:147]\n"
    "v_mfma_f32_16x16x32_bf16 a[28:31], v[196:199], v[16:19], a[28:31]\n"
    "ds_read_b64_tr_b16 v[164:165], v221 offset:28680\n"
    "ds_read_b64_tr_b16 v[166:167], v221 offset:29704\n"
    "v_mfma_f32_16x16x32_bf16 a[68:71], v[196:199], v[20:23], a[68:71]\n"
    "v_mfma_f32_16x16x32_bf16 a[108:111], v[196:199], v[24:27], a[108:111]\n"
    "s_add_u32 s57, s57, 8192\n"
    "s_cmp_eq_u32 s57, 32768\n"
    "s_cselect_b32 s57, 4096, s57\n"
    "s_cselect_b32 s60, 1146880, 0\n"
    "v_mfma_f32_16x16x32_bf16 a[148:151], v[196:199], v[28:31], a[148:151]\n"
    "v_mfma_f32_16x16x32_bf16 a[32:35], v[200:203], v[16:19], a[32:35]\n"
    "v_mfma_f32_16x16x32_bf16 a[72:75], v[200:203], v[20:23], a[72:75]\n"
    "v_mfma_f32_16x16x32_bf16 a[112:115], v[200:203], v[24:27], a[112:115]\n"
    "v_mfma_f32_16x16x32_bf16 a[152:155], v[200:203], v[28:31], a[152:155]\n"
    "s_sub_u32 s56, s56, s60\n"
    "v_mfma_f32_16x16x32_bf16 a[36:39], v[204:207], v[16:19], a[36:39]\n"
    "v_mfma_f32_16x16x32_bf16 a[76:79], v[204:207], v[20:23], a[76:79]\n"
    "v_mfma_f32_16x16x32_bf16 a[116:119], v[204:207], v[24:27], a[116:119]\n"
    "v_mfma_f32_16x16x32_bf16 a[156:159], v[204:207], v[28:31], a[156:159]\n"
    "s_waitcnt lgkmcnt(0)\n"
    "v_mfma_f32_16x16x32_bf16 a[0:3], v[128:131], v[32:35], a[0:3]\n"
    "v_mfma_f32_16x16x32_bf16 a[40:43], v[128:131], v[36:39], a[40:43]\n"
    "ds_read_b64_tr_b16 v[168:169], v221 offset:30720\n"
    "ds_read_b64_tr_b16 v[170:171], v221 offset:31744\n"
    "v_mfma_f32_16x16x32_bf16 a[80:83], v[128:131], v[40:43], a[80:83]\n"
    "v_mfma_f32_16x16x32_bf16 a[120:123], v[128:131], v[44:47], a[120:123]\n"
    "v_mfma_f32_16x16x32_bf16 a[4:7], v[132:135], v[32:35], a[4:7]\n"
    "ds_read_b64_tr_b16 v[172:173], v221 offset:30728\n"
    "ds_read_b64_tr_b16 v[174:175], v221 offset:31752\n"
    "v_mfma_f32_16x16x32_bf16 a[44:47], v[132:135], v[36:39], a[44:47]\n"
    "v_mfma_f32_16x16x32_bf16 a[84:87], v[132:135], v[40:43], a[84:87]\n"
    "v_mfma_f32_16x16x32_bf16 a[124:127], v[132:135], v[44:47], a[124:127]\n"
    "ds_read_b64_tr_b16 v[176:177], v221 offset:32768\n"
    "ds_read_b64_tr_b16 v[178:179], v221 offset:33792\n"
    "v_mfma_f32_16x16x32_bf16 a[8:11], v[136:139], v[32:35], a[8:11]\n"
    "v_mfma_f32_16x16x32_bf16 a[48:51], v[136:139], v[36:39], a[48:51]\n"
    "v_mfma_f32_16x16x32_bf16 a[88:91], v[136:139], v[40:43], a[88:91]\n"
    "ds_read_b64_tr_b16 v[180:181], v221 offset:32776\n"
    "ds_read_b64_tr_b16 v[182:183], v221 offset:33800\n"
    "v_mfma_f32_16x16x32_bf16 a[128:131], v[136:139], v[44:47], a[128:131]\n"
    "v_mfma_f32_16x16x32_bf16 a[12:15], v[140:143], v[32:35], a[12:15]\n"
    "v_mfma_f32_16x16x32_bf16 a[52:55], v[140:143], v[36:39], a[52:55]\n"
    "ds_read_b64_tr_b16 v[184:185], v221 offset:34816\n"
    "ds_read_b64_tr_b16 v[186:187], v221 offset:35840\n"
    "v_mfma_f32_16x16x32_bf16 a[92:95], v[140:143], v[40:43], a[92:95]\n"
    "v_mfma_f32_16x16x32_bf16 a[132:135], v[140:143], v[44:47], a[132:135]\n"
    "v_mfma_f32_16x16x32_bf16 a[16:19], v[144:147], v[32:35], a[16:19]\n"
    "ds_read_b64_tr_b16 v[188:189], v221 offset:34824\n"
    "ds_read_b64_tr_b16 v[190:191], v221 offset:35848\n"
    "v_mfma_f32_16x16x32_bf16 a[56:59], v[144:147], v[36:39], a[56:59]\n"
    "v_mfma_f32_16x16x32_bf16 a[96:99], v[144:147], v[40:43], a[96:99]\n"
    "v_mfma_f32_16x16x32_bf16 a[136:139], v[144:147], v[44:47], a[136:139]\n"
    "ds_read_b64_tr_b16 v[192:193], v221 offset:36864\n"
    "ds_read_b64_tr_b16 v[194:195], v221 offset:37888\n"
    "v_mfma_f32_16x16x32_bf16 a[20:23], v[148:151], v[32:35], a[20:23]\n"
    "v_mfma_f32_16x16x32_bf16 a[60:63], v[148:151], v[36:39], a[60:63]\n"
    "v_mfma_f32_16x16x32_bf16 a[100:103], v[148:151], v[40:43], a[100:103]\n"
    "ds_read_b64_tr_b16 v[196:197], v221 offset:36872\n"
    "ds_read_b64_tr_b16 v[198:199], v221 offset:37896\n"
    "v_mfma_f32_16x16x32_bf16 a[140:143], v[148:151], v[44:47], a[140:143]\n"
    "v_mfma_f32_16x16x32_bf16 a[24:27], v[152:155], v[32:35], a[24:27]\n"
    "v_mfma_f32_16x16x32_bf16 a[64:67], v[152:155], v[36:39], a[64:67]\n"
    "ds_read_b64_tr_b16 v[200:201], v221 offset:38912\n"
    "ds_read_b64_tr_b16 v[202:203], v221 offset:39936\n"
    "v_mfma_f32_16x16x32_bf16 a[104:107], v[152:155], v[40:43], a[104:107]\n"
    "v_mfma_f32_16x16x32_bf16 a[144:147], v[152:155], v[44:47], a[144:147]\n"
    "v_mfma_f32_16x16x32_bf16 a[28:31], v[156:159], v[32:35], a[28:31]\n"
    "ds_read_b64_tr_b16 v[204:205], v221 offset:38920\n"
    "ds_read_b64_tr_b16 v[206:207], v221 offset:39944\n"
    "v_mfma_f32_16x16x32_bf16 a[68:71], v[156:159], v[36:39], a[68:71]\n"
    "v_mfma_f32_16x16x32_bf16 a[108:111], v[156:159], v[40:43], a[108:111]\n"
    "v_mfma_f32_16x16x32_bf16 a[148:151], v[156:159], v[44:47], a[148:151]\n"
    "v_mfma_f32_16x16x32_bf16 a[32:35], v[160:163], v[32:35], a[32:35]\n"
    "v_mfma_f32_16x16x32_bf16 a[72:75], v[160:163], v[36:39], a[72:75]\n"
    "v_mfma_f32_16x16x32_bf16 a[112:115], v[160:163], v[40:43], a[112:115]\n"
    "v_mfma_f32_16x16x32_bf16 a[152:155], v[160:163], v[44:47], a[152:155]\n"
    "v_mfma_f32_16x16x32_bf16 a[36:39], v[164:167], v[32:35], a[36:39]\n"
    "v_mfma_f32_16x16x32_bf16 a[76:79], v[164:167], v[36:39], a[76:79]\n"
    "v_mfma_f32_16x16x32_bf16 a[116:119], v[164:167], v[40:43], a[116:119]\n"
    "v_mfma_f32_16x16x32_bf16 a[156:159], v[164:167], v[44:47], a[156:159]\n"
    "s_waitcnt lgkmcnt(0)\n"
    "v_mfma_f32_16x16x32_bf16 a[0:3], v[168:171], v[48:51], a[0:3]\n"
    "s_waitcnt vmcnt(13)\n"
    "s_barrier\n"
    "v_mfma_f32_16x16x32_bf16 a[40:43], v[168:171], v[52:55], a[40:43]\n"
    "ds_read_b64_tr_b16 v[128:129], v221 offset:40960\n"
    "ds_read_b64_tr_b16 v[130:131], v221 offset:41984\n"
    "v_mfma_f32_16x16x32_bf16 a[80:83], v[168:171], v[56:59], a[80:83]\n"
    "v_mfma_f32_16x16x32_bf16 a[120:123], v[168:171], v[60:63], a[120:123]\n"
    "v_mfma_f32_16x16x32_bf16 a[4:7], v[172:175], v[48:51], a[4:7]\n"
    "ds_read_b64_tr_b16 v[132:133], v221 offset:40968\n"
    "ds_read_b64_tr_b16 v[134:135], v221 offset:41992\n"
    "v_mfma_f32_16x16x32_bf16 a[44:47], v[172:175], v[52:55], a[44:47]\n"
    "v_mfma_f32_16x16x32_bf16 a[84:87], v[172:175], v[56:59], a[84:87]\n"
    "v_mfma_f32_16x16x32_bf16 a[124:127], v[172:175], v[60:63], a[124:127]\n"
    "ds_read_b64_tr_b16 v[136:137], v221 offset:43008\n"
    "ds_read_b64_tr_b16 v[138:139], v221 offset:44032\n"
    "v_mfma_f32_16x16x32_bf16 a[8:11], v[176:179], v[48:51], a[8:11]\n"
    "v_mfma_f32_16x16x32_bf16 a[48:51], v[176:179], v[52:55], a[48:51]\n"
    "v_mfma_f32_16x16x32_bf16 a[88:91], v[176:179], v[56:59], a[88:91]\n"
    "ds_read_b64_tr_b16 v[140:141], v221 offset:43016\n"
    "ds_read_b64_tr_b16 v[142:143], v221 offset:44040\n"
    "v_mfma_f32_16x16x32_bf16 a[128:131], v[176:179], v[60:63], a[128:131]\n"
    "v_mfma_f32_16x16x32_bf16 a[12:15], v[180:183], v[48:51], a[12:15]\n"
    "v_mfma_f32_16x16x32_bf16 a[52:55], v[180:183], v[52:55], a[52:55]\n"
    "ds_read_b64_tr_b16 v[144:145], v221 offset:45056\n"
    "ds_read_b64_tr_b16 v[146:147], v221 offset:46080\n"
    "v_mfma_f32_16x16x32_bf16 a[92:95], v[180:183], v[56:59], a[92:95]\n"
    "v_mfma_f32_16x16x32_bf16 a[132:135], v[180:183], v[60:63], a[132:135]\n"
    "v_mfma_f32_16x16x32_bf16 a[16:19], v[184:187], v[48:51], a[16:19]\n"
    "ds_read_b64_tr_b16 v[148:149], v221 offset:45064\n"
    "ds_read_b64_tr_b16 v[150:151], v221 offset:46088\n"
    "v_mfma_f32_16x16x32_bf16 a[56:59], v[184:187], v[52:55], a[56:59]\n"
    "v_mfma_f32_16x16x32_bf16 a[96:99], v[184:187], v[56:59], a[96:99]\n"
    "v_mfma_f32_16x16x32_bf16 a[136:139], v[184:187], v[60:63], a[136:139]\n"
    "ds_read_b64_tr_b16 v[152:153], v221 offset:47104\n"
    "ds_read_b64_tr_b16 v[154:155], v221 offset:48128\n"
    "v_mfma_f32_16x16x32_bf16 a[20:23], v[188:191], v[48:51], a[20:23]\n"
    "v_mfma_f32_16x16x32_bf16 a[60:63], v[188:191], v[52:55], a[60:63]\n"
    "v_mfma_f32_16x16x32_bf16 a[100:103], v[188:191], v[56:59], a[100:103]\n"
    "ds_read_b64_tr_b16 v[156:157], v221 offset:47112\n"
    "ds_read_b64_tr_b16 v[158:159], v221 offset:48136\n"
    "v_mfma_f32_16x16x32_bf16 a[140:143], v[188:191], v[60:63], a[140:143]\n"
    "v_mfma_f32_16x16x32_bf16 a[24:27], v[192:195], v[48:51], a[24:27]\n"
    "v_mfma_f32_16x16x32_bf16 a[64:67], v[192:195], v[52:55], a[64:67]\n"
    "ds_read_b64_tr_b16 v[160:161], v221 offset:49152\n"
    "ds_read_b64_tr_b16 v[162:163], v221 offset:50176\n"
    "v_mfma_f32_16x16x32_bf16 a[104:107], v[192:195], v[56:59], a[104:107]\n"
    "v_mfma_f32_16x16x32_bf16 a[144:147], v[192:195], v[60:63], a[144:147]\n"
    "v_mfma_f32_16x16x32_bf16 a[28:31], v[196:199], v[48:51], a[28:31]\n"
    "ds_read_b64_tr_b16 v[164:165], v221 offset:49160\n"
    "ds_read_b64_tr_b16 v[166:167], v221 offset:50184\n"
    "v_mfma_f32_16x16x32_bf16 a[68:71], v[196:199], v[52:55], a[68:71]\n"
    "v_mfma_f32_16x16x32_bf16 a[108:111], v[196:199], v[56:59], a[108:111]\n"
    "v_mfma_f32_16x16x32_bf16 a[148:151], v[196:199], v[60:63], a[148:151]\n"
    "v_mfma_f32_16x16x32_bf16 a[32:35], v[200:203], v[48:51], a[32:35]\n"
    "v_mfma_f32_16x16x32_bf16 a[72:75], v[200:203], v[52:55], a[72:75]\n"
    "v_mfma_f32_16x16x32_bf16 a[112:115], v[200:203], v[56:59], a[112:115]\n"
    "v_mfma_f32_16x16x32_bf16 a[152:155], v[200:203], v[60:63], a[152:155]\n"
    "v_mfma_f32_16x16x32_bf16 a[36:39], v[204:207], v[48:51], a[36:39]\n"
    "v_mfma_f32_16x16x32_bf16 a[76:79], v[204:207], v[52:55], a[76:79]\n"
    "v_mfma_f32_16x16x32_bf16 a[116:119], v[204:207], v[56:59], a[116:119]\n"
    "v_mfma_f32_16x16x32_bf16 a[156:159], v[204:207], v[60:63], a[156:159]\n"
    "s_waitcnt lgkmcnt(0)\n"
    "v_mfma_f32_16x16x32_bf16 a[0:3], v[128:131], v[64:67], a[0:3]\n"
    "v_mfma_f32_16x16x32_bf16 a[40:43], v[128:131], v[68:71], a[40:43]\n"
    "ds_read_b64_tr_b16 v[168:169], v221 offset:51200\n"
    "ds_read_b64_tr_b16 v[170:171], v221 offset:52224\n"
    "v_mfma_f32_16x16x32_bf16 a[80:83], v[128:131], v[72:75], a[80:83]\n"
    "v_mfma_f32_16x16x32_bf16 a[120:123], v[128:131], v[76:79], a[120:123]\n"
    "v_mfma_f32_16x16x32_bf16 a[4:7], v[132:135], v[64:67], a[4:7]\n"
    "ds_read_b64_tr_b16 v[172:173], v221 offset:51208\n"
    "ds_read_b64_tr_b16 v[174:175], v221 offset:52232\n"
    "v_mfma_f32_16x16x32_bf16 a[44:47], v[132:135], v[68:71], a[44:47]\n"
    "v_mfma_f32_16x16x32_bf16 a[84:87], v[132:135], v[72:75], a[84:87]\n"
    "v_mfma_f32_16x16x32_bf16 a[124:127], v[132:135], v[76:79], a[124:127]\n"
    "ds_read_b64_tr_b16 v[176:177], v221 offset:53248\n"
    "ds_read_b64_tr_b16 v[178:179], v221 offset:54272\n"
    "v_mfma_f32_16x16x32_bf16 a[8:11], v[136:139], v[64:67], a[8:11]\n"
    "v_mfma_f32_16x16x32_bf16 a[48:51], v[136:139], v[68:71], a[48:51]\n"
    "v_mfma_f32_16x16x32_bf16 a[88:91], v[136:139], v[72:75], a[88:91]\n"
    "ds_read_b64_tr_b16 v[180:181], v221 offset:53256\n"
    "ds_read_b64_tr_b16 v[182:183], v221 offset:54280\n"
    "v_mfma_f32_16x16x32_bf16 a[128:131], v[136:139], v[76:79], a[128:131]\n"
    "v_mfma_f32_16x16x32_bf16 a[12:15], v[140:143], v[64:67], a[12:15]\n"
    "v_mfma_f32_16x16x32_bf16 a[52:55], v[140:143], v[68:71], a[52:55]\n"
    "ds_read_b64_tr_b16 v[184:185], v221 offset:55296\n"
    "ds_read_b64_tr_b16 v[186:187], v221 offset:56320\n"
    "v_mfma_f32_16x16x32_bf16 a[92:95], v[140:143], v[72:75], a[92:95]\n"
    "v_mfma_f32_16x16x32_bf16 a[132:135], v[140:143], v[76:79], a[132:135]\n"
    "v_mfma_f32_16x16x32_bf16 a[16:19], v[144:147], v[64:67], a[16:19]\n"
    "ds_read_b64_tr_b16 v[188:189], v221 offset:55304\n"
    "ds_read_b64_tr_b16 v[190:191], v221 offset:56328\n"
    "v_mfma_f32_16x16x32_bf16 a[56:59], v[144:147], v[68:71], a[56:59]\n"
    "v_mfma_f32_16x16x32_bf16 a[96:99], v[144:147], v[72:75], a[96:99]\n"
    "v_mfma_f32_16x16x32_bf16 a[136:139], v[144:147], v[76:79], a[136:139]\n"
    "ds_read_b64_tr_b16 v[192:193], v221 offset:57344\n"
    "ds_read_b64_tr_b16 v[194:195], v221 offset:58368\n"
    "v_mfma_f32_16x16x32_bf16 a[20:23], v[148:151], v[64:67], a[20:23]\n"
    "v_mfma_f32_16x16x32_bf16 a[60:63], v[148:151], v[68:71], a[60:63]\n"
    "v_mfma_f32_16x16x32_bf16 a[100:103], v[148:151], v[72:75], a[100:103]\n"
    "ds_read_b64_tr_b16 v[196:197], v221 offset:57352\n"
    "ds_read_b64_tr_b16 v[198:199], v221 offset:58376\n"
    "v_mfma_f32_16x16x32_bf16 a[140:143], v[148:151], v[76:79], a[140:143]\n"
    "v_mfma_f32_16x16x32_bf16 a[24:27], v[152:155], v[64:67], a[24:27]\n"
    "v_mfma_f32_16x16x32_bf16 a[64:67], v[152:155], v[68:71], a[64:67]\n"
    "ds_read_b64_tr_b16 v[200:201], v221 offset:59392\n"
    "ds_read_b64_tr_b16 v[202:203], v221 offset:60416\n"
    "v_mfma_f32_16x16x32_bf16 a[104:107], v[152:155], v[72:75], a[104:107]\n"
    "v_mfma_f32_16x16x32_bf16 a[144:147], v[152:155], v[76:79], a[144:147]\n"
    "v_mfma_f32_16x16x32_bf16 a[28:31], v[156:159], v[64:67], a[28:31]\n"
    "ds_read_b64_tr_b16 v[204:205], v221 offset:59400\n"
    "ds_read_b64_tr_b16 v[206:207], v221 offset:60424\n"
    "v_mfma_f32_16x16x32_bf16 a[68:71], v[156:159], v[68:71], a[68:71]\n"
    "v_mfma_f32_16x16x32_bf16 a[108:111], v[156:159], v[72:75], a[108:111]\n"
    "v_mfma_f32_16x16x32_bf16 a[148:151], v[156:159], v[76:79], a[148:151]\n"
    "v_mfma_f32_16x16x32_bf16 a[32:35], v[160:163], v[64:67], a[32:35]\n"
    "v_mfma_f32_16x16x32_bf16 a[72:75], v[160:163], v[68:71], a[72:75]\n"
    "v_mfma_f32_16x16x32_bf16 a[112:115], v[160:163], v[72:75], a[112:115]\n"
    "v_mfma_f32_16x16x32_bf16 a[152:155], v[160:163], v[76:79], a[152:155]\n"
    "v_mfma_f32_16x16x32_bf16 a[36:39], v[164:167], v[64:67], a[36:39]\n"
    "v_mfma_f32_16x16x32_bf16 a[76:79], v[164:167], v[68:71], a[76:79]\n"
    "v_mfma_f32_16x16x32_bf16 a[116:119], v[164:167], v[72:75], a[116:119]\n"
    "v_mfma_f32_16x16x32_bf16 a[156:159], v[164:167], v[76:79], a[156:159]\n"
    "s_waitcnt lgkmcnt(0)\n"
    "v_mfma_f32_16x16x32_bf16 a[0:3], v[168:171], v[80:83], a[0:3]\n"
    "s_waitcnt vmcnt(0)\n"
    "s_barrier\n"
    "v_mfma_f32_16x16x32_bf16 a[40:43], v[168:171], v[84:87], a[40:43]\n"
    "ds_read_b64_tr_b16 v[128:129], v222 offset:0\n"
    "ds_read_b64_tr_b16 v[130:131], v222 offset:1024\n"
    "v_mfma_f32_16x16x32_bf16 a[80:83], v[168:171], v[88:91], a[80:83]\n"
    "v_mfma_f32_16x16x32_bf16 a[120:123], v[168:171], v[92:95], a[120:123]\n"
    "v_mfma_f32_16x16x32_bf16 a[4:7], v[172:175], v[80:83], a[4:7]\n"
    "ds_read_b64_tr_b16 v[132:133], v222 offset:8\n"
    "ds_read_b64_tr_b16 v[134:135], v222 offset:1032\n"
    "v_mfma_f32_16x16x32_bf16 a[44:47], v[172:175], v[84:87], a[44:47]\n"
    "v_mfma_f32_16x16x32_bf16 a[84:87], v[172:175], v[88:91], a[84:87]\n"
    "v_mfma_f32_16x16x32_bf16 a[124:127], v[172:175], v[92:95], a[124:127]\n"
    "ds_read_b64_tr_b16 v[136:137], v222 offset:2048\n"
    "ds_read_b64_tr_b16 v[138:139], v222 offset:3072\n"
    "v_mfma_f32_16x16x32_bf16 a[8:11], v[176:179], v[80:83], a[8:11]\n"
    "v_mfma_f32_16x16x32_bf16 a[48:51], v[176:179], v[84:87], a[48:51]\n"
    "v_mfma_f32_16x16x32_bf16 a[88:91], v[176:179], v[88:91], a[88:91]\n"
    "ds_read_b64_tr_b16 v[140:141], v222 offset:2056\n"
    "ds_read_b64_tr_b16 v[142:143], v222 offset:3080\n"
    "v_mfma_f32_16x16x32_bf16 a[128:131], v[176:179], v[92:95], a[128:131]\n"
    "v_mfma_f32_16x16x32_bf16 a[12:15], v[180:183], v[80:83], a[12:15]\n"
    "v_mfma_f32_16x16x32_bf16 a[52:55], v[180:183], v[84:87], a[52:55]\n"
    "ds_read_b64_tr_b16 v[144:145], v222 offset:4096\n"
    "ds_read_b64_tr_b16 v[146:147], v222 offset:5120\n"
    "v_mfma_f32_16x16x32_bf16 a[92:95], v[180:183], v[88:91], a[92:95]\n"
    "v_mfma_f32_16x16x32_bf16 a[132:135], v[180:183], v[92:95], a[132:135]\n"
    "v_mfma_f32_16x16x32_bf16 a[16:19], v[184:187], v[80:83], a[16:19]\n"
    "ds_read_b64_tr_b16 v[148:149], v222 offset:4104\n"
    "ds_read_b64_tr_b16 v[150:151], v222 offset:5128\n"
    "v_mfma_f32_16x16x32_bf16 a[56:59], v[184:187], v[84:87], a[56:59]\n"
    "v_mfma_f32_16x16x32_bf16 a[96:99], v[184:187], v[88:91], a[96:99]\n"
    "v_mfma_f32_16x16x32_bf16 a[136:139], v[184:187], v[92:95], a[136:139]\n"
    "ds_read_b64_tr_b16 v[152:153], v222 offset:6144\n"
    "ds_read_b64_tr_b16 v[154:155], v222 offset:7168\n"
    "v_mfma_f32_16x16x32_bf16 a[20:23], v[188:191], v[80:83], a[20:23]\n"
    "v_mfma_f32_16x16x32_bf16 a[60:63], v[188:191], v[84:87], a[60:63]\n"
    "v_mfma_f32_16x16x32_bf16 a[100:103], v[188:191], v[88:91], a[100:103]\n"
    "ds_read_b64_tr_b16 v[156:157], v222 offset:6152\n"
    "ds_read_b64_tr_b16 v[158:159], v222 offset:7176\n"
    "v_mfma_f32_16x16x32_bf16 a[140:143], v[188:191], v[92:95], a[140:143]\n"
    "v_mfma_f32_16x16x32_bf16 a[24:27], v[192:195], v[80:83], a[24:27]\n"
    "v_mfma_f32_16x16x32_bf16 a[64:67], v[192:195], v[84:87], a[64:67]\n"
    "ds_read_b64_tr_b16 v[160:161], v222 offset:8192\n"
    "ds_read_b64_tr_b16 v[162:163], v222 offset:9216\n"
    "v_mfma_f32_16x16x32_bf16 a[104:107], v[192:195], v[88:91], a[104:107]\n"
    "v_mfma_f32_16x16x32_bf16 a[144:147], v[192:195], v[92:95], a[144:147]\n"
    "v_mfma_f32_16x16x32_bf16 a[28:31], v[196:199], v[80:83], a[28:31]\n"
    "ds_read_b64_tr_b16 v[164:165], v222 offset:8200\n"
    "ds_read_b64_tr_b16 v[166:167], v222 offset:9224\n"
    "v_mfma_f32_16x16x32_bf16 a[68:71], v[196:199], v[84:87], a[68:71]\n"
    "v_mfma_f32_16x16x32_bf16 a[108:111], v[196:199], v[88:91], a[108:111]\n"
    "v_mfma_f32_16x16x32_bf16 a[148:151], v[196:199], v[92:95], a[148:151]\n"
    "v_mfma_f32_16x16x32_bf16 a[32:35], v[200:203], v[80:83], a[32:35]\n"
    "v_mfma_f32_16x16x32_bf16 a[72:75], v[200:203], v[84:87], a[72:75]\n"
    "v_mfma_f32_16x16x32_bf16 a[112:115], v[200:203], v[88:91], a[112:115]\n"
    "v_mfma_f32_16x16x32_bf16 a[152:155], v[200:203], v[92:95], a[152:155]\n"
    "v_mfma_f32_16x16x32_bf16 a[36:39], v[204:207], v[80:83], a[36:39]\n"
    "v_mfma_f32_16x16x32_bf16 a[76:79], v[204:207], v[84:87], a[76:79]\n"
    "v_mfma_f32_16x16x32_bf16 a[116:119], v[204:207], v[88:91], a[116:119]\n"
    "v_mfma_f32_16x16x32_bf16 a[156:159], v[204:207], v[92:95], a[156:159]\n"
    "s_waitcnt lgkmcnt(0)\n"
    "v_mfma_f32_16x16x32_bf16 a[0:3], v[128:131], v[96:99], a[0:3]\n"
    "v_mfma_f32_16x16x32_bf16 a[40:43], v[128:131], v[100:103], a[40:43]\n"
    "ds_read_b64_tr_b16 v[168:169], v222 offset:10240\n"
    "ds_read_b64_tr_b16 v[170:171], v222 offset:11264\n"
    "v_mfma_f32_16x16x32_bf16 a[80:83], v[128:131], v[104:107], a[80:83]\n"
    "v_mfma_f32_16x16x32_bf16 a[120:123], v[128:131], v[108:111], a[120:123]\n"
    "v_mfma_f32_16x16x32_bf16 a[4:7], v[132:135], v[96:99], a[4:7]\n"
    "ds_read_b64_tr_b16 v[172:173], v222 offset:10248\n"
    "ds_read_b64_tr_b16 v[174:175], v222 offset:11272\n"
    "v_mfma_f32_16x16x32_bf16 a[44:47], v[132:135], v[100:103], a[44:47]\n"
    "v_mfma_f32_16x16x32_bf16 a[84:87], v[132:135], v[104:107], a[84:87]\n"
    "v_mfma_f32_16x16x32_bf16 a[124:127], v[132:135], v[108:111], a[124:127]\n"
    "ds_read_b64_tr_b16 v[176:177], v222 offset:12288\n"
    "ds_read_b64_tr_b16 v[178:179], v222 offset:13312\n"
    "v_mfma_f32_16x16x32_bf16 a[8:11], v[136:139], v[96:99], a[8:11]\n"
    "v_mfma_f32_16x16x32_bf16 a[48:51], v[136:139], v[100:103], a[48:51]\n"
    "v_mfma_f32_16x16x32_bf16 a[88:91], v[136:139], v[104:107], a[88:91]\n"
    "ds_read_b64_tr_b16 v[180:181], v222 offset:12296\n"
    "ds_read_b64_tr_b16 v[182:183], v222 offset:13320\n"
    "v_mfma_f32_16x16x32_bf16 a[128:131], v[136:139], v[108:111], a[128:131]\n"
    "v_mfma_f32_16x16x32_bf16 a[12:15], v[140:143], v[96:99], a[12:15]\n"
    "v_mfma_f32_16x16x32_bf16 a[52:55], v[140:143], v[100:103], a[52:55]\n"
    "ds_read_b64_tr_b16 v[184:185], v222 offset:14336\n"
    "ds_read_b64_tr_b16 v[186:187], v222 offset:15360\n"
    "v_mfma_f32_16x16x32_bf16 a[92:95], v[140:143], v[104:107], a[92:95]\n"
    "v_mfma_f32_16x16x32_bf16 a[132:135], v[140:143], v[108:111], a[132:135]\n"
    "v_mfma_f32_16x16x32_bf16 a[16:19], v[144:147], v[96:99], a[16:19]\n"
    "ds_read_b64_tr_b16 v[188:189], v222 offset:14344\n"
    "ds_read_b64_tr_b16 v[190:191], v222 offset:15368\n"
    "v_mfma_f32_16x16x32_bf16 a[56:59], v[144:147], v[100:103], a[56:59]\n"
    "v_mfma_f32_16x16x32_bf16 a[96:99], v[144:147], v[104:107], a[96:99]\n"
    "v_mfma_f32_16x16x32_bf16 a[136:139], v[144:147], v[108:111], a[136:139]\n"
    "ds_read_b64_tr_b16 v[192:193], v222 offset:16384\n"
    "ds_read_b64_tr_b16 v[194:195], v222 offset:17408\n"
    "v_mfma_f32_16x16x32_bf16 a[20:23], v[148:151], v[96:99], a[20:23]\n"
    "v_mfma_f32_16x16x32_bf16 a[60:63], v[148:151], v[100:103], a[60:63]\n"
    "v_mfma_f32_16x16x32_bf16 a[100:103], v[148:151], v[104:107], a[100:103]\n"
    "ds_read_b64_tr_b16 v[196:197], v222 offset:16392\n"
    "ds_read_b64_tr_b16 v[198:199], v222 offset:17416\n"
    "v_mfma_f32_16x16x32_bf16 a[140:143], v[148:151], v[108:111], a[140:143]\n"
    "v_mfma_f32_16x16x32_bf16 a[24:27], v[152:155], v[96:99], a[24:27]\n"
    "v_mfma_f32_16x16x32_bf16 a[64:67], v[152:155], v[100:103], a[64:67]\n"
    "ds_read_b64_tr_b16 v[200:201], v222 offset:18432\n"
    "ds_read_b64_tr_b16 v[202:203], v222 offset:19456\n"
    "v_mfma_f32_16x16x32_bf16 a[104:107], v[152:155], v[104:107], a[104:107]\n"
    "v_mfma_f32_16x16x32_bf16 a[144:147], v[152:155], v[108:111], a[144:147]\n"
    "v_mfma_f32_16x16x32_bf16 a[28:31], v[156:159], v[96:99], a[28:31]\n"
    "ds_read_b64_tr_b16 v[204:205], v222 offset:18440\n"
    "ds_read_b64_tr_b16 v[206:207], v222 offset:19464\n"
    "v_mfma_f32_16x16x32_bf16 a[68:71], v[156:159], v[100:103], a[68:71]\n"
    "v_mfma_f32_16x16x32_bf16 a[108:111], v[156:159], v[104:107], a[108:111]\n"
    "v_mfma_f32_16x16x32_bf16 a[148:151], v[156:159], v[108:111], a[148:151]\n"
    "v_mfma_f32_16x16x32_bf16 a[32:35], v[160:163], v[96:99], a[32:35]\n"
    "v_mfma_f32_16x16x32_bf16 a[72:75], v[160:163], v[100:103], a[72:75]\n"
    "v_mfma_f32_16x16x32_bf16 a[112:115], v[160:163], v[104:107], a[112:115]\n"
    "v_mfma_f32_16x16x32_bf16 a[152:155], v[160:163], v[108:111], a[152:155]\n"
    "v_mfma_f32_16x16x32_bf16 a[36:39], v[164:167], v[96:99], a[36:39]\n"
    "v_mfma_f32_16x16x32_bf16 a[76:79], v[164:167], v[100:103], a[76:79]\n"
    "v_mfma_f32_16x16x32_bf16 a[116:119], v[164:167], v[104:107], a[116:119]\n"
    "v_mfma_f32_16x16x32_bf16 a[156:159], v[164:167], v[108:111], a[156:159]\n"
    "s_waitcnt lgkmcnt(0)\n"
    "v_mfma_f32_16x16x32_bf16 a[0:3], v[168:171], v[112:115], a[0:3]\n"
    "v_mfma_f32_16x16x32_bf16 a[40:43], v[168:171], v[116:119], a[40:43]\n"
    "v_mfma_f32_16x16x32_bf16 a[80:83], v[168:171], v[120:123], a[80:83]\n"
    "v_mfma_f32_16x16x32_bf16 a[120:123], v[168:171], v[124:127], a[120:123]\n"
    "v_mfma_f32_16x16x32_bf16 a[4:7], v[172:175], v[112:115], a[4:7]\n"
    "v_mfma_f32_16x16x32_bf16 a[44:47], v[172:175], v[116:119], a[44:47]\n"
    "v_mfma_f32_16x16x32_bf16 a[84:87], v[172:175], v[120:123], a[84:87]\n"
    "v_mfma_f32_16x16x32_bf16 a[124:127], v[172:175], v[124:127], a[124:127]\n"
    "v_mfma_f32_16x16x32_bf16 a[8:11], v[176:179], v[112:115], a[8:11]\n"
    "v_mfma_f32_16x16x32_bf16 a[48:51], v[176:179], v[116:119], a[48:51]\n"
    "v_mfma_f32_16x16x32_bf16 a[88:91], v[176:179], v[120:123], a[88:91]\n"
    "v_mfma_f32_16x16x32_bf16 a[128:131], v[176:179], v[124:127], a[128:131]\n"
    "v_mfma_f32_16x16x32_bf16 a[12:15], v[180:183], v[112:115], a[12:15]\n"
    "v_mfma_f32_16x16x32_bf16 a[52:55], v[180:183], v[116:119], a[52:55]\n"
    "v_mfma_f32_16x16x32_bf16 a[92:95], v[180:183], v[120:123], a[92:95]\n"
    "v_mfma_f32_16x16x32_bf16 a[132:135], v[180:183], v[124:127], a[132:135]\n"
    "v_mfma_f32_16x16x32_bf16 a[16:19], v[184:187], v[112:115], a[16:19]\n"
    "v_mfma_f32_16x16x32_bf16 a[56:59], v[184:187], v[116:119], a[56:59]\n"
    "v_mfma_f32_16x16x32_bf16 a[96:99], v[184:187], v[120:123], a[96:99]\n"
    "v_mfma_f32_16x16x32_bf16 a[136:139], v[184:187], v[124:127], a[136:139]\n"
    "v_mfma_f32_16x16x32_bf16 a[20:23], v[188:191], v[112:115], a[20:23]\n"
    "v_mfma_f32_16x16x32_bf16 a[60:63], v[188:191], v[116:119], a[60:63]\n"
    "v_mfma_f32_16x16x32_bf16 a[100:103], v[188:191], v[120:123], a[100:103]\n"
    "v_mfma_f32_16x16x32_bf16 a[140:143], v[188:191], v[124:127], a[140:143]\n"
    "v_mfma_f32_16x16x32_bf16 a[24:27], v[192:195], v[112:115], a[24:27]\n"
    "v_mfma_f32_16x16x32_bf16 a[64:67], v[192:195], v[116:119], a[64:67]\n"
    "v_mfma_f32_16x16x32_bf16 a[104:107], v[192:195], v[120:123], a[104:107]\n"
    "v_mfma_f32_16x16x32_bf16 a[144:147], v[192:195], v[124:127], a[144:147]\n"
    "v_mfma_f32_16x16x32_bf16 a[28:31], v[196:199], v[112:115], a[28:31]\n"
    "v_mfma_f32_16x16x32_bf16 a[68:71], v[196:199], v[116:119], a[68:71]\n"
    "v_mfma_f32_16x16x32_bf16 a[108:111], v[196:199], v[120:123], a[108:111]\n"
    "v_mfma_f32_16x16x32_bf16 a[148:151], v[196:199], v[124:127], a[148:151]\n"
    "v_mfma_f32_16x16x32_bf16 a[32:35], v[200:203], v[112:115], a[32:35]\n"
    "v_mfma_f32_16x16x32_bf16 a[72:75], v[200:203], v[116:119], a[72:75]\n"
    "v_mfma_f32_16x16x32_bf16 a[112:115], v[200:203], v[120:123], a[112:115]\n"
    "v_mfma_f32_16x16x32_bf16 a[152:155], v[200:203], v[124:127], a[152:155]\n"
    "v_mfma_f32_16x16x32_bf16 a[36:39], v[204:207], v[112:115], a[36:39]\n"
    "v_mfma_f32_16x16x32_bf16 a[76:79], v[204:207], v[116:119], a[76:79]\n"
    "v_mfma_f32_16x16x32_bf16 a[116:119], v[204:207], v[120:123], a[116:119]\n"
    "v_mfma_f32_16x16x32_bf16 a[156:159], v[204:207], v[124:127], a[156:159]\n"
    "s_waitcnt lgkmcnt(0)\n"
    "s_nop 7\n"
    "s_add_u32 s60, %14, 1\n"
    "s_and_b32 s60, s60, 3\n"
    "s_lshl_b32 s60, s60, 2\n"
    "s_add_u32 s60, s60, %14\n"
    "s_mul_i32 s60, s60, 5120\n"
    "v_accvgpr_read_b32 v40, a40\n"
    "v_accvgpr_read_b32 v41, a41\n"
    "v_accvgpr_read_b32 v42, a42\n"
    "v_accvgpr_read_b32 v43, a43\n"
    "v_accvgpr_read_b32 v44, a44\n"
    "v_accvgpr_read_b32 v45, a45\n"
    "v_accvgpr_read_b32 v46, a46\n"
    "v_accvgpr_read_b32 v47, a47\n"
    "v_cvt_pk_bf16_f32 v40, v40, v41\n"
    "v_cvt_pk_bf16_f32 v41, v42, v43\n"
    "v_cvt_pk_bf16_f32 v42, v44, v45\n"
    "v_cvt_pk_bf16_f32 v43, v46, v47\n"
    "buffer_store_dwordx4 v[40:43], v216, s[44:47], s60 offen offset:0 sc0 sc1\n"
    "v_accvgpr_read_b32 v48, a48\n"
    "v_accvgpr_read_b32 v49, a49\n"
    "v_accvgpr_read_b32 v50, a50\n"
    "v_accvgpr_read_b32 v51, a51\n"
    "v_accvgpr_read_b32 v52, a52\n"
    "v_accvgpr_read_b32 v53, a53\n"
    "v_accvgpr_read_b32 v54, a54\n"
    "v_accvgpr_read_b32 v55, a55\n"
    "v_cvt_pk_bf16_f32 v48, v48, v49\n"
    "v_cvt_pk_bf16_f32 v49, v50, v51\n"
    "v_cvt_pk_bf16_f32 v50, v52, v53\n"
    "v_cvt_pk_bf16_f32 v51, v54, v55\n"
    "buffer_store_dwordx4 v[48:51], v216, s[44:47], s60 offen offset:1024 sc0 sc1\n"
    "v_accvgpr_read_b32 v56, a56\n"
    "v_accvgpr_read_b32 v57, a57\n"
    "v_accvgpr_read_b32 v58, a58\n"
    "v_accvgpr_read_b32 v59, a59\n"
    "v_accvgpr_read_b32 v60, a60\n"
    "v_accvgpr_read_b32 v61, a61\n"
    "v_accvgpr_read_b32 v62, a62\n"
    "v_accvgpr_read_b32 v63, a63\n"
    "v_cvt_pk_bf16_f32 v56, v56, v57\n"
    "v_cvt_pk_bf16_f32 v57, v58, v59\n"
    "v_cvt_pk_bf16_f32 v58, v60, v61\n"
    "v_cvt_pk_bf16_f32 v59, v62, v63\n"
    "buffer_store_dwordx4 v[56:59], v216, s[44:47], s60 offen offset:2048 sc0 sc1\n"
    "v_accvgpr_read_b32 v64, a64\n"
    "v_accvgpr_read_b32 v65, a65\n"
    "v_accvgpr_read_b32 v66, a66\n"
    "v_accvgpr_read_b32 v67, a67\n"
    "v_accvgpr_read_b32 v68, a68\n"
    "v_accvgpr_read_b32 v69, a69\n"
    "v_accvgpr_read_b32 v70, a70\n"
    "v_accvgpr_read_b32 v71, a71\n"
    "v_cvt_pk_bf16_f32 v64, v64, v65\n"
    "v_cvt_pk_bf16_f32 v65, v66, v67\n"
    "v_cvt_pk_bf16_f32 v66, v68, v69\n"
    "v_cvt_pk_bf16_f32 v67, v70, v71\n"
    "buffer_store_dwordx4 v[64:67], v216, s[44:47], s60 offen offset:3072 sc0 sc1\n"
    "v_accvgpr_read_b32 v72, a72\n"
    "v_accvgpr_read_b32 v73, a73\n"
    "v_accvgpr_read_b32 v74, a74\n"
    "v_accvgpr_read_b32 v75, a75\n"
    "v_accvgpr_read_b32 v76, a76\n"
    "v_accvgpr_read_b32 v77, a77\n"
    "v_accvgpr_read_b32 v78, a78\n"
    "v_accvgpr_read_b32 v79, a79\n"
    "v_cvt_pk_bf16_f32 v72, v72, v73\n"
    "v_cvt_pk_bf16_f32 v73, v74, v75\n"
    "v_cvt_pk_bf16_f32 v74, v76, v77\n"
    "v_cvt_pk_bf16_f32 v75, v78, v79\n"
    "buffer_store_dwordx4 v[72:75], v223, s[44:47], s60 offen offset:0 sc0 sc1\n"
    "s_add_u32 s60, %14, 2\n"
    "s_and_b32 s60, s60, 3\n"
    "s_lshl_b32 s60, s60, 2\n"
    "s_add_u32 s60, s60, %14\n"
    "s_mul_i32 s60, s60, 5120\n"
    "v_accvgpr_read_b32 v80, a80\n"
    "v_accvgpr_read_b32 v81, a81\n"
    "v_accvgpr_read_b32 v82, a82\n"
    "v_accvgpr_read_b32 v83, a83\n"
    "v_accvgpr_read_b32 v84, a84\n"
    "v_accvgpr_read_b32 v85, a85\n"
    "v_accvgpr_read_b32 v86, a86\n"
    "v_accvgpr_read_b32 v87, a87\n"
    "v_cvt_pk_bf16_f32 v80, v80, v81\n"
    "v_cvt_pk_bf16_f32 v81, v82, v83\n"
    "v_cvt_pk_bf16_f32 v82, v84, v85\n"
    "v_cvt_pk_bf16_f32 v83, v86, v87\n"
    "buffer_store_dwordx4 v[80:83], v216, s[44:47], s60 offen offset:0 sc0 sc1\n"
    "v_accvgpr_read_b32 v88, a88\n"
    "v_accvgpr_read_b32 v89, a89\n"
    "v_accvgpr_read_b32 v90, a90\n"
    "v_accvgpr_read_b32 v91, a91\n"
    "v_accvgpr_read_b32 v92, a92\n"
    "v_accvgpr_read_b32 v93, a93\n"
    "v_accvgpr_read_b32 v94, a94\n"
    "v_accvgpr_read_b32 v95, a95\n"
    "v_cvt_pk_bf16_f32 v88, v88, v89\n"
    "v_cvt_pk_bf16_f32 v89, v90, v91\n"
    "v_cvt_pk_bf16_f32 v90, v92, v93\n"
    "v_cvt_pk_bf16_f32 v91, v94, v95\n"
    "buffer_store_dwordx4 v[88:91], v216, s[44:47], s60 offen offset:1024 sc0 sc1\n"
    "v_accvgpr_read_b32 v96, a96\n"
    "v_accvgpr_read_b32 v97, a97\n"
    "v_accvgpr_read_b32 v98, a98\n"
    "v_accvgpr_read_b32 v99, a99\n"
    "v_accvgpr_read_b32 v100, a100\n"
    "v_accvgpr_read_b32 v101, a101\n"
    "v_accvgpr_read_b32 v102, a102\n"
    "v_accvgpr_read_b32 v103, a103\n"
    "v_cvt_pk_bf16_f32 v96, v96, v97\n"
    "v_cvt_pk_bf16_f32 v97, v98, v99\n"
    "v_cvt_pk_bf16_f32 v98, v100, v101\n"
    "v_cvt_pk_bf16_f32 v99, v102, v103\n"
    "buffer_store_dwordx4 v[96:99], v216, s[44:47], s60 offen offset:2048 sc0 sc1\n"
    "v_accvgpr_read_b32 v104, a104\n"
    "v_accvgpr_read_b32 v105, a105\n"
    "v_accvgpr_read_b32 v106, a106\n"
    "v_accvgpr_read_b32 v107, a107\n"
    "v_accvgpr_read_b32 v108, a108\n"
    "v_accvgpr_read_b32 v109, a109\n"
    "v_accvgpr_read_b32 v110, a110\n"
    "v_accvgpr_read_b32 v111, a111\n"
    "v_cvt_pk_bf16_f32 v104, v104, v105\n"
    "v_cvt_pk_bf16_f32 v105, v106, v107\n"
    "v_cvt_pk_bf16_f32 v106, v108, v109\n"
    "v_cvt_pk_bf16_f32 v107, v110, v111\n"
    "buffer_store_dwordx4 v[104:107], v216, s[44:47], s60 offen offset:3072 sc0 sc1\n"
    "v_accvgpr_read_b32 v112, a112\n"
    "v_accvgpr_read_b32 v113, a113\n"
    "v_accvgpr_read_b32 v114, a114\n"
    "v_accvgpr_read_b32 v115, a115\n"
    "v_accvgpr_read_b32 v116, a116\n"
    "v_accvgpr_read_b32 v117, a117\n"
    "v_accvgpr_read_b32 v118, a118\n"
    "v_accvgpr_read_b32 v119, a119\n"
    "v_cvt_pk_bf16_f32 v112, v112, v113\n"
    "v_cvt_pk_bf16_f32 v113, v114, v115\n"
    "v_cvt_pk_bf16_f32 v114, v116, v117\n"
    "v_cvt_pk_bf16_f32 v115, v118, v119\n"
    "buffer_store_dwordx4 v[112:115], v223, s[44:47], s60 offen offset:0 sc0 sc1\n"
    "s_add_u32 s60, %14, 3\n"
    "s_and_b32 s60, s60, 3\n"
    "s_lshl_b32 s60, s60, 2\n"
    "s_add_u32 s60, s60, %14\n"
    "s_mul_i32 s60, s60, 5120\n"
    "v_accvgpr_read_b32 v120, a120\n"
    "v_accvgpr_read_b32 v121, a121\n"
    "v_accvgpr_read_b32 v122, a122\n"
    "v_accvgpr_read_b32 v123, a123\n"
    "v_accvgpr_read_b32 v124, a124\n"
    "v_accvgpr_read_b32 v125, a125\n"
    "v_accvgpr_read_b32 v126, a126\n"
    "v_accvgpr_read_b32 v127, a127\n"
    "v_cvt_pk_bf16_f32 v120, v120, v121\n"
    "v_cvt_pk_bf16_f32 v121, v122, v123\n"
    "v_cvt_pk_bf16_f32 v122, v124, v125\n"
    "v_cvt_pk_bf16_f32 v123, v126, v127\n"
    "buffer_store_dwordx4 v[120:123], v216, s[44:47], s60 offen offset:0 sc0 sc1\n"
    "v_accvgpr_read_b32 v128, a128\n"
    "v_accvgpr_read_b32 v129, a129\n"
    "v_accvgpr_read_b32 v130, a130\n"
    "v_accvgpr_read_b32 v131, a131\n"
    "v_accvgpr_read_b32 v132, a132\n"
    "v_accvgpr_read_b32 v133, a133\n"
    "v_accvgpr_read_b32 v134, a134\n"
    "v_accvgpr_read_b32 v135, a135\n"
    "v_cvt_pk_bf16_f32 v128, v128, v129\n"
    "v_cvt_pk_bf16_f32 v129, v130, v131\n"
    "v_cvt_pk_bf16_f32 v130, v132, v133\n"
    "v_cvt_pk_bf16_f32 v131, v134, v135\n"
    "buffer_store_dwordx4 v[128:131], v216, s[44:47], s60 offen offset:1024 sc0 sc1\n"
    "v_accvgpr_read_b32 v136, a136\n"
    "v_accvgpr_read_b32 v137, a137\n"
    "v_accvgpr_read_b32 v138, a138\n"
    "v_accvgpr_read_b32 v139, a139\n"
    "v_accvgpr_read_b32 v140, a140\n"
    "v_accvgpr_read_b32 v141, a141\n"
    "v_accvgpr_read_b32 v142, a142\n"
    "v_accvgpr_read_b32 v143, a143\n"
    "v_cvt_pk_bf16_f32 v136, v136, v137\n"
    "v_cvt_pk_bf16_f32 v137, v138, v139\n"
    "v_cvt_pk_bf16_f32 v138, v140, v141\n"
    "v_cvt_pk_bf16_f32 v139, v142, v143\n"
    "buffer_store_dwordx4 v[136:139], v216, s[44:47], s60 offen offset:2048 sc0 sc1\n"
    "v_accvgpr_read_b32 v144, a144\n"
    "v_accvgpr_read_b32 v145, a145\n"
    "v_accvgpr_read_b32 v146, a146\n"
    "v_accvgpr_read_b32 v147, a147\n"
    "v_accvgpr_read_b32 v148, a148\n"
    "v_accvgpr_read_b32 v149, a149\n"
    "v_accvgpr_read_b32 v150, a150\n"
    "v_accvgpr_read_b32 v151, a151\n"
    "v_cvt_pk_bf16_f32 v144, v144, v145\n"
    "v_cvt_pk_bf16_f32 v145, v146, v147\n"
    "v_cvt_pk_bf16_f32 v146, v148, v149\n"
    "v_cvt_pk_bf16_f32 v147, v150, v151\n"
    "buffer_store_dwordx4 v[144:147], v216, s[44:47], s60 offen offset:3072 sc0 sc1\n"
    "v_accvgpr_read_b32 v152, a152\n"
    "v_accvgpr_read_b32 v153, a153\n"
    "v_accvgpr_read_b32 v154, a154\n"
    "v_accvgpr_read_b32 v155, a155\n"
    "v_accvgpr_read_b32 v156, a156\n"
    "v_accvgpr_read_b32 v157, a157\n"
    "v_accvgpr_read_b32 v158, a158\n"
    "v_accvgpr_read_b32 v159, a159\n"
    "v_cvt_pk_bf16_f32 v152, v152, v153\n"
    "v_cvt_pk_bf16_f32 v153, v154, v155\n"
    "v_cvt_pk_bf16_f32 v154, v156, v157\n"
    "v_cvt_pk_bf16_f32 v155, v158, v159\n"
    "buffer_store_dwordx4 v[152:155], v223, s[44:47], s60 offen offset:0 sc0 sc1\n"
    "v_accvgpr_read_b32 v0, a0\n"
    "v_accvgpr_read_b32 v1, a1\n"
    "v_accvgpr_read_b32 v2, a2\n"
    "v_accvgpr_read_b32 v3, a3\n"
    "v_accvgpr_read_b32 v4, a4\n"
    "v_accvgpr_read_b32 v5, a5\n"
    "v_accvgpr_read_b32 v6, a6\n"
    "v_accvgpr_read_b32 v7, a7\n"
    "v_accvgpr_read_b32 v8, a8\n"
    "v_accvgpr_read_b32 v9, a9\n"
    "v_accvgpr_read_b32 v10, a10\n"
    "v_accvgpr_read_b32 v11, a11\n"
    "v_accvgpr_read_b32 v12, a12\n"
    "v_accvgpr_read_b32 v13, a13\n"
    "v_accvgpr_read_b32 v14, a14\n"
    "v_accvgpr_read_b32 v15, a15\n"
    "v_accvgpr_read_b32 v16, a16\n"
    "v_accvgpr_read_b32 v17, a17\n"
    "v_accvgpr_read_b32 v18, a18\n"
    "v_accvgpr_read_b32 v19, a19\n"
    "v_accvgpr_read_b32 v20, a20\n"
    "v_accvgpr_read_b32 v21, a21\n"
    "v_accvgpr_read_b32 v22, a22\n"
    "v_accvgpr_read_b32 v23, a23\n"
    "v_accvgpr_read_b32 v24, a24\n"
    "v_accvgpr_read_b32 v25, a25\n"
    "v_accvgpr_read_b32 v26, a26\n"
    "v_accvgpr_read_b32 v27, a27\n"
    "v_accvgpr_read_b32 v28, a28\n"
    "v_accvgpr_read_b32 v29, a29\n"
    "v_accvgpr_read_b32 v30, a30\n"
    "v_accvgpr_read_b32 v31, a31\n"
    "v_accvgpr_read_b32 v32, a32\n"
    "v_accvgpr_read_b32 v33, a33\n"
    "v_accvgpr_read_b32 v34, a34\n"
    "v_accvgpr_read_b32 v35, a35\n"
    "v_accvgpr_read_b32 v36, a36\n"
    "v_accvgpr_read_b32 v37, a37\n"
    "v_accvgpr_read_b32 v38, a38\n"
    "v_accvgpr_read_b32 v39, a39\n"
    "s_add_u32 s61, %14, 4\n"
    "s_lshr_b32 s60, s64, s61\n"
    "s_andn2_b32 s60, 1, s60\n"
    "v_mov_b32 v255, s60\n"
    "s_waitcnt vmcnt(0)\n"
    "buffer_store_dword v255, off, s[48:51], s70 offset:384 sc0 sc1\n"
    "K2BP%=:\n"
    "buffer_load_dword v228, v226, s[48:51], 0 offen sc0 sc1\n"
    "s_sub_u32 s62, s62, 1\ns_cmp_lt_i32 s62, 0\n"
    "s_cbranch_scc1 K2BQ%=\n"
    "s_waitcnt vmcnt(0)\n"
    "v_and_b32 v228, 1, v228\n"
    "v_cmp_ne_u32 vcc, 0, v228\n"
    "s_xor_b32 s60, vcc_lo, s64\n"
    "s_andn2_b32 s60, 240, s60\n"
    "s_cbranch_scc1 K2BP%=\n"
    "K2BQ%=:\n"
    "s_add_u32 s60, %14, 1\n"
    "s_and_b32 s60, s60, 3\n"
    "s_lshl_b32 s61, %14, 2\n"
    "s_add_u32 s60, s60, s61\n"
    "s_mul_i32 s60, s60, 5120\n"
    "buffer_load_dwordx4 v[40:43], v216, s[44:47], s60 offen offset:0 sc0 sc1\n"
    "buffer_load_dwordx4 v[44:47], v216, s[44:47], s60 offen offset:1024 sc0 sc1\n"
    "buffer_load_dwordx4 v[48:51], v216, s[44:47], s60 offen offset:2048 sc0 sc1\n"
    "buffer_load_dwordx4 v[52:55], v216, s[44:47], s60 offen offset:3072 sc0 sc1\n"
    "buffer_load_dwordx4 v[56:59], v223, s[44:47], s60 offen offset:0 sc0 sc1\n"
    "s_add_u32 s60, %14, 2\n"
    "s_and_b32 s60, s60, 3\n"
    "s_lshl_b32 s61, %14, 2\n"
    "s_add_u32 s60, s60, s61\n"
    "s_mul_i32 s60, s60, 5120\n"
    "buffer_load_dwordx4 v[60:63], v216, s[44:47], s60 offen offset:0 sc0 sc1\n"
    "buffer_load_dwordx4 v[64:67], v216, s[44:47], s60 offen offset:1024 sc0 sc1\n"
    "buffer_load_dwordx4 v[68:71], v216, s[44:47], s60 offen offset:2048 sc0 sc1\n"
    "buffer_load_dwordx4 v[72:75], v216, s[44:47], s60 offen offset:3072 sc0 sc1\n"
    "buffer_load_dwordx4 v[76:79], v223, s[44:47], s60 offen offset:0 sc0 sc1\n"
    "s_add_u32 s60, %14, 3\n"
    "s_and_b32 s60, s60, 3\n"
    "s_lshl_b32 s61, %14, 2\n"
    "s_add_u32 s60, s60, s61\n"
    "s_mul_i32 s60, s60, 5120\n"
    "buffer_load_dwordx4 v[80:83], v216, s[44:47], s60 offen offset:0 sc0 sc1\n"
    "buffer_load_dwordx4 v[84:87], v216, s[44:47], s60 offen offset:1024 sc0 sc1\n"
    "buffer_load_dwordx4 v[88:91], v216, s[44:47], s60 offen offset:2048 sc0 sc1\n"
    "buffer_load_dwordx4 v[92:95], v216, s[44:47], s60 offen offset:3072 sc0 sc1\n"
    "buffer_load_dwordx4 v[96:99], v223, s[44:47], s60 offen offset:0 sc0 sc1\n"
    "s_waitcnt vmcnt(10)\n"
    "v_lshlrev_b32 v224, 16, v40\n"
    "v_and_b32 v225, 0xffff0000, v40\n"
    "v_add_f32 v0, v0, v224\n"
    "v_add_f32 v1, v1, v225\n"
    "v_lshlrev_b32 v224, 16, v41\n"
    "v_and_b32 v225, 0xffff0000, v41\n"
    "v_add_f32 v2, v2, v224\n"
    "v_add_f32 v3, v3, v225\n"
    "v_lshlrev_b32 v224, 16, v42\n"
    "v_and_b32 v225, 0xffff0000, v42\n"
    "v_add_f32 v4, v4, v224\n"
    "v_add_f32 v5, v5, v225\n"
    "v_lshlrev_b32 v224, 16, v43\n"
    "v_and_b32 v225, 0xffff0000, v43\n"
    "v_add_f32 v6, v6, v224\n"
    "v_add_f32 v7, v7, v225\n"
    "v_lshlrev_b32 v224, 16, v44\n"
    "v_and_b32 v225, 0xffff0000, v44\n"
    "v_add_f32 v8, v8, v224\n"
    "v_add_f32 v9, v9, v225\n"
    "v_lshlrev_b32 v224, 16, v45\n"
    "v_and_b32 v225, 0xffff0000, v45\n"
    "v_add_f32 v10, v10, v224\n"
    "v_add_f32 v11, v11, v225\n"
    "v_lshlrev_b32 v224, 16, v46\n"
    "v_and_b32 v225, 0xffff0000, v46\n"
    "v_add_f32 v12, v12, v224\n"
    "v_add_f32 v13, v13, v225\n"
    "v_lshlrev_b32 v224, 16, v47\n"
    "v_and_b32 v225, 0xffff0000, v47\n"
    "v_add_f32 v14, v14, v224\n"
    "v_add_f32 v15, v15, v225\n"
    "v_lshlrev_b32 v224, 16, v48\n"
    "v_and_b32 v225, 0xffff0000, v48\n"
    "v_add_f32 v16, v16, v224\n"
    "v_add_f32 v17, v17, v225\n"
    "v_lshlrev_b32 v224, 16, v49\n"
    "v_and_b32 v225, 0xffff0000, v49\n"
    "v_add_f32 v18, v18, v224\n"
    "v_add_f32 v19, v19, v225\n"
    "v_lshlrev_b32 v224, 16, v50\n"
    "v_and_b32 v225, 0xffff0000, v50\n"
    "v_add_f32 v20, v20, v224\n"
    "v_add_f32 v21, v21, v225\n"
    "v_lshlrev_b32 v224, 16, v51\n"
    "v_and_b32 v225, 0xffff0000, v51\n"
    "v_add_f32 v22, v22, v224\n"
    "v_add_f32 v23, v23, v225\n"
    "v_lshlrev_b32 v224, 16, v52\n"
    "v_and_b32 v225, 0xffff0000, v52\n"
    "v_add_f32 v24, v24, v224\n"
    "v_add_f32 v25, v25, v225\n"
    "v_lshlrev_b32 v224, 16, v53\n"
    "v_and_b32 v225, 0xffff0000, v53\n"
    "v_add_f32 v26, v26, v224\n"
    "v_add_f32 v27, v27, v225\n"
    "v_lshlrev_b32 v224, 16, v54\n"
    "v_and_b32 v225, 0xffff0000, v54\n"
    "v_add_f32 v28, v28, v224\n"
    "v_add_f32 v29, v29, v225\n"
    "v_lshlrev_b32 v224, 16, v55\n"
    "v_and_b32 v225, 0xffff0000, v55\n"
    "v_add_f32 v30, v30, v224\n"
    "v_add_f32 v31, v31, v225\n"
    "v_lshlrev_b32 v224, 16, v56\n"
    "v_and_b32 v225, 0xffff0000, v56\n"
    "v_add_f32 v32, v32, v224\n"
    "v_add_f32 v33, v33, v225\n"
    "v_lshlrev_b32 v224, 16, v57\n"
    "v_and_b32 v225, 0xffff0000, v57\n"
    "v_add_f32 v34, v34, v224\n"
    "v_add_f32 v35, v35, v225\n"
    "v_lshlrev_b32 v224, 16, v58\n"
    "v_and_b32 v225, 0xffff0000, v58\n"
    "v_add_f32 v36, v36, v224\n"
    "v_add_f32 v37, v37, v225\n"
    "v_lshlrev_b32 v224, 16, v59\n"
    "v_and_b32 v225, 0xffff0000, v59\n"
    "v_add_f32 v38, v38, v224\n"
    "v_add_f32 v39, v39, v225\n"
    "s_waitcnt vmcnt(5)\n"
    "v_lshlrev_b32 v224, 16, v60\n"
    "v_and_b32 v225, 0xffff0000, v60\n"
    "v_add_f32 v0, v0, v224\n"
    "v_add_f32 v1, v1, v225\n"
    "v_lshlrev_b32 v224, 16, v61\n"
    "v_and_b32 v225, 0xffff0000, v61\n"
    "v_add_f32 v2, v2, v224\n"
    "v_add_f32 v3, v3, v225\n"
    "v_lshlrev_b32 v224, 16, v62\n"
    "v_and_b32 v225, 0xffff0000, v62\n"
    "v_add_f32 v4, v4, v224\n"
    "v_add_f32 v5, v5, v225\n"
    "v_lshlrev_b32 v224, 16, v63\n"
    "v_and_b32 v225, 0xffff0000, v63\n"
    "v_add_f32 v6, v6, v224\n"
    "v_add_f32 v7, v7, v225\n"
    "v_lshlrev_b32 v224, 16, v64\n"
    "v_and_b32 v225, 0xffff0000, v64\n"
    "v_add_f32 v8, v8, v224\n"
    "v_add_f32 v9, v9, v225\n"
    "v_lshlrev_b32 v224, 16, v65\n"
    "v_and_b32 v225, 0xffff0000, v65\n"
    "v_add_f32 v10, v10, v224\n"
    "v_add_f32 v11, v11, v225\n"
    "v_lshlrev_b32 v224, 16, v66\n"
    "v_and_b32 v225, 0xffff0000, v66\n"
    "v_add_f32 v12, v12, v224\n"
    "v_add_f32 v13, v13, v225\n"
    "v_lshlrev_b32 v224, 16, v67\n"
    "v_and_b32 v225, 0xffff0000, v67\n"
    "v_add_f32 v14, v14, v224\n"
    "v_add_f32 v15, v15, v225\n"
    "v_lshlrev_b32 v224, 16, v68\n"
    "v_and_b32 v225, 0xffff0000, v68\n"
    "v_add_f32 v16, v16, v224\n"
    "v_add_f32 v17, v17, v225\n"
    "v_lshlrev_b32 v224, 16, v69\n"
    "v_and_b32 v225, 0xffff0000, v69\n"
    "v_add_f32 v18, v18, v224\n"
    "v_add_f32 v19, v19, v225\n"
    "v_lshlrev_b32 v224, 16, v70\n"
    "v_and_b32 v225, 0xffff0000, v70\n"
    "v_add_f32 v20, v20, v224\n"
    "v_add_f32 v21, v21, v225\n"
    "v_lshlrev_b32 v224, 16, v71\n"
    "v_and_b32 v225, 0xffff0000, v71\n"
    "v_add_f32 v22, v22, v224\n"
    "v_add_f32 v23, v23, v225\n"
    "v_lshlrev_b32 v224, 16, v72\n"
    "v_and_b32 v225, 0xffff0000, v72\n"
    "v_add_f32 v24, v24, v224\n"
    "v_add_f32 v25, v25, v225\n"
    "v_lshlrev_b32 v224, 16, v73\n"
    "v_and_b32 v225, 0xffff0000, v73\n"
    "v_add_f32 v26, v26, v224\n"
    "v_add_f32 v27, v27, v225\n"
    "v_lshlrev_b32 v224, 16, v74\n"
    "v_and_b32 v225, 0xffff0000, v74\n"
    "v_add_f32 v28, v28, v224\n"
    "v_add_f32 v29, v29, v225\n"
    "v_lshlrev_b32 v224, 16, v75\n"
    "v_and_b32 v225, 0xffff0000, v75\n"
    "v_add_f32 v30, v30, v224\n"
    "v_add_f32 v31, v31, v225\n"
    "v_lshlrev_b32 v224, 16, v76\n"
    "v_and_b32 v225, 0xffff0000, v76\n"
    "v_add_f32 v32, v32, v224\n"
    "v_add_f32 v33, v33, v225\n"
    "v_lshlrev_b32 v224, 16, v77\n"
    "v_and_b32 v225, 0xffff0000, v77\n"
    "v_add_f32 v34, v34, v224\n"
    "v_add_f32 v35, v35, v225\n"
    "v_lshlrev_b32 v224, 16, v78\n"
    "v_and_b32 v225, 0xffff0000, v78\n"
    "v_add_f32 v36, v36, v224\n"
    "v_add_f32 v37, v37, v225\n"
    "v_lshlrev_b32 v224, 16, v79\n"
    "v_and_b32 v225, 0xffff0000, v79\n"
    "v_add_f32 v38, v38, v224\n"
    "v_add_f32 v39, v39, v225\n"
    "s_waitcnt vmcnt(0)\n"
    "v_lshlrev_b32 v224, 16, v80\n"
    "v_and_b32 v225, 0xffff0000, v80\n"
    "v_add_f32 v0, v0, v224\n"
    "v_add_f32 v1, v1, v225\n"
    "v_lshlrev_b32 v224, 16, v81\n"
    "v_and_b32 v225, 0xffff0000, v81\n"
    "v_add_f32 v2, v2, v224\n"
    "v_add_f32 v3, v3, v225\n"
    "v_lshlrev_b32 v224, 16, v82\n"
    "v_and_b32 v225, 0xffff0000, v82\n"
    "v_add_f32 v4, v4, v224\n"
    "v_add_f32 v5, v5, v225\n"
    "v_lshlrev_b32 v224, 16, v83\n"
    "v_and_b32 v225, 0xffff0000, v83\n"
    "v_add_f32 v6, v6, v224\n"
    "v_add_f32 v7, v7, v225\n"
    "v_lshlrev_b32 v224, 16, v84\n"
    "v_and_b32 v225, 0xffff0000, v84\n"
    "v_add_f32 v8, v8, v224\n"
    "v_add_f32 v9, v9, v225\n"
    "v_lshlrev_b32 v224, 16, v85\n"
    "v_and_b32 v225, 0xffff0000, v85\n"
    "v_add_f32 v10, v10, v224\n"
    "v_add_f32 v11, v11, v225\n"
    "v_lshlrev_b32 v224, 16, v86\n"
    "v_and_b32 v225, 0xffff0000, v86\n"
    "v_add_f32 v12, v12, v224\n"
    "v_add_f32 v13, v13, v225\n"
    "v_lshlrev_b32 v224, 16, v87\n"
    "v_and_b32 v225, 0xffff0000, v87\n"
    "v_add_f32 v14, v14, v224\n"
    "v_add_f32 v15, v15, v225\n"
    "v_lshlrev_b32 v224, 16, v88\n"
    "v_and_b32 v225, 0xffff0000, v88\n"
    "v_add_f32 v16, v16, v224\n"
    "v_add_f32 v17, v17, v225\n"
    "v_lshlrev_b32 v224, 16, v89\n"
    "v_and_b32 v225, 0xffff0000, v89\n"
    "v_add_f32 v18, v18, v224\n"
    "v_add_f32 v19, v19, v225\n"
    "v_lshlrev_b32 v224, 16, v90\n"
    "v_and_b32 v225, 0xffff0000, v90\n"
    "v_add_f32 v20, v20, v224\n"
    "v_add_f32 v21, v21, v225\n"
    "v_lshlrev_b32 v224, 16, v91\n"
    "v_and_b32 v225, 0xffff0000, v91\n"
    "v_add_f32 v22, v22, v224\n"
    "v_add_f32 v23, v23, v225\n"
    "v_lshlrev_b32 v224, 16, v92\n"
    "v_and_b32 v225, 0xffff0000, v92\n"
    "v_add_f32 v24, v24, v224\n"
    "v_add_f32 v25, v25, v225\n"
    "v_lshlrev_b32 v224, 16, v93\n"
    "v_and_b32 v225, 0xffff0000, v93\n"
    "v_add_f32 v26, v26, v224\n"
    "v_add_f32 v27, v27, v225\n"
    "v_lshlrev_b32 v224, 16, v94\n"
    "v_and_b32 v225, 0xffff0000, v94\n"
    "v_add_f32 v28, v28, v224\n"
    "v_add_f32 v29, v29, v225\n"
    "v_lshlrev_b32 v224, 16, v95\n"
    "v_and_b32 v225, 0xffff0000, v95\n"
    "v_add_f32 v30, v30, v224\n"
    "v_add_f32 v31, v31, v225\n"
    "v_lshlrev_b32 v224, 16, v96\n"
    "v_and_b32 v225, 0xffff0000, v96\n"
    "v_add_f32 v32, v32, v224\n"
    "v_add_f32 v33, v33, v225\n"
    "v_lshlrev_b32 v224, 16, v97\n"
    "v_and_b32 v225, 0xffff0000, v97\n"
    "v_add_f32 v34, v34, v224\n"
    "v_add_f32 v35, v35, v225\n"
    "v_lshlrev_b32 v224, 16, v98\n"
    "v_and_b32 v225, 0xffff0000, v98\n"
    "v_add_f32 v36, v36, v224\n"
    "v_add_f32 v37, v37, v225\n"
    "v_lshlrev_b32 v224, 16, v99\n"
    "v_and_b32 v225, 0xffff0000, v99\n"
    "v_add_f32 v38, v38, v224\n"
    "v_add_f32 v39, v39, v225\n"
    "v_cvt_pk_bf16_f32 v0, v0, v1\n"
    "v_cvt_pk_bf16_f32 v1, v2, v3\n"
    "v_cvt_pk_bf16_f32 v2, v4, v5\n"
    "v_cvt_pk_bf16_f32 v3, v6, v7\n"
    "buffer_store_dwordx4 v[0:3], v217, s[52:55], 0 offen offset:0 sc0 sc1\n"
    "v_cvt_pk_bf16_f32 v8, v8, v9\n"
    "v_cvt_pk_bf16_f32 v9, v10, v11\n"
    "v_cvt_pk_bf16_f32 v10, v12, v13\n"
    "v_cvt_pk_bf16_f32 v11, v14, v15\n"
    "buffer_store_dwordx4 v[8:11], v217, s[52:55], 0 offen offset:64 sc0 sc1\n"
    "v_cvt_pk_bf16_f32 v16, v16, v17\n"
    "v_cvt_pk_bf16_f32 v17, v18, v19\n"
    "v_cvt_pk_bf16_f32 v18, v20, v21\n"
    "v_cvt_pk_bf16_f32 v19, v22, v23\n"
    "buffer_store_dwordx4 v[16:19], v217, s[52:55], 0 offen offset:128 sc0 sc1\n"
    "v_cvt_pk_bf16_f32 v24, v24, v25\n"
    "v_cvt_pk_bf16_f32 v25, v26, v27\n"
    "v_cvt_pk_bf16_f32 v26, v28, v29\n"
    "v_cvt_pk_bf16_f32 v27, v30, v31\n"
    "buffer_store_dwordx4 v[24:27], v217, s[52:55], 0 offen offset:192 sc0 sc1\n"
    "v_cvt_pk_bf16_f32 v32, v32, v33\n"
    "v_cvt_pk_bf16_f32 v33, v34, v35\n"
    "v_cvt_pk_bf16_f32 v34, v36, v37\n"
    "v_cvt_pk_bf16_f32 v35, v38, v39\n"
    "buffer_store_dwordx4 v[32:35], v217, s[52:55], 0 offen offset:256 sc0 sc1\n"
    "K2E%=:\n"
    :: "s"(x), "s"(wq), "s"(k), "s"(v), "s"(of), "s"(qs), "s"(wo), "s"(w), "s"(ldsb), "s"(i >> 2), "s"(i & 3u), "s"(wf), "s"(ex), "s"(y), "s"(sl)
    : "v0", "v1", "v2", "v3", "v4", "v5", "v6", "v7", "v8", "v9", "v10", "v11", "v12", "v13", "v14", "v15", "v16", "v17", "v18", "v19", "v20", "v21", "v22", "v23", "v24", "v25", "v26", "v27", "v28", "v29", "v30", "v31", "v32", "v33", "v34", "v35", "v36", "v37", "v38", "v39", "v40", "v41", "v42", "v43", "v44", "v45", "v46", "v47", "v48", "v49", "v50", "v51", "v52", "v53", "v54", "v55", "v56", "v57", "v58", "v59", "v60", "v61", "v62", "v63", "v64", "v65", "v66", "v67", "v68", "v69", "v70", "v71", "v72", "v73", "v74", "v75", "v76", "v77", "v78", "v79", "v80", "v81", "v82", "v83", "v84", "v85", "v86", "v87", "v88", "v89", "v90", "v91", "v92", "v93", "v94", "v95", "v96", "v97", "v98", "v99", "v100", "v101", "v102", "v103", "v104", "v105", "v106", "v107", "v108", "v109", "v110", "v111", "v112", "v113", "v114", "v115", "v116", "v117", "v118", "v119", "v120", "v121", "v122", "v123", "v124", "v125", "v126", "v127", "v128", "v129", "v130", "v131", "v132", "v133", "v134", "v135", "v136", "v137", "v138", "v139", "v140", "v141", "v142", "v143", "v144", "v145", "v146", "v147", "v148", "v149", "v150", "v151", "v152", "v153", "v154", "v155", "v156", "v157", "v158", "v159", "v160", "v161", "v162", "v163", "v164", "v165", "v166", "v167", "v168", "v169", "v170", "v171", "v172", "v173", "v174", "v175", "v176", "v177", "v178", "v179", "v180", "v181", "v182", "v183", "v184", "v185", "v186", "v187", "v188", "v189", "v190", "v191", "v192", "v193", "v194", "v195", "v196", "v197", "v198", "v199", "v200", "v201", "v202", "v203", "v204", "v205", "v206", "v207", "v208", "v209", "v210", "v211", "v212", "v213", "v214", "v215", "v216", "v217", "v218", "v219", "v220", "v221", "v222", "v223", "v224", "v225", "v226", "v227", "v228", "v229", "v230", "v231", "v232", "v233", "v234", "v235", "v236", "v237", "v238", "v239", "v240", "v241", "v242", "v243", "v244", "v245", "v246", "v247", "v248", "v249", "v250", "v251", "v252", "v253", "v254", "v255", "a0", "a1", "a2", "a3", "a4", "a5", "a6", "a7", "a8", "a9", "a10", "a11", "a12", "a13", "a14", "a15", "a16", "a17", "a18", "a19", "a20", "a21", "a22", "a23", "a24", "a25", "a26", "a27", "a28", "a29", "a30", "a31", "a32", "a33", "a34", "a35", "a36", "a37", "a38", "a39", "a40", "a41", "a42", "a43", "a44", "a45", "a46", "a47", "a48", "a49", "a50", "a51", "a52", "a53", "a54", "a55", "a56", "a57", "a58", "a59", "a60", "a61", "a62", "a63", "a64", "a65", "a66", "a67", "a68", "a69", "a70", "a71", "a72", "a73", "a74", "a75", "a76", "a77", "a78", "a79", "a80", "a81", "a82", "a83", "a84", "a85", "a86", "a87", "a88", "a89", "a90", "a91", "a92", "a93", "a94", "a95", "a96", "a97", "a98", "a99", "a100", "a101", "a102", "a103", "a104", "a105", "a106", "a107", "a108", "a109", "a110", "a111", "a112", "a113", "a114", "a115", "a116", "a117", "a118", "a119", "a120", "a121", "a122", "a123", "a124", "a125", "a126", "a127", "a128", "a129", "a130", "a131", "a132", "a133", "a134", "a135", "a136", "a137", "a138", "a139", "a140", "a141", "a142", "a143", "a144", "a145", "a146", "a147", "a148", "a149", "a150", "a151", "a152", "a153", "a154", "a155", "a156", "a157", "a158", "a159", "a160", "a161", "a162", "a163", "a164", "a165", "a166", "a167", "a168", "a169", "a170", "a171", "a172", "a173", "a174", "a175", "a176", "a177", "a178", "a179", "a180", "a181", "a182", "a183", "a184", "a185", "a186", "a187", "a188", "a189", "a190", "a191", "a192", "a193", "a194", "a195", "a196", "a197", "a198", "a199", "a200", "a201", "a202", "a203", "a204", "a205", "a206", "a207", "a208", "a209", "a210", "a211", "a212", "a213", "a214", "a215", "a216", "a217", "a218", "a219", "a220", "a221", "a222", "a223", "a224", "a225", "a226", "a227", "a228", "a229", "a230", "a231", "a232", "a233", "a234", "a235", "a236", "a237", "a238", "a239", "a240", "a241", "a242", "a243", "a244", "a245", "a246", "a247", "a248", "a249", "a250", "a251", "a252", "a253", "a254", "a255", "s36", "s37", "s38", "s39", "s40", "s41", "s42", "s43", "s44", "s45", "s46", "s47", "s48", "s49", "s50", "s51", "s52", "s53", "s54", "s55", "s56", "s57", "s58", "s59", "s60", "s61", "s62", "s63", "s64", "s65", "s66", "s67", "s68", "s69", "s70", "s71", "s72", "s73", "s74", "s75", "s76", "s77", "s78", "s79", "s80", "s81", "s82", "s83", "s84", "s85", "s86", "s87", "s88", "s89", "s90", "s91", "s92", "s93", "s94", "s95", "s96", "s97", "s98", "s99", "vcc", "scc", "memory");
}

extern "C" void kernel_launch(void* const* d_in, const int* in_sizes, int n_in, void* d_out, int out_size, void* d_ws, size_t ws_size, hipStream_t stream) {
  (void)hipFuncSetAttribute((const void*)k1, hipFuncAttributeMaxDynamicSharedMemorySize, 163840);
  k1<<<256, 256, 163840, stream>>>((const char*)d_in[0], (const char*)d_in[1], (const char*)d_in[2], (const char*)d_in[3], (const char*)d_in[4],
                                   (const char*)d_in[5], (const char*)d_in[6], (const char*)d_in[7], (const char*)d_in[8], (const char*)d_in[9], (char*)d_ws, (char*)d_out);
}
```
